```python
import torch

def _softmax(x, axis=-1):
    return torch.softmax(x, dim=axis)
def _gelu(x):
    return torch.nn.functional.gelu(x, approximate="tanh")
def _silu(x):
    return torch.nn.functional.silu(x)
def _sigmoid(x):
    return torch.sigmoid(x)
def _layernorm(x, axis=-1, eps=1e-5):
    m = x.mean(dim=axis, keepdim=True)
    v = x.var(dim=axis, keepdim=True, unbiased=False)
    return (x - m) / torch.sqrt(v + eps)
def _scatter_add(tgt, idx, src, axis=0):
    return tgt.clone().index_add_(axis, idx, src)
def _segment_sum(src, seg, n):
    out = torch.zeros((n,) + src.shape[1:], dtype=src.dtype)
    return out.index_add_(0, seg, src)
def _pad(x, pw):
    flat = tuple(v for pair in reversed(pw) for v in pair)
    return torch.nn.functional.pad(x, flat)

kernel_name = 'standalone__layernorm_linear__sampling'

def setup_inputs():

    _w = (lambda a: (lambda t: t.to(torch.bfloat16) if t.is_floating_point() else t)(torch.as_tensor(a)))
    B, S, D = 1024, 4, 1024
    return {
        "x": _w((lambda *s: torch.randn(*s, dtype=torch.bfloat16) * 0.05)(B, S, D)),
        "g": _w((lambda *s: torch.randn(*s, dtype=torch.bfloat16) * 0.05)(D) + 1.0),
        "b": _w((lambda *s: torch.randn(*s, dtype=torch.bfloat16) * 0.4)(D)),
        "w": _w((lambda *s: torch.randn(*s, dtype=torch.bfloat16) * 0.05)(D, D)),
    }

def reference(x, g, b, w):
    return (_layernorm(x, axis=-1) * g + b) @ w
```

```cpp
#include <hip/hip_runtime.h>
#include <hip/hip_bf16.h>
typedef short bf16x8 __attribute__((ext_vector_type(8)));
typedef float f32x16 __attribute__((ext_vector_type(16)));
typedef unsigned u32x4 __attribute__((ext_vector_type(4)));
typedef unsigned short u16;

__device__ __forceinline__ float bf_lo(unsigned u){ return __uint_as_float(u<<16); }
__device__ __forceinline__ float bf_hi(unsigned u){ return __uint_as_float(u&0xffff0000u); }
__device__ __forceinline__ unsigned cvt_pk(float lo,float hi){ unsigned r; asm("v_cvt_pk_bf16_f32 %0,%1,%2":"=v"(r):"v"(lo),"v"(hi)); return r; }

__global__ __launch_bounds__(256) void ln_kernel(const u16* x,const u16* g,const u16* b,u16* y){
  int wave=threadIdx.x>>6, lane=threadIdx.x&63;
  int row=blockIdx.x*4+wave;
  const u32x4* xr=(const u32x4*)(x+(size_t)row*1024);
  u32x4 v0=xr[lane], v1=xr[lane+64];
  float f[16];
  for(int i=0;i<4;i++){ f[2*i]=bf_lo(v0[i]); f[2*i+1]=bf_hi(v0[i]); f[8+2*i]=bf_lo(v1[i]); f[8+2*i+1]=bf_hi(v1[i]); }
  float s=0; for(int i=0;i<16;i++) s+=f[i];
  for(int o=32;o;o>>=1) s+=__shfl_xor(s,o);
  float mean=s*(1.f/1024);
  float q=0; for(int i=0;i<16;i++){ float d=f[i]-mean; q+=d*d; }
  for(int o=32;o;o>>=1) q+=__shfl_xor(q,o);
  float rstd=rsqrtf(q*(1.f/1024)+1e-5f);
  u32x4 g0=((const u32x4*)g)[lane], g1=((const u32x4*)g)[lane+64], b0=((const u32x4*)b)[lane], b1=((const u32x4*)b)[lane+64];
  u32x4 o0,o1;
  for(int i=0;i<4;i++){
    o0[i]=cvt_pk((f[2*i]-mean)*rstd*bf_lo(g0[i])+bf_lo(b0[i]),(f[2*i+1]-mean)*rstd*bf_hi(g0[i])+bf_hi(b0[i]));
    o1[i]=cvt_pk((f[8+2*i]-mean)*rstd*bf_lo(g1[i])+bf_lo(b1[i]),(f[8+2*i+1]-mean)*rstd*bf_hi(g1[i])+bf_hi(b1[i]));
  }
  u32x4* yr=(u32x4*)(y+(size_t)row*1024);
  yr[lane]=o0; yr[lane+64]=o1;
}

__global__ __launch_bounds__(256) void gemm_kernel(const u16* y,const u16* w,u16* out){
  __shared__ __attribute__((aligned(16))) u16 sA[128*40];
  __shared__ __attribute__((aligned(16))) u16 sB[128*40];
  int bid=blockIdx.x; int xcd=bid&7, idx=bid>>3; int mi=xcd*4+(idx>>3), ni=idx&7;
  int t=threadIdx.x, wave=t>>6, lane=t&63, wm=wave>>1, wn=wave&1, r=lane&31, h=lane>>5;
  f32x16 acc[2][2]={};
  for(int k0=0;k0<1024;k0+=32){
    for(int i=0;i<2;i++){ int c=t+256*i; int row=c>>2, ch=c&3;
      *(u32x4*)(sA+row*40+ch*8)=*(const u32x4*)(y+(size_t)(mi*128+row)*1024+k0+ch*8); }
    for(int i=0;i<2;i++){ int c=t+256*i; int kk=c>>4, ch=c&15;
      u32x4 v=*(const u32x4*)(w+(size_t)(k0+kk)*1024+ni*128+ch*8);
      for(int e=0;e<4;e++){ sB[(ch*8+2*e)*40+kk]=(u16)(v[e]&0xffff); sB[(ch*8+2*e+1)*40+kk]=(u16)(v[e]>>16);} }
    __syncthreads();
    for(int ks=0;ks<2;ks++){
      bf16x8 a[2],bb[2];
      for(int i=0;i<2;i++){ a[i]=*(const bf16x8*)(sA+(wm*64+i*32+r)*40+ks*16+h*8); bb[i]=*(const bf16x8*)(sB+(wn*64+i*32+r)*40+ks*16+h*8);}
      for(int i=0;i<2;i++)for(int j=0;j<2;j++) acc[i][j]=__builtin_amdgcn_mfma_f32_32x32x16_bf16(bb[j],a[i],acc[i][j],0,0,0);
    }
    __syncthreads();
  }
  for(int i=0;i<2;i++)for(int j=0;j<2;j++){
    int m=mi*128+wm*64+i*32+r;
    for(int q=0;q<4;q++){
      int n=ni*128+wn*64+j*32+8*q+4*h;
      uint2 o; o.x=cvt_pk(acc[i][j][4*q],acc[i][j][4*q+1]); o.y=cvt_pk(acc[i][j][4*q+2],acc[i][j][4*q+3]);
      *(uint2*)(out+(size_t)m*1024+n)=o;
    }
  }
}

extern "C" void kernel_launch(void* const* d_in, const int* in_sizes, int n_in, void* d_out, int out_size, void* d_ws, size_t ws_size, hipStream_t stream){
  const u16* x=(const u16*)d_in[0]; const u16* g=(const u16*)d_in[1]; const u16* b=(const u16*)d_in[2]; const u16* w=(const u16*)d_in[3];
  u16* y=(u16*)d_ws;
  ln_kernel<<<1024,256,0,stream>>>(x,g,b,y);
  gemm_kernel<<<256,256,0,stream>>>(y,w,(u16*)d_out);
}
```

```cpp
#include <hip/hip_runtime.h>
#include <hip/hip_bf16.h>
#include <utility>

typedef short bf16x8 __attribute__((ext_vector_type(8)));
typedef float f32x16 __attribute__((ext_vector_type(16)));
typedef float f32x4 __attribute__((ext_vector_type(4)));
typedef float f32x2 __attribute__((ext_vector_type(2)));
typedef unsigned u32x4 __attribute__((ext_vector_type(4)));
typedef unsigned u32x2 __attribute__((ext_vector_type(2)));
typedef unsigned short u16;
typedef unsigned long long u64;
#define GAS __attribute__((address_space(1)))
#define LAS __attribute__((address_space(3)))

#define XR 6
#define WD 4

constexpr int KD = 1024, ND = 1024;
constexpr int NST = 16;
constexpr int STG = 16384;
constexpr int LDS_X = 0;
constexpr int LDS_W = XR * STG;
constexpr int LDS_TAB = LDS_W + 2 * STG;
constexpr int LDS_SCR = LDS_TAB + 2048;
constexpr int LDS_TOTAL = LDS_SCR + 2048;

constexpr size_t WS_EPR = 0;
constexpr size_t WS_EPC = 32 * 128;
constexpr size_t WS_RS = 8192;
constexpr size_t WS_C = WS_RS + 4096 * 2 * 8;

template <int B, int E, class F> __device__ __forceinline__ void static_for(F&& f) {
  if constexpr (B < E) { f(std::integral_constant<int, B>{}); static_for<B + 1, E>(f); }
}
__device__ __forceinline__ float bf_lo(unsigned u) { return __uint_as_float(u << 16); }
__device__ __forceinline__ float bf_hi(unsigned u) { return __uint_as_float(u & 0xffff0000u); }
__device__ __forceinline__ unsigned cvt_pk(float lo, float hi) {
  unsigned r; asm("v_cvt_pk_bf16_f32 %0, %1, %2" : "=v"(r) : "v"(lo), "v"(hi)); return r;
}
template <int CTRL> __device__ __forceinline__ float dpp_add(float x) {
  return x + __builtin_bit_cast(float, __builtin_amdgcn_update_dpp(0, __builtin_bit_cast(int, x), CTRL, 0xf, 0xf, true));
}
__device__ __forceinline__ float sum32(float x) {
  x = dpp_add<0x128>(x); x = dpp_add<0x124>(x); x = dpp_add<0x122>(x); x = dpp_add<0x121>(x);
  auto s = __builtin_amdgcn_permlane16_swap(__float_as_uint(x), __float_as_uint(x), false, false);
  return __uint_as_float(s[0]) + __uint_as_float(s[1]);
}
#define BARRIER() asm volatile("s_waitcnt lgkmcnt(0)\n\ts_barrier" ::: "memory")
template <int N> __device__ __forceinline__ void wait_vm() { asm volatile("s_waitcnt vmcnt(%0)" ::"n"(N) : "memory"); }

__device__ __forceinline__ void glds16(unsigned voff, const void* sbase, unsigned lds_dst) {
  unsigned keep;
  asm volatile("s_mov_b32 %0, m0\n\ts_mov_b32 m0, %3\n\ts_nop 0\n\tglobal_load_lds_dwordx4 %1, %2\n\ts_mov_b32 m0, %0"
               : "=&s"(keep) : "v"(voff), "s"(sbase), "s"(lds_dst) : "memory");
}
__device__ __forceinline__ u32x4 gload16_asm(unsigned voff, const void* sbase) {
  u32x4 r; asm volatile("global_load_dwordx4 %0, %1, %2" : "=v"(r) : "v"(voff), "s"(sbase) : "memory"); return r;
}
__device__ __forceinline__ unsigned gload_u16_asm(unsigned voff, const void* sbase) {
  unsigned r; asm volatile("global_load_ushort %0, %1, %2" : "=v"(r) : "v"(voff), "s"(sbase) : "memory"); return r;
}
__device__ __forceinline__ unsigned gload_u32_sc1_asm(unsigned voff, const void* sbase) {
  unsigned r; asm volatile("global_load_dword %0, %1, %2 sc1" : "=v"(r) : "v"(voff), "s"(sbase) : "memory"); return r;
}
__device__ __forceinline__ u32x2 gload_u64_sc1_asm(unsigned voff, const void* sbase) {
  u32x2 r; asm volatile("global_load_dwordx2 %0, %1, %2 sc1" : "=v"(r) : "v"(voff), "s"(sbase) : "memory"); return r;
}
__device__ __forceinline__ void store_granule(char* ws, size_t off, unsigned tag, float v) {
  __hip_atomic_store((GAS u64*)(ws + off), ((u64)tag << 32) | __float_as_uint(v), __ATOMIC_RELAXED, __HIP_MEMORY_SCOPE_AGENT);
}
__device__ __forceinline__ u64 load_granule(const char* ws, size_t off) {
  return __hip_atomic_load((GAS u64*)(ws + off), __ATOMIC_RELAXED, __HIP_MEMORY_SCOPE_AGENT);
}

struct Side { u32x4 xr[4]; u32x4 wv; unsigned gv, bv, epr, epc; };
constexpr int NSIDE = 9;

__device__ __forceinline__ void side_issue(Side& s, const u16* x, const u16* g, const u16* b, const u16* w, const char* ws,
                                           int mi, int ni, int sv, int lane) {
  s.epr = gload_u32_sc1_asm(0u, ws + WS_EPR + mi * 128);
  s.epc = gload_u32_sc1_asm(0u, ws + WS_EPC + ni * 128);
  const unsigned xo = (unsigned)(mi * 128 + ni * 16 + sv * 2 + (lane >> 5)) * 2048u + (unsigned)(lane & 31) * 16u;
  s.xr[0] = gload16_asm(xo, x); s.xr[1] = gload16_asm(xo + 512, x); s.xr[2] = gload16_asm(xo + 1024, x); s.xr[3] = gload16_asm(xo + 1536, x);
  const int u = sv * 64 + lane, k = (mi >> 2) * 128 + (u >> 2);
  s.wv = gload16_asm((unsigned)k * 2048u + (unsigned)(ni * 128 + (mi & 3) * 32 + (u & 3) * 8) * 2u, w);
  s.gv = gload_u16_asm((unsigned)k * 2u, g);
  s.bv = gload_u16_asm((unsigned)k * 2u, b);
}
template <int YOUNGER> __device__ __forceinline__ void side_compute(Side& s, char* ws, char* smem, int mi, int ni, int sv, int lane) {
  asm volatile("s_waitcnt vmcnt(%9)"
               : "+v"(s.xr[0]), "+v"(s.xr[1]), "+v"(s.xr[2]), "+v"(s.xr[3]), "+v"(s.wv), "+v"(s.gv), "+v"(s.bv), "+v"(s.epr), "+v"(s.epc)
               : "n"(YOUNGER) : "memory");
  float f[32];
#pragma unroll
  for (int j = 0; j < 4; ++j)
#pragma unroll
    for (int i = 0; i < 4; ++i) { f[j * 8 + 2 * i] = bf_lo(s.xr[j][i]); f[j * 8 + 2 * i + 1] = bf_hi(s.xr[j][i]); }
  float s0 = 0.f, s1 = 0.f, s2 = 0.f, s3 = 0.f;
#pragma unroll
  for (int i = 0; i < 32; i += 4) { s0 += f[i]; s1 += f[i + 1]; s2 += f[i + 2]; s3 += f[i + 3]; }
  const float mean = sum32((s0 + s1) + (s2 + s3)) * (1.f / 1024.f);
  float q0 = 0.f, q1 = 0.f, q2 = 0.f, q3 = 0.f;
#pragma unroll
  for (int i = 0; i < 32; i += 4) {
    float d0 = f[i] - mean, d1 = f[i + 1] - mean, d2 = f[i + 2] - mean, d3 = f[i + 3] - mean;
    q0 = fmaf(d0, d0, q0); q1 = fmaf(d1, d1, q1); q2 = fmaf(d2, d2, q2); q3 = fmaf(d3, d3, q3);
  }
  const float var = sum32((q0 + q1) + (q2 + q3)) * (1.f / 1024.f);
  const float rstd = 1.0f / sqrtf(var + 1e-5f);
  if ((lane & 31) < 2) {
    const int row = mi * 128 + ni * 16 + sv * 2 + (lane >> 5);
    store_granule(ws, WS_RS + ((size_t)row * 2 + (lane & 1)) * 8, s.epr + 1u, (lane & 1) ? rstd : mean);
  }
  const float gk = bf_lo(s.gv), bk = bf_lo(s.bv);
  float p1[8], p2[8];
#pragma unroll
  for (int i = 0; i < 4; ++i) {
    const float lo = bf_lo(s.wv[i]), hi = bf_hi(s.wv[i]);
    const unsigned pk = cvt_pk(gk * lo, gk * hi);
    p1[2 * i] = bf_lo(pk); p1[2 * i + 1] = bf_hi(pk);
    p2[2 * i] = bk * lo; p2[2 * i + 1] = bk * hi;
  }
  float a[8];
#pragma unroll
  for (int i = 0; i < 8; ++i) {
    auto r = __builtin_amdgcn_permlane32_swap(__float_as_uint(p1[i]), __float_as_uint(p2[i]), false, false);
    a[i] = __uint_as_float(r[0]) + __uint_as_float(r[1]);
  }
  f32x4 c;
#pragma unroll
  for (int i = 0; i < 4; ++i) {
    auto r = __builtin_amdgcn_permlane16_swap(__float_as_uint(a[i]), __float_as_uint(a[4 + i]), false, false);
    float t = __uint_as_float(r[0]) + __uint_as_float(r[1]);
    t = dpp_add<0x128>(t); t = dpp_add<0x124>(t);
    c[i] = t;
  }
  if ((lane & 12) == 0)
    *(LAS f32x4*)(smem + LDS_SCR + sv * 256 + (lane >> 5) * 128 + (lane & 3) * 32 + ((lane >> 4) & 1) * 16) = c;
}

__global__ __launch_bounds__(1024, 4) void lnlin_kernel(const u16* __restrict__ x, const u16* __restrict__ g, const u16* __restrict__ b,
                                                        const u16* __restrict__ w, u16* __restrict__ out, char* ws) {
  extern __shared__ __attribute__((aligned(16))) char smem_g[];
  LAS char* smem_l = (LAS char*)smem_g;
  char* smem = smem_g;
  const int bid = blockIdx.x, xcd = bid & 7, idx = bid >> 3;
  const int mi = xcd * 4 + (idx >> 3), ni = idx & 7;
  const int tid = threadIdx.x, lane = tid & 63;
  const int wave = __builtin_amdgcn_readfirstlane(tid >> 6);
  const unsigned lds0 = (unsigned)(size_t)smem_l;

  if (wave < 4) {
    __builtin_amdgcn_s_setprio(2);
    {
      Side s; side_issue(s, x, g, b, w, ws, mi, ni, wave, lane);
      side_compute<0>(s, ws, smem, mi, ni, wave, lane);
    }
    const int wm = wave & 1, wn = wave >> 1, r = lane & 31, h = lane >> 5, sw = (r >> 1) & 7;
    unsigned offx[4], offw[4];
#pragma unroll
    for (int ks = 0; ks < 4; ++ks) {
      const unsigned o = (unsigned)r * 128u + (unsigned)(((2 * ks + h) ^ sw) * 16);
      offx[ks] = o + (unsigned)wm * 8192u + LDS_X;
      offw[ks] = o + (unsigned)wn * 8192u + LDS_W;
    }
    f32x16 acc[2][2] = {};
    bf16x8 fa[2][2], fb[2][2];
    auto ldfrag = [&](auto J) {
      constexpr int j = J, s = j >> 2, ks = j & 3, p = j & 1;
#pragma unroll
      for (int t = 0; t < 2; ++t) {
        fa[p][t] = *(const LAS bf16x8*)(smem_l + offw[ks] + (s & 1) * STG + t * 4096);
        fb[p][t] = *(const LAS bf16x8*)(smem_l + offx[ks] + (s % XR) * STG + t * 4096);
      }
    };
    BARRIER();
    ldfrag(std::integral_constant<int, 0>{});
    static_for<0, 64>([&](auto J) {
      constexpr int j = J, ks = j & 3, p = j & 1;
      if constexpr (ks == 3 && j < 63) {
        acc[0][0] = __builtin_amdgcn_mfma_f32_32x32x16_bf16(fa[p][0], fb[p][0], acc[0][0], 0, 0, 0);
        __builtin_amdgcn_sched_barrier(0);
        BARRIER();
        ldfrag(std::integral_constant<int, j + 1>{});
        __builtin_amdgcn_sched_barrier(0);
      } else {
        if constexpr (j < 63) ldfrag(std::integral_constant<int, j + 1>{});
        __builtin_amdgcn_sched_barrier(0);
        acc[0][0] = __builtin_amdgcn_mfma_f32_32x32x16_bf16(fa[p][0], fb[p][0], acc[0][0], 0, 0, 0);
      }
      acc[0][1] = __builtin_amdgcn_mfma_f32_32x32x16_bf16(fa[p][0], fb[p][1], acc[0][1], 0, 0, 0);
      acc[1][0] = __builtin_amdgcn_mfma_f32_32x32x16_bf16(fa[p][1], fb[p][0], acc[1][0], 0, 0, 0);
      acc[1][1] = __builtin_amdgcn_mfma_f32_32x32x16_bf16(fa[p][1], fb[p][1], acc[1][1], 0, 0, 0);
      __builtin_amdgcn_sched_barrier(0);
    });
    constexpr int STAGE = ((NST - 4) % XR) * STG;
    static_assert(((NST - 3) % XR) == ((NST - 4) % XR) + 1, "staging slots");
    f32x2 rs[2];
#pragma unroll
    for (int mt = 0; mt < 2; ++mt) rs[mt] = *(const LAS f32x2*)(smem_l + LDS_TAB + (wm * 64 + mt * 32 + r) * 8);
#pragma unroll
    for (int q = 0; q < 4; ++q) {
      const int cc = (q & 1) + 2 * h + 4 * (q >> 1);
      const LAS f32x4* pc1 = (const LAS f32x4*)(smem_l + LDS_TAB + 1024 + (wn * 64 + cc * 8) * 4);
      const LAS f32x4* pc2 = (const LAS f32x4*)(smem_l + LDS_TAB + 1536 + (wn * 64 + cc * 8) * 4);
      const f32x4 c1a = pc1[0], c1b = pc1[1], c2a = pc2[0], c2b = pc2[1];
#pragma unroll
      for (int mt = 0; mt < 2; ++mt) {
        const float nm = -rs[mt][0], rstd = rs[mt][1];
        u32x4 o;
#pragma unroll
        for (int ep = 0; ep < 4; ++ep) {
          const int tt = ep >> 1, reg = 2 * (q & 1) + 4 * ((q >> 1) + 2 * (ep & 1));
          const float c1l = tt ? c1b[2 * (ep & 1)] : c1a[2 * (ep & 1)], c1h = tt ? c1b[2 * (ep & 1) + 1] : c1a[2 * (ep & 1) + 1];
          const float c2l = tt ? c2b[2 * (ep & 1)] : c2a[2 * (ep & 1)], c2h = tt ? c2b[2 * (ep & 1) + 1] : c2a[2 * (ep & 1) + 1];
          const float lo = fmaf(rstd, fmaf(nm, c1l, acc[tt][mt][reg]), c2l);
          const float hi = fmaf(rstd, fmaf(nm, c1h, acc[tt][mt][reg + 1]), c2h);
          o[ep] = cvt_pk(lo, hi);
        }
        const int row = wm * 64 + mt * 32 + r;
        *(LAS u32x4*)(smem_l + LDS_X + STAGE + row * 256 + (((wn * 8 + cc) ^ (row & 7)) * 16)) = o;
      }
    }
  } else if (wave < 12) {
    const int f = tid - 256, c = (f & 7) + 8 * ((f >> 5) & 1), kp = ((f >> 3) & 3) + 4 * (f >> 6);
    const u16* wp = w + (size_t)(2 * kp) * ND + ni * 128 + c * 8;
    const unsigned* gp = (const unsigned*)g + kp;
    const unsigned wo = LDS_W + (unsigned)(64 * (c >> 3) + 2 * (c & 7)) * 128u + (unsigned)(((kp >> 2) ^ (c & 7)) * 16 + (kp & 3) * 4);
    u32x4 wr[WD][2]; unsigned gr[WD];
    auto issue = [&](auto S) {
      constexpr int s = S;
      wr[s % WD][0] = *(const u32x4*)(wp + (size_t)s * 64 * ND);
      wr[s % WD][1] = *(const u32x4*)(wp + (size_t)s * 64 * ND + ND);
      gr[s % WD] = gp[s * 32];
    };
    static_for<0, WD>([&](auto S) { issue(S); });
    static_for<0, NST>([&](auto S) {
      constexpr int s = S;
      const float g0 = bf_lo(gr[s % WD]), g1 = bf_hi(gr[s % WD]);
      const u32x4 w0 = wr[s % WD][0], w1 = wr[s % WD][1];
#pragma unroll
      for (int i = 0; i < 4; ++i) {
        const unsigned lo = cvt_pk(g0 * bf_lo(w0[i]), g1 * bf_lo(w1[i]));
        const unsigned hi = cvt_pk(g0 * bf_hi(w0[i]), g1 * bf_hi(w1[i]));
        const unsigned eo = (unsigned)((i >> 1) * 4096 + (i & 1) * 2048);
        *(LAS unsigned*)(smem_l + wo + (s & 1) * STG + eo) = lo;
        *(LAS unsigned*)(smem_l + wo + (s & 1) * STG + eo + 128) = hi;
      }
      if constexpr (s + WD < NST) issue(std::integral_constant<int, s + WD>{});
      BARRIER();
    });
  } else {
    const int d = wave - 12;
    Side s; side_issue(s, x, g, b, w, ws, mi, ni, 4 + d, lane);
    unsigned xo[4];
#pragma unroll
    for (int i = 0; i < 4; ++i) {
      const int row = d * 32 + i * 8 + (lane >> 3);
      xo[i] = (unsigned)(mi * 128 + row) * 2048u + (unsigned)(((lane & 7) ^ ((row >> 1) & 7)) * 16);
    }
    const unsigned ldsx = lds0 + LDS_X + (unsigned)d * 4096u;
    auto dma = [&](auto S) {
      constexpr int st = S;
      const char* base = (const char*)x + st * 128;
#pragma unroll
      for (int i = 0; i < 4; ++i) glds16(xo[i], base, ldsx + (st % XR) * STG + i * 1024);
    };
    static_for<0, XR>([&](auto S) { dma(S); });
    side_compute<4 * XR>(s, ws, smem, mi, ni, 4 + d, lane);
    const unsigned tagr = s.epr + 1u, tagc = s.epc + 1u;
    wait_vm<4 * (XR - 1)>();
    BARRIER();
    const int t = tid - 768;
    constexpr int GP = 11;
    static_assert(GP >= NST - XR, "granule loads must follow the last DMA");
    u32x2 gr[9];
    static_for<0, NST - 1>([&](auto S) {
      constexpr int st = S;
      if constexpr (st == 0) {
        if (d == 0) {
          float v = 0.f;
#pragma unroll
          for (int i = 0; i < 8; ++i) v += *(const LAS float*)(smem_l + LDS_SCR + i * 256 + lane * 4);
          store_granule(ws, WS_C + ((size_t)((ni * 8 + (mi >> 2)) * 128 + (mi & 3) * 32 + (lane & 31)) * 2 + (lane >> 5)) * 8, tagc, v);
        }
      }
      if constexpr (st >= 1 && st - 1 + XR < NST) dma(std::integral_constant<int, st - 1 + XR>{});
      if constexpr (st == GP) {
        gr[0] = gload_u64_sc1_asm((unsigned)t * 8u, ws + WS_RS + (size_t)mi * 2048);
#pragma unroll
        for (int j = 0; j < 8; ++j) gr[1 + j] = gload_u64_sc1_asm((unsigned)t * 8u + j * 2048u, ws + WS_C + (size_t)ni * 16384);
      }
      constexpr int last = (st - 1 + XR < NST) ? (st == 0 ? XR - 1 : st - 1 + XR) : NST - 1;
      constexpr int younger = 4 * (last - (st + 1)) + (st >= GP ? 9 : 0);
      if constexpr (st < NST - 2) {
        wait_vm<younger>();
      } else {
        asm volatile("s_waitcnt vmcnt(0)"
                     : "+v"(gr[0]), "+v"(gr[1]), "+v"(gr[2]), "+v"(gr[3]), "+v"(gr[4]), "+v"(gr[5]), "+v"(gr[6]), "+v"(gr[7]), "+v"(gr[8])
                     :: "memory");
        bool ok = gr[0][1] == tagr;
#pragma unroll
        for (int j = 0; j < 8; ++j) ok &= gr[1 + j][1] == tagc;
        float vr = __uint_as_float(gr[0][0]), vc = 0.f;
#pragma unroll
        for (int j = 0; j < 8; ++j) vc += __uint_as_float(gr[1 + j][0]);
        if (!__all(ok)) {
          for (int it = 0; it < 20000; ++it) {
            __builtin_amdgcn_s_sleep(8);
            u64 q = load_granule(ws, WS_RS + (size_t)mi * 2048 + (size_t)t * 8);
            ok = (unsigned)(q >> 32) == tagr; vr = __uint_as_float((unsigned)q); vc = 0.f;
            for (int j = 0; j < 8; ++j) {
              q = load_granule(ws, WS_C + (size_t)ni * 16384 + (size_t)j * 2048 + (size_t)t * 8);
              ok &= (unsigned)(q >> 32) == tagc; vc += __uint_as_float((unsigned)q);
            }
            if (__all(ok)) break;
          }
          if (!__all(ok)) {
            const u16* xr = x + (size_t)(mi * 128 + (t >> 1)) * KD;
            float sm = 0.f, sq = 0.f;
            for (int k = 0; k < KD; ++k) sm += bf_lo(xr[k]);
            sm *= (1.f / 1024.f);
            for (int k = 0; k < KD; ++k) { const float dd = bf_lo(xr[k]) - sm; sq = fmaf(dd, dd, sq); }
            vr = (t & 1) ? 1.0f / sqrtf(sq * (1.f / 1024.f) + 1e-5f) : sm;
            const u16* wc = w + ni * 128 + (t >> 1);
            vc = 0.f;
            for (int k = 0; k < KD; ++k) {
              const float wv = bf_lo(wc[(size_t)k * ND]);
              vc += (t & 1) ? bf_lo(b[k]) * wv : bf_lo(cvt_pk(bf_lo(g[k]) * wv, 0.f));
            }
          }
        }
        *(LAS float*)(smem_l + LDS_TAB + t * 4) = vr;
        *(LAS float*)(smem_l + LDS_TAB + 1024 + (t & 1) * 512 + (t >> 1) * 4) = vc;
      }
      BARRIER();
    });
    if (t == 0) {
      __hip_atomic_fetch_add((GAS unsigned*)(ws + WS_EPR + mi * 128), 1u, __ATOMIC_RELAXED, __HIP_MEMORY_SCOPE_AGENT);
      __hip_atomic_fetch_add((GAS unsigned*)(ws + WS_EPC + ni * 128), 1u, __ATOMIC_RELAXED, __HIP_MEMORY_SCOPE_AGENT);
    }
  }
  BARRIER();
  {
    constexpr int STAGE = ((NST - 4) % XR) * STG;
#pragma unroll
    for (int i = 0; i < 2; ++i) {
      const int row = wave * 8 + i * 4 + (lane >> 4), ch = lane & 15;
      const u32x4 v = *(const LAS u32x4*)(smem_l + LDS_X + STAGE + row * 256 + ((ch ^ (row & 7)) * 16));
      *(u32x4*)(out + (size_t)(mi * 128 + row) * ND + ni * 128 + ch * 8) = v;
    }
  }
}

extern "C" void kernel_launch(void* const* d_in, const int* in_sizes, int n_in, void* d_out, int out_size, void* d_ws, size_t ws_size,
                              hipStream_t stream) {
  const u16* x = (const u16*)d_in[0]; const u16* g = (const u16*)d_in[1]; const u16* b = (const u16*)d_in[2]; const u16* w = (const u16*)d_in[3];
  hipFuncSetAttribute((const void*)lnlin_kernel, hipFuncAttributeMaxDynamicSharedMemorySize, LDS_TOTAL);
  lnlin_kernel<<<256, 1024, LDS_TOTAL, stream>>>(x, g, b, w, (u16*)d_out, (char*)d_ws);
}
```

```cpp
#include <hip/hip_runtime.h>
#include <hip/hip_bf16.h>
#include <utility>
#pragma clang diagnostic ignored "-Wint-to-pointer-cast"

typedef short bf16x8 __attribute__((ext_vector_type(8)));
typedef short s16x4 __attribute__((ext_vector_type(4)));
typedef float f32x16 __attribute__((ext_vector_type(16)));
typedef float f32x4 __attribute__((ext_vector_type(4)));
typedef float f32x2 __attribute__((ext_vector_type(2)));
typedef unsigned u32x4 __attribute__((ext_vector_type(4)));
typedef unsigned u32x2 __attribute__((ext_vector_type(2)));
typedef __bf16 bf16x2_t __attribute__((ext_vector_type(2)));
typedef unsigned short u16;
typedef unsigned long long u64;
#define GAS __attribute__((address_space(1)))
#define LAS __attribute__((address_space(3)))

#define PRIO_M 2
#define PRIO_L 0

constexpr int KD = 1024, ND = 1024;
constexpr int NST = 16;
constexpr int RING = 4;
constexpr int SLOT = 32768;
constexpr int LDS_G = RING * SLOT;
constexpr int LDS_TAB = LDS_G + 2048 + 64;
constexpr int LDS_TAG = LDS_TAB + 2048;
constexpr int LDS_TOTAL = LDS_TAG + 16;

constexpr size_t WS_EPR = 0;
constexpr size_t WS_EPC = 32 * 128;
constexpr size_t WS_RS = 8192;
constexpr size_t WS_C = WS_RS + 4096 * 2 * 8;

#define STAMP_DECL
#define STAMP_RT(i)
#define STAMP(i)
#define STAMP_DYN(i)
#define STAMP_FLUSH()

template <int B, int E, class F> __device__ __forceinline__ void static_for(F&& f) {
  if constexpr (B < E) { f(std::integral_constant<int, B>{}); static_for<B + 1, E>(f); }
}
__device__ __forceinline__ float bf_lo(unsigned u) { return __uint_as_float(u << 16); }
__device__ __forceinline__ float bf_hi(unsigned u) { return __uint_as_float(u & 0xffff0000u); }
__device__ __forceinline__ unsigned cvt_pk(float lo, float hi) {
  f32x2 v = {lo, hi}; return __builtin_bit_cast(unsigned, __builtin_convertvector(v, bf16x2_t));
}
template <int CTRL> __device__ __forceinline__ float dpp_add(float x) {
  return x + __builtin_bit_cast(float, __builtin_amdgcn_update_dpp(0, __builtin_bit_cast(int, x), CTRL, 0xf, 0xf, true));
}
__device__ __forceinline__ float sum16(float x) {
  x = dpp_add<0x128>(x); x = dpp_add<0x124>(x); x = dpp_add<0x122>(x); x = dpp_add<0x121>(x);
  return x;
}
#define SCHED() __builtin_amdgcn_sched_barrier(0)
#define BARRIER_L() do { SCHED(); asm volatile("s_waitcnt lgkmcnt(0)\n\ts_barrier" ::: "memory"); SCHED(); } while (0)
#define BARRIER_N() do { SCHED(); asm volatile("s_barrier" ::: "memory"); SCHED(); } while (0)
template <int N> __device__ __forceinline__ void wait_vm() { asm volatile("s_waitcnt vmcnt(%0)" ::"n"(N) : "memory"); }

__device__ __forceinline__ void glds16x4(unsigned v0, unsigned v1, unsigned v2, unsigned v3, const void* sbase, unsigned lds_dst) {
  unsigned keep;
  asm volatile("s_mov_b32 %0, m0\n\ts_mov_b32 m0, %6\n\ts_nop 0\n\t"
               "global_load_lds_dwordx4 %1, %5\n\tglobal_load_lds_dwordx4 %2, %5 offset:1024\n\t"
               "global_load_lds_dwordx4 %3, %5 offset:2048\n\tglobal_load_lds_dwordx4 %4, %5 offset:3072\n\ts_mov_b32 m0, %0"
               : "=&s"(keep) : "v"(v0), "v"(v1), "v"(v2), "v"(v3), "s"(sbase), "s"(lds_dst) : "memory");
}
__device__ __forceinline__ void glds4x2(unsigned v0, const void* sbase, unsigned lds_dst) {
  unsigned keep;
  asm volatile("s_mov_b32 %0, m0\n\ts_mov_b32 m0, %3\n\ts_nop 0\n\t"
               "global_load_lds_dword %1, %2\n\tglobal_load_lds_dword %1, %2 offset:256\n\ts_mov_b32 m0, %0"
               : "=&s"(keep) : "v"(v0), "s"(sbase), "s"(lds_dst) : "memory");
}
template <int OFF> __device__ __forceinline__ u32x4 gload16_asm(unsigned voff, const void* sbase) {
  u32x4 r; asm volatile("global_load_dwordx4 %0, %1, %2 offset:%3" : "=v"(r) : "v"(voff), "s"(sbase), "n"(OFF) : "memory"); return r;
}
__device__ __forceinline__ unsigned gload_u32_sc1_asm(unsigned voff, const void* sbase) {
  unsigned r; asm volatile("global_load_dword %0, %1, %2 sc1" : "=v"(r) : "v"(voff), "s"(sbase) : "memory"); return r;
}
template <int OFF> __device__ __forceinline__ u32x2 gload_u64_sc1_asm(unsigned voff, const void* sbase) {
  u32x2 r; asm volatile("global_load_dwordx2 %0, %1, %2 offset:%3 sc1" : "=v"(r) : "v"(voff), "s"(sbase), "n"(OFF) : "memory"); return r;
}
__device__ __forceinline__ void store_granule(char* ws, size_t off, unsigned tag, float v) {
  __hip_atomic_store((GAS u64*)(ws + off), ((u64)tag << 32) | __float_as_uint(v), __ATOMIC_RELAXED, __HIP_MEMORY_SCOPE_AGENT);
}
__device__ __forceinline__ u64 load_granule(const char* ws, size_t off) {
  return __hip_atomic_load((GAS u64*)(ws + off), __ATOMIC_RELAXED, __HIP_MEMORY_SCOPE_AGENT);
}
__device__ __forceinline__ unsigned load_word(const char* ws, size_t off) {
  return __hip_atomic_load((GAS unsigned*)(ws + off), __ATOMIC_RELAXED, __HIP_MEMORY_SCOPE_AGENT);
}

__global__ __launch_bounds__(512, 3) void lnlin_kernel(const u16* __restrict__ x, const u16* __restrict__ g, const u16* __restrict__ b,
                                                       const u16* __restrict__ w, u16* __restrict__ out, char* ws) {
  u64 code_pc; unsigned code_len;
  asm volatile("s_getpc_b64 %0\nlnlin_code_begin:\n\ts_mov_b32 %1, lnlin_code_end-lnlin_code_begin" : "=s"(code_pc), "=s"(code_len));
  extern __shared__ __attribute__((aligned(16))) char smem_g[];
  const int bid = blockIdx.x, xcd = bid & 7, idx = bid >> 3;
  const int mi = xcd * 4 + (idx >> 3), ni = idx & 7;
  const int tid = threadIdx.x, lane = tid & 63;
  const int wave = __builtin_amdgcn_readfirstlane(tid >> 6);
  const unsigned lds0 = (unsigned)(size_t)(LAS char*)smem_g;
  STAMP_DECL STAMP_RT(0); STAMP(1);

  if (wave >= 4) {
    const int d = wave - 4, t = tid - 256;
    const unsigned co = (unsigned)d * 3072u + (unsigned)lane * 16u, lim = (code_len & ~15u) - 16u;
    u32x4 t0 = gload16_asm<0>(min(co, lim), (const void*)code_pc), t1 = gload16_asm<0>(min(co + 1024u, lim), (const void*)code_pc),
          t2 = gload16_asm<0>(min(co + 2048u, lim), (const void*)code_pc);
    if (PRIO_L) __builtin_amdgcn_s_setprio(PRIO_L);
    unsigned xo[4], wo[4];
#pragma unroll
    for (int i = 0; i < 4; ++i) {
      const int row = i * 8 + (lane >> 3);
      xo[i] = (unsigned)row * 2048u + (unsigned)(((lane & 7) ^ ((row >> 1) & 7)) * 16) - (unsigned)i * 1024u;
      const int kr = i * 4 + (lane >> 4);
      wo[i] = (unsigned)kr * 2048u + (unsigned)(((lane & 15) ^ (((lane >> 4) << 2) | i)) * 16) - (unsigned)i * 1024u;
    }
    const unsigned dst = lds0 + (unsigned)d * 4096u;
    const char* xb = (const char*)x + (size_t)mi * 262144 + d * 65536;
    const char* wb = (const char*)w + d * 32768 + ni * 256;
    auto issue = [&](int st) {
      const unsigned ds_ = dst + (unsigned)(st & (RING - 1)) * SLOT;
      glds16x4(wo[0], wo[1], wo[2], wo[3], wb + (size_t)st * 131072, ds_ + 16384u);
      glds16x4(xo[0], xo[1], xo[2], xo[3], xb + st * 128, ds_);
    };
    glds4x2((unsigned)lane * 4u, (const char*)g + d * 512, lds0 + LDS_G + (unsigned)d * 512u);
    issue(0); issue(1);
    STAMP(2);
    asm volatile("s_waitcnt vmcnt(8)" : "+v"(t0), "+v"(t1), "+v"(t2)::"memory");
    STAMP(3);
    BARRIER_N();
    constexpr int GP = 11;
#pragma unroll 1
    for (int j = 0; j < GP; ++j) {
      issue(j + 2);
      wait_vm<8>();
      BARRIER_N();
      STAMP_DYN(j + 4);
    }
    u32x2 gr[9];
    issue(13);
    gr[0] = gload_u64_sc1_asm<0>((unsigned)t * 8u, ws + WS_RS + (size_t)mi * 2048);
    static_for<0, 8>([&](auto J) { constexpr int j = J;
      gr[1 + j] = gload_u64_sc1_asm<(j & 1) * 2048>((unsigned)t * 8u + (j >> 1) * 4096u, ws + WS_C + (size_t)ni * 16384); });
    wait_vm<17>(); BARRIER_N();
    STAMP(15);
    issue(14);
    wait_vm<17>(); BARRIER_N();
    STAMP(16);
    issue(15);
    asm volatile("s_waitcnt vmcnt(8)"
                 : "+v"(gr[0]), "+v"(gr[1]), "+v"(gr[2]), "+v"(gr[3]), "+v"(gr[4]), "+v"(gr[5]), "+v"(gr[6]), "+v"(gr[7]), "+v"(gr[8]) :: "memory");
    BARRIER_N();
    STAMP(17);
    const u32x2 tags = *(const LAS u32x2*)(lds0 + LDS_TAG);
    const unsigned tagr = tags[0], tagc = tags[1];
    bool ok = gr[0][1] == tagr;
#pragma unroll
    for (int j = 0; j < 8; ++j) ok &= gr[1 + j][1] == tagc;
    float vr = __uint_as_float(gr[0][0]), vc = 0.f;
#pragma unroll
    for (int j = 0; j < 8; ++j) vc += __uint_as_float(gr[1 + j][0]);
    if (!__all(ok)) {
#pragma unroll 1
      for (int it = 0; it < 20000; ++it) {
        u64 q[9];
        q[0] = load_granule(ws, WS_RS + (size_t)mi * 2048 + (size_t)t * 8);
#pragma unroll
        for (int j = 0; j < 8; ++j) q[1 + j] = load_granule(ws, WS_C + (size_t)ni * 16384 + (size_t)j * 2048 + (size_t)t * 8);
        ok = (unsigned)(q[0] >> 32) == tagr; vr = __uint_as_float((unsigned)q[0]); vc = 0.f;
#pragma unroll
        for (int j = 0; j < 8; ++j) { ok &= (unsigned)(q[1 + j] >> 32) == tagc; vc += __uint_as_float((unsigned)q[1 + j]); }
        if (__all(ok)) break;
        __builtin_amdgcn_s_sleep(4);
      }
      if (!__all(ok)) {
        const u16* xrow = x + (size_t)(mi * 128 + (t >> 1)) * KD;
        float sm = 0.f, sq = 0.f;
#pragma unroll 1
        for (int k = 0; k < KD; ++k) sm += bf_lo(xrow[k]);
        sm *= (1.f / 1024.f);
#pragma unroll 1
        for (int k = 0; k < KD; ++k) { const float dd = bf_lo(xrow[k]) - sm; sq = fmaf(dd, dd, sq); }
        vr = (t & 1) ? __builtin_amdgcn_rsqf(sq * (1.f / 1024.f) + 1e-5f) : sm;
        const u16* wc = w + ni * 128 + (t >> 1);
        const u16* gb = (t & 1) ? b : g;
        vc = 0.f;
#pragma unroll 1
        for (int k = 0; k < KD; ++k) {
          const float pr = bf_lo(gb[k]) * bf_lo(wc[(size_t)k * ND]);
          vc += (t & 1) ? pr : bf_lo(cvt_pk(pr, 0.f));
        }
      }
    }
    *(LAS float*)(lds0 + LDS_TAB + t * 4) = vr;
    *(LAS float*)(lds0 + LDS_TAB + 1024 + (t & 1) * 512 + (t >> 1) * 4) = vc;
    STAMP(18);
    wait_vm<0>();
    BARRIER_L();
    STAMP(19);
    if (t == 0) {
      __hip_atomic_fetch_add((GAS unsigned*)(ws + WS_EPR + mi * 128), 1u, __ATOMIC_RELAXED, __HIP_MEMORY_SCOPE_AGENT);
      __hip_atomic_fetch_add((GAS unsigned*)(ws + WS_EPC + ni * 128), 1u, __ATOMIC_RELAXED, __HIP_MEMORY_SCOPE_AGENT);
    }
  } else {
    const int wn = wave;
    {
      const int srow = mi * 128 + ni * 16 + wn * 4 + (lane >> 4);
      const char* xs = (const char*)x + (size_t)srow * 2048 + (lane & 15) * 16;
      u32x4 xr[8];
#pragma unroll
      for (int j = 0; j < 8; ++j) xr[j] = *(const GAS u32x4*)(xs + j * 256);
      const int sk = (mi >> 2) * 128 + lane, scol = (mi & 3) * 32 + wn * 8;
      const char* wsl = (const char*)w + (size_t)sk * 2048 + (ni * 128 + scol) * 2;
      const u32x4 wv0 = *(const GAS u32x4*)wsl, wv1 = *(const GAS u32x4*)(wsl + 64 * 2048);
      const unsigned gv0 = g[sk], gv1 = g[sk + 64], bv0 = b[sk], bv1 = b[sk + 64];
      const unsigned epr = load_word(ws, WS_EPR + mi * 128), epc = load_word(ws, WS_EPC + ni * 128);
      SCHED();
      float s0 = 0.f, s1 = 0.f, s2 = 0.f, s3 = 0.f;
#pragma unroll
      for (int j = 0; j < 8; ++j) {
        s0 += bf_lo(xr[j][0]); s1 += bf_hi(xr[j][0]); s2 += bf_lo(xr[j][1]); s3 += bf_hi(xr[j][1]);
        s0 += bf_lo(xr[j][2]); s1 += bf_hi(xr[j][2]); s2 += bf_lo(xr[j][3]); s3 += bf_hi(xr[j][3]);
      }
      const float mean = sum16((s0 + s1) + (s2 + s3)) * (1.f / 1024.f);
      float q0 = 0.f, q1 = 0.f, q2 = 0.f, q3 = 0.f;
#pragma unroll
      for (int j = 0; j < 8; ++j)
#pragma unroll
        for (int i = 0; i < 4; i += 2) {
          const float d0 = bf_lo(xr[j][i]) - mean, d1 = bf_hi(xr[j][i]) - mean, d2 = bf_lo(xr[j][i + 1]) - mean, d3 = bf_hi(xr[j][i + 1]) - mean;
          q0 = fmaf(d0, d0, q0); q1 = fmaf(d1, d1, q1); q2 = fmaf(d2, d2, q2); q3 = fmaf(d3, d3, q3);
        }
      const float var = sum16((q0 + q1) + (q2 + q3)) * (1.f / 1024.f);
      const float rstd = __builtin_amdgcn_rsqf(var + 1e-5f);
      STAMP(2);
      float p1[8], p2[8];
      const float ga = bf_lo(gv0), gb = bf_lo(gv1), ba = bf_lo(bv0), bb = bf_lo(bv1);
#pragma unroll
      for (int i = 0; i < 4; ++i) {
        const float l0 = bf_lo(wv0[i]), h0 = bf_hi(wv0[i]), l1 = bf_lo(wv1[i]), h1 = bf_hi(wv1[i]);
        const unsigned pa = cvt_pk(ga * l0, ga * h0), pb = cvt_pk(gb * l1, gb * h1);
        p1[2 * i] = bf_lo(pa) + bf_lo(pb); p1[2 * i + 1] = bf_hi(pa) + bf_hi(pb);
        p2[2 * i] = fmaf(ba, l0, bb * l1); p2[2 * i + 1] = fmaf(ba, h0, bb * h1);
      }
      float a[8];
#pragma unroll
      for (int i = 0; i < 8; ++i) {
        auto rr = __builtin_amdgcn_permlane32_swap(__float_as_uint(p1[i]), __float_as_uint(p2[i]), false, false);
        a[i] = __uint_as_float(rr[0]) + __uint_as_float(rr[1]);
      }
      float cs[4];
#pragma unroll
      for (int i = 0; i < 4; ++i) {
        auto rr = __builtin_amdgcn_permlane16_swap(__float_as_uint(a[i]), __float_as_uint(a[4 + i]), false, false);
        cs[i] = sum16(__uint_as_float(rr[0]) + __uint_as_float(rr[1]));
      }
      const int l4 = lane & 15;
      const float cv = l4 == 0 ? cs[0] : (l4 == 1 ? cs[1] : (l4 == 2 ? cs[2] : cs[3]));
      if (l4 < 2) store_granule(ws, WS_RS + ((size_t)srow * 2 + (lane & 1)) * 8, epr + 1u, (lane & 1) ? rstd : mean);
      if (l4 < 4) store_granule(ws, WS_C + ((size_t)((ni * 8 + (mi >> 2)) * 128 + scol + ((lane >> 4) & 1) * 4 + l4) * 2 + (lane >> 5)) * 8, epc + 1u, cv);
      if (tid == 0) *(LAS u32x2*)(lds0 + LDS_TAG) = (u32x2){epr + 1u, epc + 1u};
    }
    STAMP(3);
    if (PRIO_M) __builtin_amdgcn_s_setprio(PRIO_M);
    const int r = lane & 31, h = lane >> 5, sw = (r >> 1) & 7;
    unsigned offx[4], offw[2];
#pragma unroll
    for (int ks = 0; ks < 4; ++ks) offx[ks] = lds0 + (unsigned)r * 128u + (unsigned)(((2 * ks + h) ^ sw) * 16);
    {
      const int blk = (lane >> 4) & 1, q = (lane & 15) >> 2, p = lane & 3;
#pragma unroll
      for (int tt = 0; tt < 2; ++tt) {
        const int row = 8 * h + 4 * tt + q, ch = 4 * wn + blk + 2 * (p & 1);
        offw[tt] = lds0 + 16384u + (unsigned)(256 * row + 16 * (ch ^ ((q << 2) | ((2 * h + tt) & 3))) + 8 * (p >> 1));
      }
    }
    unsigned offg = lds0 + LDS_G + (unsigned)h * 16u;
    const unsigned li = lane & 3;
    const unsigned mk0 = li == 0 ? 0xffffu : (li == 1 ? 0xffff0000u : 0u), mk1 = li == 2 ? 0xffffu : (li == 3 ? 0xffff0000u : 0u);
    f32x16 acc[4] = {};
    bf16x8 xf[4];
    s16x4 wra[2], wrb[2];
    u32x4 gq, dg;
    u32x4 wf, wfn;
    f32x4 d0, d1;
    const f32x4 z4 = {0.f, 0.f, 0.f, 0.f};
#define RX(mt, ks) xf[mt] = *(const LAS bf16x8*)(offx[ks] + (mt) * 4096)
#define RW(R, tt, ks) R[tt] = __builtin_amdgcn_ds_read_tr16_b64_v4i16((LAS s16x4*)(offw[tt] + (ks) * 4096))
#define RG(o) gq = *(const LAS u32x4*)(offg + (o) * 32)
#define DG0() do { dg[0] = gq[0] & mk[0]; dg[1] = gq[1] & mk[1]; } while (0)
#define DG1() do { dg[2] = gq[2] & mk[0]; dg[3] = gq[3] & mk[1]; } while (0)
#define SM0(R) d0 = __builtin_amdgcn_mfma_f32_4x4x4bf16_1k(__builtin_bit_cast(s16x4, (u32x2){dg[0], dg[1]}), R[0], z4, 0, 0, 0)
#define SM1(R) d1 = __builtin_amdgcn_mfma_f32_4x4x4bf16_1k(__builtin_bit_cast(s16x4, (u32x2){dg[2], dg[3]}), R[1], z4, 0, 0, 0)
#define CV0(W) do { W[0] = cvt_pk(d0[0], d0[1]); W[1] = cvt_pk(d0[2], d0[3]); } while (0)
#define CV1(W) do { W[2] = cvt_pk(d1[0], d1[1]); W[3] = cvt_pk(d1[2], d1[3]); } while (0)
#define MM(mt, W) acc[mt] = __builtin_amdgcn_mfma_f32_32x32x16_bf16(__builtin_bit_cast(bf16x8, W), xf[mt], acc[mt], 0, 0, 0)
#define STEP(WC, WN, RA, RB, kx, kw, kg, EXTRA2, EXTRA3) do { \
      SM0(RA); SM1(RA); SCHED(); \
      MM(0, WC); SCHED(); RX(0, kx); RW(RB, 0, kw); DG0(); SCHED(); \
      MM(1, WC); SCHED(); RX(1, kx); RW(RB, 1, kw); DG1(); SCHED(); \
      MM(2, WC); SCHED(); RX(2, kx); RG(kg); CV0(WN); EXTRA2; SCHED(); \
      MM(3, WC); SCHED(); RX(3, kx); CV1(WN); EXTRA3; SCHED(); } while (0)
    unsigned mk[2] = {mk0, mk1};
    asm volatile("" : "+v"(acc[0]), "+v"(acc[1]), "+v"(acc[2]), "+v"(acc[3]));
    asm volatile("" : "+v"(offx[0]), "+v"(offx[1]), "+v"(offx[2]), "+v"(offx[3]), "+v"(offw[0]), "+v"(offw[1]), "+v"(offg), "+v"(mk[0]), "+v"(mk[1]));
    __builtin_amdgcn_s_waitcnt(0xC07F);
    STAMP(4);
    BARRIER_N();
    STAMP(5);
    RG(0); RW(wrb, 0, 0); RW(wrb, 1, 0); RX(0, 0); RW(wra, 0, 1); RW(wra, 1, 1); RX(1, 0); RX(2, 0); RX(3, 0);
    DG0(); DG1(); SCHED(); RG(1); SM0(wrb); SM1(wrb); SCHED();
    CV0(wf); CV1(wf); DG0(); DG1(); SCHED(); RG(2); SCHED();
#pragma unroll 1
    for (int s = 0; s < NST - 1; ++s) {
      const int dlt = ((s & (RING - 1)) == RING - 1) ? -(RING - 1) * SLOT : SLOT;
      STEP(wf, wfn, wra, wrb, 1, 2, 3, (void)0, (void)0);
      STEP(wfn, wf, wrb, wra, 2, 3, 4, (offx[0] += dlt, offw[0] += dlt), (offw[1] += dlt, offx[1] += dlt));
      BARRIER_N();
      STEP(wf, wfn, wra, wrb, 3, 0, 5, (offx[2] += dlt), (void)0);
      STEP(wfn, wf, wrb, wra, 0, 1, 6, (offx[3] += dlt), (offg += 128u));
    }
    f32x4 c1v[4], c2v[4];
    f32x2 rs[4];
    const unsigned tc = lds0 + LDS_TAB + 1024 + (unsigned)(wn * 32 + h * 16) * 4u, tr = lds0 + LDS_TAB + (unsigned)r * 8u;
#define RC(q) do { c1v[q] = *(const LAS f32x4*)(tc + (q) * 16); c2v[q] = *(const LAS f32x4*)(tc + 512 + (q) * 16); rs[q] = *(const LAS f32x2*)(tr + (q) * 256); } while (0)
    STEP(wf, wfn, wra, wrb, 1, 2, 3, (void)0, (void)0);
    STEP(wfn, wf, wrb, wra, 2, 3, 4, (void)0, (void)0);
    SM0(wra); SM1(wra); SCHED();
    MM(0, wf); SCHED(); RX(0, 3); SCHED();
    MM(1, wf); SCHED(); RX(1, 3); SCHED();
    MM(2, wf); SCHED(); RX(2, 3); CV0(wfn); SCHED();
    MM(3, wf); SCHED(); RX(3, 3); CV1(wfn); SCHED();
    MM(0, wfn); SCHED(); RC(0); SCHED(); MM(1, wfn); SCHED(); RC(1); SCHED(); MM(2, wfn); SCHED(); RC(2); SCHED(); MM(3, wfn); SCHED(); RC(3); SCHED();
    STAMP(20);
    const unsigned so = lds0 + (unsigned)r * 256u, sc0 = (unsigned)(((wn * 4 + h * 2) ^ (r & 7)) * 16), sc1 = sc0 ^ 16u;
#pragma unroll
    for (int mt = 0; mt < 4; ++mt) {
      const float nm = -rs[mt][0], rstd = rs[mt][1];
      f32x4 tq[4];
#pragma unroll
      for (int q = 0; q < 4; ++q) {
        const f32x4 av = {acc[mt][4 * q], acc[mt][4 * q + 1], acc[mt][4 * q + 2], acc[mt][4 * q + 3]};
        tq[q] = av + c1v[q] * nm;
      }
#pragma unroll
      for (int q = 0; q < 4; ++q) tq[q] = tq[q] * rstd + c2v[q];
      const u32x4 o0 = {cvt_pk(tq[0][0], tq[0][1]), cvt_pk(tq[0][2], tq[0][3]), cvt_pk(tq[1][0], tq[1][1]), cvt_pk(tq[1][2], tq[1][3])};
      const u32x4 o1 = {cvt_pk(tq[2][0], tq[2][1]), cvt_pk(tq[2][2], tq[2][3]), cvt_pk(tq[3][0], tq[3][1]), cvt_pk(tq[3][2], tq[3][3])};
      *(LAS u32x4*)(so + mt * 8192 + sc0) = o0;
      *(LAS u32x4*)(so + mt * 8192 + sc1) = o1;
      SCHED();
    }
  }
  STAMP(24);
  BARRIER_L();
  STAMP(25);
#pragma unroll
  for (int i = 0; i < 4; ++i) {
    const int row = (wave * 4 + i) * 4 + (lane >> 4), ch = lane & 15;
    const u32x4 v = *(const LAS u32x4*)(lds0 + row * 256 + ((ch ^ (row & 7)) * 16));
    *(u32x4*)(out + (size_t)(mi * 128 + row) * ND + ni * 128 + ch * 8) = v;
  }
  STAMP_FLUSH();
  asm volatile("lnlin_code_end:");
}

extern "C" void kernel_launch(void* const* d_in, const int* in_sizes, int n_in, void* d_out, int out_size, void* d_ws, size_t ws_size,
                              hipStream_t stream) {
  const u16* x = (const u16*)d_in[0]; const u16* g = (const u16*)d_in[1]; const u16* b = (const u16*)d_in[2]; const u16* w = (const u16*)d_in[3];
  (void)hipFuncSetAttribute((const void*)lnlin_kernel, hipFuncAttributeMaxDynamicSharedMemorySize, LDS_TOTAL);
  lnlin_kernel<<<256, 512, LDS_TOTAL, stream>>>(x, g, b, w, (u16*)d_out, (char*)d_ws);
}
```

```cpp
#define NO_CODE_PF
#include <hip/hip_runtime.h>
#include <hip/hip_bf16.h>
#include <utility>
#pragma clang diagnostic ignored "-Wint-to-pointer-cast"

typedef short bf16x8 __attribute__((ext_vector_type(8)));
typedef short s16x4 __attribute__((ext_vector_type(4)));
typedef float f32x16 __attribute__((ext_vector_type(16)));
typedef float f32x4 __attribute__((ext_vector_type(4)));
typedef float f32x2 __attribute__((ext_vector_type(2)));
typedef unsigned u32x4 __attribute__((ext_vector_type(4)));
typedef unsigned u32x2 __attribute__((ext_vector_type(2)));
typedef __bf16 bf16x2_t __attribute__((ext_vector_type(2)));
typedef unsigned short u16;
typedef unsigned long long u64;
#define GAS __attribute__((address_space(1)))
#define LAS __attribute__((address_space(3)))

#define PRIO_M 2
#define PRIO_L 0

constexpr int KD = 1024, ND = 1024;
constexpr int NST = 16;
constexpr int RING = 4;
constexpr int SLOT = 32768;
constexpr int LDS_G = RING * SLOT;
constexpr int LDS_TAB = LDS_G + 2048 + 64;
constexpr int LDS_TAG = LDS_TAB + 2048;
constexpr int LDS_TOTAL = LDS_TAG + 16;

constexpr size_t WS_EPR = 0;
constexpr size_t WS_EPC = 32 * 128;
constexpr size_t WS_RS = 8192;
constexpr size_t WS_C = WS_RS + 4096 * 2 * 8;

#define STAMP_DECL
#define STAMP_RT(i)
#define STAMP(i)
#define STAMP_DYN(i)
#define STAMP_FLUSH()

template <int B, int E, class F> __device__ __forceinline__ void static_for(F&& f) {
  if constexpr (B < E) { f(std::integral_constant<int, B>{}); static_for<B + 1, E>(f); }
}
__device__ __forceinline__ float bf_lo(unsigned u) { return __uint_as_float(u << 16); }
__device__ __forceinline__ float bf_hi(unsigned u) { return __uint_as_float(u & 0xffff0000u); }
__device__ __forceinline__ unsigned cvt_pk(float lo, float hi) {
  f32x2 v = {lo, hi}; return __builtin_bit_cast(unsigned, __builtin_convertvector(v, bf16x2_t));
}
template <int CTRL> __device__ __forceinline__ float dpp_add(float x) {
  return x + __builtin_bit_cast(float, __builtin_amdgcn_update_dpp(0, __builtin_bit_cast(int, x), CTRL, 0xf, 0xf, true));
}
__device__ __forceinline__ float sum16(float x) {
  x = dpp_add<0x128>(x); x = dpp_add<0x124>(x); x = dpp_add<0x122>(x); x = dpp_add<0x121>(x);
  return x;
}
#define SCHED() __builtin_amdgcn_sched_barrier(0)
#define BARRIER_L() do { SCHED(); asm volatile("s_waitcnt lgkmcnt(0)\n\ts_barrier" ::: "memory"); SCHED(); } while (0)
#define BARRIER_N() do { SCHED(); asm volatile("s_barrier" ::: "memory"); SCHED(); } while (0)
template <int N> __device__ __forceinline__ void wait_vm() { asm volatile("s_waitcnt vmcnt(%0)" ::"n"(N) : "memory"); }

__device__ __forceinline__ void glds16x4(unsigned v0, unsigned v1, unsigned v2, unsigned v3, const void* sbase, unsigned lds_dst) {
  unsigned keep;
  asm volatile("s_mov_b32 %0, m0\n\ts_mov_b32 m0, %6\n\ts_nop 0\n\t"
               "global_load_lds_dwordx4 %1, %5\n\tglobal_load_lds_dwordx4 %2, %5 offset:1024\n\t"
               "global_load_lds_dwordx4 %3, %5 offset:2048\n\tglobal_load_lds_dwordx4 %4, %5 offset:3072\n\ts_mov_b32 m0, %0"
               : "=&s"(keep) : "v"(v0), "v"(v1), "v"(v2), "v"(v3), "s"(sbase), "s"(lds_dst) : "memory");
}
__device__ __forceinline__ void glds4x2(unsigned v0, const void* sbase, unsigned lds_dst) {
  unsigned keep;
  asm volatile("s_mov_b32 %0, m0\n\ts_mov_b32 m0, %3\n\ts_nop 0\n\t"
               "global_load_lds_dword %1, %2\n\tglobal_load_lds_dword %1, %2 offset:256\n\ts_mov_b32 m0, %0"
               : "=&s"(keep) : "v"(v0), "s"(sbase), "s"(lds_dst) : "memory");
}
template <int OFF> __device__ __forceinline__ u32x4 gload16_asm(unsigned voff, const void* sbase) {
  u32x4 r; asm volatile("global_load_dwordx4 %0, %1, %2 offset:%3" : "=v"(r) : "v"(voff), "s"(sbase), "n"(OFF) : "memory"); return r;
}
__device__ __forceinline__ unsigned gload_u32_sc1_asm(unsigned voff, const void* sbase) {
  unsigned r; asm volatile("global_load_dword %0, %1, %2 sc1" : "=v"(r) : "v"(voff), "s"(sbase) : "memory"); return r;
}
template <int OFF> __device__ __forceinline__ u32x2 gload_u64_sc1_asm(unsigned voff, const void* sbase) {
  u32x2 r; asm volatile("global_load_dwordx2 %0, %1, %2 offset:%3 sc1" : "=v"(r) : "v"(voff), "s"(sbase), "n"(OFF) : "memory"); return r;
}
__device__ __forceinline__ void store_granule(char* ws, size_t off, unsigned tag, float v) {
  __hip_atomic_store((GAS u64*)(ws + off), ((u64)tag << 32) | __float_as_uint(v), __ATOMIC_RELAXED, __HIP_MEMORY_SCOPE_AGENT);
}
__device__ __forceinline__ u64 load_granule(const char* ws, size_t off) {
  return __hip_atomic_load((GAS u64*)(ws + off), __ATOMIC_RELAXED, __HIP_MEMORY_SCOPE_AGENT);
}
__device__ __forceinline__ unsigned load_word(const char* ws, size_t off) {
  return __hip_atomic_load((GAS unsigned*)(ws + off), __ATOMIC_RELAXED, __HIP_MEMORY_SCOPE_AGENT);
}

__global__ __launch_bounds__(512, 3) void lnlin_kernel(const u16* __restrict__ x, const u16* __restrict__ g, const u16* __restrict__ b,
                                                       const u16* __restrict__ w, u16* __restrict__ out, char* ws) {
  u64 code_pc; unsigned code_len;
  asm volatile("s_getpc_b64 %0\nlnlin_code_begin:\n\ts_mov_b32 %1, lnlin_code_end-lnlin_code_begin" : "=s"(code_pc), "=s"(code_len));
  extern __shared__ __attribute__((aligned(16))) char smem_g[];
  const int bid = blockIdx.x, xcd = bid & 7, idx = bid >> 3;
  const int mi = xcd * 4 + (idx >> 3), ni = idx & 7;
  const int tid = threadIdx.x, lane = tid & 63;
  const int wave = __builtin_amdgcn_readfirstlane(tid >> 6);
  const unsigned lds0 = (unsigned)(size_t)(LAS char*)smem_g;
  STAMP_DECL STAMP_RT(0); STAMP(1);

  if (wave >= 4) {
    const int d = wave - 4, t = tid - 256;
    if (PRIO_L) __builtin_amdgcn_s_setprio(PRIO_L);
    unsigned xo[4], wo[4];
#pragma unroll
    for (int i = 0; i < 4; ++i) {
      const int row = i * 8 + (lane >> 3);
      xo[i] = (unsigned)row * 2048u + (unsigned)(((lane & 7) ^ ((row >> 1) & 7)) * 16) - (unsigned)i * 1024u;
      const int kr = i * 4 + (lane >> 4);
      wo[i] = (unsigned)kr * 2048u + (unsigned)(((lane & 15) ^ (((lane >> 4) << 2) | i)) * 16) - (unsigned)i * 1024u;
    }
    const unsigned dst = lds0 + (unsigned)d * 4096u;
    const char* xb = (const char*)x + (size_t)mi * 262144 + d * 65536;
    const char* wb = (const char*)w + d * 32768 + ni * 256;
    auto issue = [&](int st) {
      const unsigned ds_ = dst + (unsigned)(st & (RING - 1)) * SLOT;
      glds16x4(wo[0], wo[1], wo[2], wo[3], wb + (size_t)st * 131072, ds_ + 16384u);
      glds16x4(xo[0], xo[1], xo[2], xo[3], xb + st * 128, ds_);
    };
    glds4x2((unsigned)lane * 4u, (const char*)g + d * 512, lds0 + LDS_G + (unsigned)d * 512u);
    issue(0); issue(1);
    STAMP(2);
    wait_vm<8>();
    STAMP(3);
    BARRIER_N();
    constexpr int GP = 11;
#pragma unroll 1
    for (int j = 0; j < GP; ++j) {
      issue(j + 2);
      wait_vm<8>();
      BARRIER_N();
      STAMP_DYN(j + 4);
    }
    u32x2 gr[9];
    issue(13);
    gr[0] = gload_u64_sc1_asm<0>((unsigned)t * 8u, ws + WS_RS + (size_t)mi * 2048);
    static_for<0, 8>([&](auto J) { constexpr int j = J;
      gr[1 + j] = gload_u64_sc1_asm<(j & 1) * 2048>((unsigned)t * 8u + (j >> 1) * 4096u, ws + WS_C + (size_t)ni * 16384); });
    wait_vm<17>(); BARRIER_N();
    STAMP(15);
    issue(14);
    wait_vm<17>(); BARRIER_N();
    STAMP(16);
    issue(15);
    asm volatile("s_waitcnt vmcnt(8)"
                 : "+v"(gr[0]), "+v"(gr[1]), "+v"(gr[2]), "+v"(gr[3]), "+v"(gr[4]), "+v"(gr[5]), "+v"(gr[6]), "+v"(gr[7]), "+v"(gr[8]) :: "memory");
    BARRIER_N();
    STAMP(17);
    const u32x2 tags = *(const LAS u32x2*)(lds0 + LDS_TAG);
    const unsigned tagr = tags[0], tagc = tags[1];
    bool ok = gr[0][1] == tagr;
#pragma unroll
    for (int j = 0; j < 8; ++j) ok &= gr[1 + j][1] == tagc;
    float vr = __uint_as_float(gr[0][0]), vc = 0.f;
#pragma unroll
    for (int j = 0; j < 8; ++j) vc += __uint_as_float(gr[1 + j][0]);
    if (!__all(ok)) {
#pragma unroll 1
      for (int it = 0; it < 20000; ++it) {
        u64 q[9];
        q[0] = load_granule(ws, WS_RS + (size_t)mi * 2048 + (size_t)t * 8);
#pragma unroll
        for (int j = 0; j < 8; ++j) q[1 + j] = load_granule(ws, WS_C + (size_t)ni * 16384 + (size_t)j * 2048 + (size_t)t * 8);
        ok = (unsigned)(q[0] >> 32) == tagr; vr = __uint_as_float((unsigned)q[0]); vc = 0.f;
#pragma unroll
        for (int j = 0; j < 8; ++j) { ok &= (unsigned)(q[1 + j] >> 32) == tagc; vc += __uint_as_float((unsigned)q[1 + j]); }
        if (__all(ok)) break;
        __builtin_amdgcn_s_sleep(4);
      }
      if (!__all(ok)) {
        const u16* xrow = x + (size_t)(mi * 128 + (t >> 1)) * KD;
        float sm = 0.f, sq = 0.f;
#pragma unroll 1
        for (int k = 0; k < KD; ++k) sm += bf_lo(xrow[k]);
        sm *= (1.f / 1024.f);
#pragma unroll 1
        for (int k = 0; k < KD; ++k) { const float dd = bf_lo(xrow[k]) - sm; sq = fmaf(dd, dd, sq); }
        vr = (t & 1) ? __builtin_amdgcn_rsqf(sq * (1.f / 1024.f) + 1e-5f) : sm;
        const u16* wc = w + ni * 128 + (t >> 1);
        const u16* gb = (t & 1) ? b : g;
        vc = 0.f;
#pragma unroll 1
        for (int k = 0; k < KD; ++k) {
          const float pr = bf_lo(gb[k]) * bf_lo(wc[(size_t)k * ND]);
          vc += (t & 1) ? pr : bf_lo(cvt_pk(pr, 0.f));
        }
      }
    }
    *(LAS float*)(lds0 + LDS_TAB + t * 4) = vr;
    *(LAS float*)(lds0 + LDS_TAB + 1024 + (t & 1) * 512 + (t >> 1) * 4) = vc;
    STAMP(18);
    wait_vm<0>();
    BARRIER_L();
    STAMP(19);
    if (t == 0) {
      __hip_atomic_fetch_add((GAS unsigned*)(ws + WS_EPR + mi * 128), 1u, __ATOMIC_RELAXED, __HIP_MEMORY_SCOPE_AGENT);
      __hip_atomic_fetch_add((GAS unsigned*)(ws + WS_EPC + ni * 128), 1u, __ATOMIC_RELAXED, __HIP_MEMORY_SCOPE_AGENT);
    }
  } else {
    const int wn = wave;
    {
      const int srow = mi * 128 + ni * 16 + wn * 4 + (lane >> 4);
      const char* xs = (const char*)x + (size_t)srow * 2048 + (lane & 15) * 16;
      u32x4 xr[8];
#pragma unroll
      for (int j = 0; j < 8; ++j) xr[j] = *(const GAS u32x4*)(xs + j * 256);
      const int sk = (mi >> 2) * 128 + lane, scol = (mi & 3) * 32 + wn * 8;
      const char* wsl = (const char*)w + (size_t)sk * 2048 + (ni * 128 + scol) * 2;
      const u32x4 wv0 = *(const GAS u32x4*)wsl, wv1 = *(const GAS u32x4*)(wsl + 64 * 2048);
      const unsigned gv0 = g[sk], gv1 = g[sk + 64], bv0 = b[sk], bv1 = b[sk + 64];
      const unsigned epr = load_word(ws, WS_EPR + mi * 128), epc = load_word(ws, WS_EPC + ni * 128);
      SCHED();
      float s0 = 0.f, s1 = 0.f, s2 = 0.f, s3 = 0.f;
#pragma unroll
      for (int j = 0; j < 8; ++j) {
        s0 += bf_lo(xr[j][0]); s1 += bf_hi(xr[j][0]); s2 += bf_lo(xr[j][1]); s3 += bf_hi(xr[j][1]);
        s0 += bf_lo(xr[j][2]); s1 += bf_hi(xr[j][2]); s2 += bf_lo(xr[j][3]); s3 += bf_hi(xr[j][3]);
      }
      const float mean = sum16((s0 + s1) + (s2 + s3)) * (1.f / 1024.f);
      float q0 = 0.f, q1 = 0.f, q2 = 0.f, q3 = 0.f;
#pragma unroll
      for (int j = 0; j < 8; ++j)
#pragma unroll
        for (int i = 0; i < 4; i += 2) {
          const float d0 = bf_lo(xr[j][i]) - mean, d1 = bf_hi(xr[j][i]) - mean, d2 = bf_lo(xr[j][i + 1]) - mean, d3 = bf_hi(xr[j][i + 1]) - mean;
          q0 = fmaf(d0, d0, q0); q1 = fmaf(d1, d1, q1); q2 = fmaf(d2, d2, q2); q3 = fmaf(d3, d3, q3);
        }
      const float var = sum16((q0 + q1) + (q2 + q3)) * (1.f / 1024.f);
      const float rstd = __builtin_amdgcn_rsqf(var + 1e-5f);
      STAMP(2);
      float p1[8], p2[8];
      const float ga = bf_lo(gv0), gb = bf_lo(gv1), ba = bf_lo(bv0), bb = bf_lo(bv1);
#pragma unroll
      for (int i = 0; i < 4; ++i) {
        const float l0 = bf_lo(wv0[i]), h0 = bf_hi(wv0[i]), l1 = bf_lo(wv1[i]), h1 = bf_hi(wv1[i]);
        const unsigned pa = cvt_pk(ga * l0, ga * h0), pb = cvt_pk(gb * l1, gb * h1);
        p1[2 * i] = bf_lo(pa) + bf_lo(pb); p1[2 * i + 1] = bf_hi(pa) + bf_hi(pb);
        p2[2 * i] = fmaf(ba, l0, bb * l1); p2[2 * i + 1] = fmaf(ba, h0, bb * h1);
      }
      float a[8];
#pragma unroll
      for (int i = 0; i < 8; ++i) {
        auto rr = __builtin_amdgcn_permlane32_swap(__float_as_uint(p1[i]), __float_as_uint(p2[i]), false, false);
        a[i] = __uint_as_float(rr[0]) + __uint_as_float(rr[1]);
      }
      float cs[4];
#pragma unroll
      for (int i = 0; i < 4; ++i) {
        auto rr = __builtin_amdgcn_permlane16_swap(__float_as_uint(a[i]), __float_as_uint(a[4 + i]), false, false);
        cs[i] = sum16(__uint_as_float(rr[0]) + __uint_as_float(rr[1]));
      }
      const int l4 = lane & 15;
      const float cv = l4 == 0 ? cs[0] : (l4 == 1 ? cs[1] : (l4 == 2 ? cs[2] : cs[3]));
      if (l4 < 2) store_granule(ws, WS_RS + ((size_t)srow * 2 + (lane & 1)) * 8, epr + 1u, (lane & 1) ? rstd : mean);
      if (l4 < 4) store_granule(ws, WS_C + ((size_t)((ni * 8 + (mi >> 2)) * 128 + scol + ((lane >> 4) & 1) * 4 + l4) * 2 + (lane >> 5)) * 8, epc + 1u, cv);
      if (tid == 0) *(LAS u32x2*)(lds0 + LDS_TAG) = (u32x2){epr + 1u, epc + 1u};
    }
    STAMP(3);
    if (PRIO_M) __builtin_amdgcn_s_setprio(PRIO_M);
    const int r = lane & 31, h = lane >> 5, sw = (r >> 1) & 7;
    unsigned offx[4], offw[2];
#pragma unroll
    for (int ks = 0; ks < 4; ++ks) offx[ks] = lds0 + (unsigned)r * 128u + (unsigned)(((2 * ks + h) ^ sw) * 16);
    {
      const int blk = (lane >> 4) & 1, q = (lane & 15) >> 2, p = lane & 3;
#pragma unroll
      for (int tt = 0; tt < 2; ++tt) {
        const int row = 8 * h + 4 * tt + q, ch = 4 * wn + blk + 2 * (p & 1);
        offw[tt] = lds0 + 16384u + (unsigned)(256 * row + 16 * (ch ^ ((q << 2) | ((2 * h + tt) & 3))) + 8 * (p >> 1));
      }
    }
    unsigned offg = lds0 + LDS_G + (unsigned)h * 16u;
    const unsigned li = lane & 3;
    const unsigned mk0 = li == 0 ? 0xffffu : (li == 1 ? 0xffff0000u : 0u), mk1 = li == 2 ? 0xffffu : (li == 3 ? 0xffff0000u : 0u);
    f32x16 acc[4] = {};
    bf16x8 xf[4];
    s16x4 wra[2], wrb[2];
    u32x4 gq, dg;
    u32x4 wf, wfn;
    f32x4 d0, d1;
    const f32x4 z4 = {0.f, 0.f, 0.f, 0.f};
#define RX(mt, ks) xf[mt] = *(const LAS bf16x8*)(offx[ks] + (mt) * 4096)
#define RW(R, tt, ks) R[tt] = __builtin_amdgcn_ds_read_tr16_b64_v4i16((LAS s16x4*)(offw[tt] + (ks) * 4096))
#define RG(o) gq = *(const LAS u32x4*)(offg + (o) * 32)
#define DG0() do { dg[0] = gq[0] & mk[0]; dg[1] = gq[1] & mk[1]; } while (0)
#define DG1() do { dg[2] = gq[2] & mk[0]; dg[3] = gq[3] & mk[1]; } while (0)
#define SM0(R) d0 = __builtin_amdgcn_mfma_f32_4x4x4bf16_1k(__builtin_bit_cast(s16x4, (u32x2){dg[0], dg[1]}), R[0], z4, 0, 0, 0)
#define SM1(R) d1 = __builtin_amdgcn_mfma_f32_4x4x4bf16_1k(__builtin_bit_cast(s16x4, (u32x2){dg[2], dg[3]}), R[1], z4, 0, 0, 0)
#define CV0(W) do { W[0] = cvt_pk(d0[0], d0[1]); W[1] = cvt_pk(d0[2], d0[3]); } while (0)
#define CV1(W) do { W[2] = cvt_pk(d1[0], d1[1]); W[3] = cvt_pk(d1[2], d1[3]); } while (0)
#define MM(mt, W) acc[mt] = __builtin_amdgcn_mfma_f32_32x32x16_bf16(__builtin_bit_cast(bf16x8, W), xf[mt], acc[mt], 0, 0, 0)
#define STEP(WC, WN, RA, RB, kx, kw, kg, EXTRA2, EXTRA3) do { \
      SM0(RA); SM1(RA); SCHED(); \
      MM(0, WC); SCHED(); RX(0, kx); RW(RB, 0, kw); DG0(); SCHED(); \
      MM(1, WC); SCHED(); RX(1, kx); RW(RB, 1, kw); DG1(); SCHED(); \
      MM(2, WC); SCHED(); RX(2, kx); RG(kg); CV0(WN); EXTRA2; SCHED(); \
      MM(3, WC); SCHED(); RX(3, kx); CV1(WN); EXTRA3; SCHED(); } while (0)
    unsigned mk[2] = {mk0, mk1};
    asm volatile("" : "+v"(acc[0]), "+v"(acc[1]), "+v"(acc[2]), "+v"(acc[3]));
    asm volatile("" : "+v"(offx[0]), "+v"(offx[1]), "+v"(offx[2]), "+v"(offx[3]), "+v"(offw[0]), "+v"(offw[1]), "+v"(offg), "+v"(mk[0]), "+v"(mk[1]));
    __builtin_amdgcn_s_waitcnt(0xC07F);
    STAMP(4);
    BARRIER_N();
    STAMP(5);
    RG(0); RW(wrb, 0, 0); RW(wrb, 1, 0); RX(0, 0); RW(wra, 0, 1); RW(wra, 1, 1); RX(1, 0); RX(2, 0); RX(3, 0);
    DG0(); DG1(); SCHED(); RG(1); SM0(wrb); SM1(wrb); SCHED();
    CV0(wf); CV1(wf); DG0(); DG1(); SCHED(); RG(2); SCHED();
#pragma unroll 1
    for (int s = 0; s < NST - 1; ++s) {
      const int dlt = ((s & (RING - 1)) == RING - 1) ? -(RING - 1) * SLOT : SLOT;
      STEP(wf, wfn, wra, wrb, 1, 2, 3, (void)0, (void)0);
      STEP(wfn, wf, wrb, wra, 2, 3, 4, (offx[0] += dlt, offw[0] += dlt), (offw[1] += dlt, offx[1] += dlt));
      BARRIER_N();
      STEP(wf, wfn, wra, wrb, 3, 0, 5, (offx[2] += dlt), (void)0);
      STEP(wfn, wf, wrb, wra, 0, 1, 6, (offx[3] += dlt), (offg += 128u));
    }
    f32x4 c1v[4], c2v[4];
    f32x2 rs[4];
    const unsigned tc = lds0 + LDS_TAB + 1024 + (unsigned)(wn * 32 + h * 16) * 4u, tr = lds0 + LDS_TAB + (unsigned)r * 8u;
#define RC(q) do { c1v[q] = *(const LAS f32x4*)(tc + (q) * 16); c2v[q] = *(const LAS f32x4*)(tc + 512 + (q) * 16); rs[q] = *(const LAS f32x2*)(tr + (q) * 256); } while (0)
    STEP(wf, wfn, wra, wrb, 1, 2, 3, (void)0, (void)0);
    STEP(wfn, wf, wrb, wra, 2, 3, 4, (void)0, (void)0);
    SM0(wra); SM1(wra); SCHED();
    MM(0, wf); SCHED(); RX(0, 3); SCHED();
    MM(1, wf); SCHED(); RX(1, 3); SCHED();
    MM(2, wf); SCHED(); RX(2, 3); CV0(wfn); SCHED();
    MM(3, wf); SCHED(); RX(3, 3); CV1(wfn); SCHED();
    MM(0, wfn); SCHED(); RC(0); SCHED(); MM(1, wfn); SCHED(); RC(1); SCHED(); MM(2, wfn); SCHED(); RC(2); SCHED(); MM(3, wfn); SCHED(); RC(3); SCHED();
    STAMP(20);
    const unsigned so = lds0 + (unsigned)r * 256u, sc0 = (unsigned)(((wn * 4 + h * 2) ^ (r & 7)) * 16), sc1 = sc0 ^ 16u;
#pragma unroll
    for (int mt = 0; mt < 4; ++mt) {
      const float nm = -rs[mt][0], rstd = rs[mt][1];
      f32x4 tq[4];
#pragma unroll
      for (int q = 0; q < 4; ++q) {
        const f32x4 av = {acc[mt][4 * q], acc[mt][4 * q + 1], acc[mt][4 * q + 2], acc[mt][4 * q + 3]};
        tq[q] = av + c1v[q] * nm;
      }
#pragma unroll
      for (int q = 0; q < 4; ++q) tq[q] = tq[q] * rstd + c2v[q];
      const u32x4 o0 = {cvt_pk(tq[0][0], tq[0][1]), cvt_pk(tq[0][2], tq[0][3]), cvt_pk(tq[1][0], tq[1][1]), cvt_pk(tq[1][2], tq[1][3])};
      const u32x4 o1 = {cvt_pk(tq[2][0], tq[2][1]), cvt_pk(tq[2][2], tq[2][3]), cvt_pk(tq[3][0], tq[3][1]), cvt_pk(tq[3][2], tq[3][3])};
      *(LAS u32x4*)(so + mt * 8192 + sc0) = o0;
      *(LAS u32x4*)(so + mt * 8192 + sc1) = o1;
      SCHED();
    }
  }
  STAMP(24);
  BARRIER_L();
  STAMP(25);
#pragma unroll
  for (int i = 0; i < 4; ++i) {
    const int row = (wave * 4 + i) * 4 + (lane >> 4), ch = lane & 15;
    const u32x4 v = *(const LAS u32x4*)(lds0 + row * 256 + ((ch ^ (row & 7)) * 16));
    *(u32x4*)(out + (size_t)(mi * 128 + row) * ND + ni * 128 + ch * 8) = v;
  }
  STAMP_FLUSH();
  asm volatile("lnlin_code_end:");
}

extern "C" void kernel_launch(void* const* d_in, const int* in_sizes, int n_in, void* d_out, int out_size, void* d_ws, size_t ws_size,
                              hipStream_t stream) {
  const u16* x = (const u16*)d_in[0]; const u16* g = (const u16*)d_in[1]; const u16* b = (const u16*)d_in[2]; const u16* w = (const u16*)d_in[3];
  (void)hipFuncSetAttribute((const void*)lnlin_kernel, hipFuncAttributeMaxDynamicSharedMemorySize, LDS_TOTAL);
  lnlin_kernel<<<256, 512, LDS_TOTAL, stream>>>(x, g, b, w, (u16*)d_out, (char*)d_ws);
}
```

```cpp
#define EXP_DELAY 800
#define EXP_TOUCH
#include <hip/hip_runtime.h>
#include <hip/hip_bf16.h>
#include <utility>
#pragma clang diagnostic ignored "-Wint-to-pointer-cast"

typedef short bf16x8 __attribute__((ext_vector_type(8)));
typedef short s16x4 __attribute__((ext_vector_type(4)));
typedef float f32x16 __attribute__((ext_vector_type(16)));
typedef float f32x4 __attribute__((ext_vector_type(4)));
typedef float f32x2 __attribute__((ext_vector_type(2)));
typedef unsigned u32x4 __attribute__((ext_vector_type(4)));
typedef unsigned u32x2 __attribute__((ext_vector_type(2)));
typedef __bf16 bf16x2_t __attribute__((ext_vector_type(2)));
typedef unsigned short u16;
typedef unsigned long long u64;
#define GAS __attribute__((address_space(1)))
#define LAS __attribute__((address_space(3)))

#define PRIO_M 2
#define PRIO_L 0

constexpr int KD = 1024, ND = 1024;
constexpr int NST = 16;
constexpr int RING = 4;
constexpr int SLOT = 32768;
constexpr int LDS_G = RING * SLOT;
constexpr int LDS_TAB = LDS_G + 2048 + 64;
constexpr int LDS_TAG = LDS_TAB + 2048;
constexpr int LDS_TOTAL = LDS_TAG + 16;

constexpr size_t WS_EPR = 0;
constexpr size_t WS_EPC = 32 * 128;
constexpr size_t WS_RS = 8192;
constexpr size_t WS_C = WS_RS + 4096 * 2 * 8;

#define STAMP_DECL
#define STAMP_RT(i)
#define STAMP(i)
#define STAMP_DYN(i)
#define STAMP_FLUSH()

template <int B, int E, class F> __device__ __forceinline__ void static_for(F&& f) {
  if constexpr (B < E) { f(std::integral_constant<int, B>{}); static_for<B + 1, E>(f); }
}
__device__ __forceinline__ float bf_lo(unsigned u) { return __uint_as_float(u << 16); }
__device__ __forceinline__ float bf_hi(unsigned u) { return __uint_as_float(u & 0xffff0000u); }
__device__ __forceinline__ unsigned cvt_pk(float lo, float hi) {
  f32x2 v = {lo, hi}; return __builtin_bit_cast(unsigned, __builtin_convertvector(v, bf16x2_t));
}
template <int CTRL> __device__ __forceinline__ float dpp_add(float x) {
  return x + __builtin_bit_cast(float, __builtin_amdgcn_update_dpp(0, __builtin_bit_cast(int, x), CTRL, 0xf, 0xf, true));
}
__device__ __forceinline__ float sum16(float x) {
  x = dpp_add<0x128>(x); x = dpp_add<0x124>(x); x = dpp_add<0x122>(x); x = dpp_add<0x121>(x);
  return x;
}
#define SCHED() __builtin_amdgcn_sched_barrier(0)
#define BARRIER_L() do { SCHED(); asm volatile("s_waitcnt lgkmcnt(0)\n\ts_barrier" ::: "memory"); SCHED(); } while (0)
#define BARRIER_N() do { SCHED(); asm volatile("s_barrier" ::: "memory"); SCHED(); } while (0)
template <int N> __device__ __forceinline__ void wait_vm() { asm volatile("s_waitcnt vmcnt(%0)" ::"n"(N) : "memory"); }

__device__ __forceinline__ void glds16x4(unsigned v0, unsigned v1, unsigned v2, unsigned v3, const void* sbase, unsigned lds_dst) {
  unsigned keep;
  asm volatile("s_mov_b32 %0, m0\n\ts_mov_b32 m0, %6\n\ts_nop 0\n\t"
               "global_load_lds_dwordx4 %1, %5\n\tglobal_load_lds_dwordx4 %2, %5 offset:1024\n\t"
               "global_load_lds_dwordx4 %3, %5 offset:2048\n\tglobal_load_lds_dwordx4 %4, %5 offset:3072\n\ts_mov_b32 m0, %0"
               : "=&s"(keep) : "v"(v0), "v"(v1), "v"(v2), "v"(v3), "s"(sbase), "s"(lds_dst) : "memory");
}
__device__ __forceinline__ void glds4x2(unsigned v0, const void* sbase, unsigned lds_dst) {
  unsigned keep;
  asm volatile("s_mov_b32 %0, m0\n\ts_mov_b32 m0, %3\n\ts_nop 0\n\t"
               "global_load_lds_dword %1, %2\n\tglobal_load_lds_dword %1, %2 offset:256\n\ts_mov_b32 m0, %0"
               : "=&s"(keep) : "v"(v0), "s"(sbase), "s"(lds_dst) : "memory");
}
template <int OFF> __device__ __forceinline__ u32x4 gload16_asm(unsigned voff, const void* sbase) {
  u32x4 r; asm volatile("global_load_dwordx4 %0, %1, %2 offset:%3" : "=v"(r) : "v"(voff), "s"(sbase), "n"(OFF) : "memory"); return r;
}
__device__ __forceinline__ unsigned gload_u32_sc1_asm(unsigned voff, const void* sbase) {
  unsigned r; asm volatile("global_load_dword %0, %1, %2 sc1" : "=v"(r) : "v"(voff), "s"(sbase) : "memory"); return r;
}
template <int OFF> __device__ __forceinline__ u32x2 gload_u64_sc1_asm(unsigned voff, const void* sbase) {
  u32x2 r; asm volatile("global_load_dwordx2 %0, %1, %2 offset:%3 sc1" : "=v"(r) : "v"(voff), "s"(sbase), "n"(OFF) : "memory"); return r;
}
__device__ __forceinline__ void store_granule(char* ws, size_t off, unsigned tag, float v) {
  __hip_atomic_store((GAS u64*)(ws + off), ((u64)tag << 32) | __float_as_uint(v), __ATOMIC_RELAXED, __HIP_MEMORY_SCOPE_AGENT);
}
__device__ __forceinline__ u64 load_granule(const char* ws, size_t off) {
  return __hip_atomic_load((GAS u64*)(ws + off), __ATOMIC_RELAXED, __HIP_MEMORY_SCOPE_AGENT);
}
__device__ __forceinline__ unsigned load_word(const char* ws, size_t off) {
  return __hip_atomic_load((GAS unsigned*)(ws + off), __ATOMIC_RELAXED, __HIP_MEMORY_SCOPE_AGENT);
}

__global__ __launch_bounds__(512, 3) void lnlin_kernel(const u16* __restrict__ x, const u16* __restrict__ g, const u16* __restrict__ b,
                                                       const u16* __restrict__ w, u16* __restrict__ out, char* ws) {
  u64 code_pc; unsigned code_len;
  asm volatile("s_getpc_b64 %0\nlnlin_code_begin:\n\ts_mov_b32 %1, lnlin_code_end-lnlin_code_begin" : "=s"(code_pc), "=s"(code_len));
  extern __shared__ __attribute__((aligned(16))) char smem_g[];
  const int bid = blockIdx.x, xcd = bid & 7, idx = bid >> 3;
  const int mi = xcd * 4 + (idx >> 3), ni = idx & 7;
  const int tid = threadIdx.x, lane = tid & 63;
  const int wave = __builtin_amdgcn_readfirstlane(tid >> 6);
  const unsigned lds0 = (unsigned)(size_t)(LAS char*)smem_g;
  STAMP_DECL STAMP_RT(0); STAMP(1);
  { unsigned tq; const char* p = (lane & 3) == 0 ? (const char*)x + (size_t)mi * 262144 + (size_t)tid * 512 : ((lane & 3) == 1 ? (const char*)w + (size_t)tid * 4096 + ni * 256 : ((lane & 3) == 2 ? (const char*)out + (size_t)mi * 262144 + (size_t)tid * 512 : ws + (size_t)tid * 256));
    asm volatile("global_load_dword %0, %1, off" : "=v"(tq) : "v"(p) : "memory"); }
  { const u64 ts = __builtin_amdgcn_s_memrealtime(); while (__builtin_amdgcn_s_memrealtime() - ts < EXP_DELAY) __builtin_amdgcn_s_sleep(2); }
  asm volatile("s_waitcnt vmcnt(0)" ::: "memory");

  if (wave >= 4) {
    const int d = wave - 4, t = tid - 256;
    const unsigned co = (unsigned)d * 3072u + (unsigned)lane * 16u, lim = (code_len & ~15u) - 16u;
    u32x4 t0 = gload16_asm<0>(min(co, lim), (const void*)code_pc), t1 = gload16_asm<0>(min(co + 1024u, lim), (const void*)code_pc),
          t2 = gload16_asm<0>(min(co + 2048u, lim), (const void*)code_pc);
    if (PRIO_L) __builtin_amdgcn_s_setprio(PRIO_L);
    unsigned xo[4], wo[4];
#pragma unroll
    for (int i = 0; i < 4; ++i) {
      const int row = i * 8 + (lane >> 3);
      xo[i] = (unsigned)row * 2048u + (unsigned)(((lane & 7) ^ ((row >> 1) & 7)) * 16) - (unsigned)i * 1024u;
      const int kr = i * 4 + (lane >> 4);
      wo[i] = (unsigned)kr * 2048u + (unsigned)(((lane & 15) ^ (((lane >> 4) << 2) | i)) * 16) - (unsigned)i * 1024u;
    }
    const unsigned dst = lds0 + (unsigned)d * 4096u;
    const char* xb = (const char*)x + (size_t)mi * 262144 + d * 65536;
    const char* wb = (const char*)w + d * 32768 + ni * 256;
    auto issue = [&](int st) {
      const unsigned ds_ = dst + (unsigned)(st & (RING - 1)) * SLOT;
      glds16x4(wo[0], wo[1], wo[2], wo[3], wb + (size_t)st * 131072, ds_ + 16384u);
      glds16x4(xo[0], xo[1], xo[2], xo[3], xb + st * 128, ds_);
    };
    glds4x2((unsigned)lane * 4u, (const char*)g + d * 512, lds0 + LDS_G + (unsigned)d * 512u);
    issue(0); issue(1);
    STAMP(2);
    asm volatile("s_waitcnt vmcnt(8)" : "+v"(t0), "+v"(t1), "+v"(t2)::"memory");
    STAMP(3);
    BARRIER_N();
    constexpr int GP = 11;
#pragma unroll 1
    for (int j = 0; j < GP; ++j) {
      issue(j + 2);
      wait_vm<8>();
      BARRIER_N();
      STAMP_DYN(j + 4);
    }
    u32x2 gr[9];
    issue(13);
    gr[0] = gload_u64_sc1_asm<0>((unsigned)t * 8u, ws + WS_RS + (size_t)mi * 2048);
    static_for<0, 8>([&](auto J) { constexpr int j = J;
      gr[1 + j] = gload_u64_sc1_asm<(j & 1) * 2048>((unsigned)t * 8u + (j >> 1) * 4096u, ws + WS_C + (size_t)ni * 16384); });
    wait_vm<17>(); BARRIER_N();
    STAMP(15);
    issue(14);
    wait_vm<17>(); BARRIER_N();
    STAMP(16);
    issue(15);
    asm volatile("s_waitcnt vmcnt(8)"
                 : "+v"(gr[0]), "+v"(gr[1]), "+v"(gr[2]), "+v"(gr[3]), "+v"(gr[4]), "+v"(gr[5]), "+v"(gr[6]), "+v"(gr[7]), "+v"(gr[8]) :: "memory");
    BARRIER_N();
    STAMP(17);
    const u32x2 tags = *(const LAS u32x2*)(lds0 + LDS_TAG);
    const unsigned tagr = tags[0], tagc = tags[1];
    bool ok = gr[0][1] == tagr;
#pragma unroll
    for (int j = 0; j < 8; ++j) ok &= gr[1 + j][1] == tagc;
    float vr = __uint_as_float(gr[0][0]), vc = 0.f;
#pragma unroll
    for (int j = 0; j < 8; ++j) vc += __uint_as_float(gr[1 + j][0]);
    if (!__all(ok)) {
#pragma unroll 1
      for (int it = 0; it < 20000; ++it) {
        u64 q[9];
        q[0] = load_granule(ws, WS_RS + (size_t)mi * 2048 + (size_t)t * 8);
#pragma unroll
        for (int j = 0; j < 8; ++j) q[1 + j] = load_granule(ws, WS_C + (size_t)ni * 16384 + (size_t)j * 2048 + (size_t)t * 8);
        ok = (unsigned)(q[0] >> 32) == tagr; vr = __uint_as_float((unsigned)q[0]); vc = 0.f;
#pragma unroll
        for (int j = 0; j < 8; ++j) { ok &= (unsigned)(q[1 + j] >> 32) == tagc; vc += __uint_as_float((unsigned)q[1 + j]); }
        if (__all(ok)) break;
        __builtin_amdgcn_s_sleep(4);
      }
      if (!__all(ok)) {
        const u16* xrow = x + (size_t)(mi * 128 + (t >> 1)) * KD;
        float sm = 0.f, sq = 0.f;
#pragma unroll 1
        for (int k = 0; k < KD; ++k) sm += bf_lo(xrow[k]);
        sm *= (1.f / 1024.f);
#pragma unroll 1
        for (int k = 0; k < KD; ++k) { const float dd = bf_lo(xrow[k]) - sm; sq = fmaf(dd, dd, sq); }
        vr = (t & 1) ? __builtin_amdgcn_rsqf(sq * (1.f / 1024.f) + 1e-5f) : sm;
        const u16* wc = w + ni * 128 + (t >> 1);
        const u16* gb = (t & 1) ? b : g;
        vc = 0.f;
#pragma unroll 1
        for (int k = 0; k < KD; ++k) {
          const float pr = bf_lo(gb[k]) * bf_lo(wc[(size_t)k * ND]);
          vc += (t & 1) ? pr : bf_lo(cvt_pk(pr, 0.f));
        }
      }
    }
    *(LAS float*)(lds0 + LDS_TAB + t * 4) = vr;
    *(LAS float*)(lds0 + LDS_TAB + 1024 + (t & 1) * 512 + (t >> 1) * 4) = vc;
    STAMP(18);
    wait_vm<0>();
    BARRIER_L();
    STAMP(19);
    if (t == 0) {
      __hip_atomic_fetch_add((GAS unsigned*)(ws + WS_EPR + mi * 128), 1u, __ATOMIC_RELAXED, __HIP_MEMORY_SCOPE_AGENT);
      __hip_atomic_fetch_add((GAS unsigned*)(ws + WS_EPC + ni * 128), 1u, __ATOMIC_RELAXED, __HIP_MEMORY_SCOPE_AGENT);
    }
  } else {
    const int wn = wave;
    {
      const int srow = mi * 128 + ni * 16 + wn * 4 + (lane >> 4);
      const char* xs = (const char*)x + (size_t)srow * 2048 + (lane & 15) * 16;
      u32x4 xr[8];
#pragma unroll
      for (int j = 0; j < 8; ++j) xr[j] = *(const GAS u32x4*)(xs + j * 256);
      const int sk = (mi >> 2) * 128 + lane, scol = (mi & 3) * 32 + wn * 8;
      const char* wsl = (const char*)w + (size_t)sk * 2048 + (ni * 128 + scol) * 2;
      const u32x4 wv0 = *(const GAS u32x4*)wsl, wv1 = *(const GAS u32x4*)(wsl + 64 * 2048);
      const unsigned gv0 = g[sk], gv1 = g[sk + 64], bv0 = b[sk], bv1 = b[sk + 64];
      const unsigned epr = load_word(ws, WS_EPR + mi * 128), epc = load_word(ws, WS_EPC + ni * 128);
      SCHED();
      float s0 = 0.f, s1 = 0.f, s2 = 0.f, s3 = 0.f;
#pragma unroll
      for (int j = 0; j < 8; ++j) {
        s0 += bf_lo(xr[j][0]); s1 += bf_hi(xr[j][0]); s2 += bf_lo(xr[j][1]); s3 += bf_hi(xr[j][1]);
        s0 += bf_lo(xr[j][2]); s1 += bf_hi(xr[j][2]); s2 += bf_lo(xr[j][3]); s3 += bf_hi(xr[j][3]);
      }
      const float mean = sum16((s0 + s1) + (s2 + s3)) * (1.f / 1024.f);
      float q0 = 0.f, q1 = 0.f, q2 = 0.f, q3 = 0.f;
#pragma unroll
      for (int j = 0; j < 8; ++j)
#pragma unroll
        for (int i = 0; i < 4; i += 2) {
          const float d0 = bf_lo(xr[j][i]) - mean, d1 = bf_hi(xr[j][i]) - mean, d2 = bf_lo(xr[j][i + 1]) - mean, d3 = bf_hi(xr[j][i + 1]) - mean;
          q0 = fmaf(d0, d0, q0); q1 = fmaf(d1, d1, q1); q2 = fmaf(d2, d2, q2); q3 = fmaf(d3, d3, q3);
        }
      const float var = sum16((q0 + q1) + (q2 + q3)) * (1.f / 1024.f);
      const float rstd = __builtin_amdgcn_rsqf(var + 1e-5f);
      STAMP(2);
      float p1[8], p2[8];
      const float ga = bf_lo(gv0), gb = bf_lo(gv1), ba = bf_lo(bv0), bb = bf_lo(bv1);
#pragma unroll
      for (int i = 0; i < 4; ++i) {
        const float l0 = bf_lo(wv0[i]), h0 = bf_hi(wv0[i]), l1 = bf_lo(wv1[i]), h1 = bf_hi(wv1[i]);
        const unsigned pa = cvt_pk(ga * l0, ga * h0), pb = cvt_pk(gb * l1, gb * h1);
        p1[2 * i] = bf_lo(pa) + bf_lo(pb); p1[2 * i + 1] = bf_hi(pa) + bf_hi(pb);
        p2[2 * i] = fmaf(ba, l0, bb * l1); p2[2 * i + 1] = fmaf(ba, h0, bb * h1);
      }
      float a[8];
#pragma unroll
      for (int i = 0; i < 8; ++i) {
        auto rr = __builtin_amdgcn_permlane32_swap(__float_as_uint(p1[i]), __float_as_uint(p2[i]), false, false);
        a[i] = __uint_as_float(rr[0]) + __uint_as_float(rr[1]);
      }
      float cs[4];
#pragma unroll
      for (int i = 0; i < 4; ++i) {
        auto rr = __builtin_amdgcn_permlane16_swap(__float_as_uint(a[i]), __float_as_uint(a[4 + i]), false, false);
        cs[i] = sum16(__uint_as_float(rr[0]) + __uint_as_float(rr[1]));
      }
      const int l4 = lane & 15;
      const float cv = l4 == 0 ? cs[0] : (l4 == 1 ? cs[1] : (l4 == 2 ? cs[2] : cs[3]));
      if (l4 < 2) store_granule(ws, WS_RS + ((size_t)srow * 2 + (lane & 1)) * 8, epr + 1u, (lane & 1) ? rstd : mean);
      if (l4 < 4) store_granule(ws, WS_C + ((size_t)((ni * 8 + (mi >> 2)) * 128 + scol + ((lane >> 4) & 1) * 4 + l4) * 2 + (lane >> 5)) * 8, epc + 1u, cv);
      if (tid == 0) *(LAS u32x2*)(lds0 + LDS_TAG) = (u32x2){epr + 1u, epc + 1u};
    }
    STAMP(3);
    if (PRIO_M) __builtin_amdgcn_s_setprio(PRIO_M);
    const int r = lane & 31, h = lane >> 5, sw = (r >> 1) & 7;
    unsigned offx[4], offw[2];
#pragma unroll
    for (int ks = 0; ks < 4; ++ks) offx[ks] = lds0 + (unsigned)r * 128u + (unsigned)(((2 * ks + h) ^ sw) * 16);
    {
      const int blk = (lane >> 4) & 1, q = (lane & 15) >> 2, p = lane & 3;
#pragma unroll
      for (int tt = 0; tt < 2; ++tt) {
        const int row = 8 * h + 4 * tt + q, ch = 4 * wn + blk + 2 * (p & 1);
        offw[tt] = lds0 + 16384u + (unsigned)(256 * row + 16 * (ch ^ ((q << 2) | ((2 * h + tt) & 3))) + 8 * (p >> 1));
      }
    }
    unsigned offg = lds0 + LDS_G + (unsigned)h * 16u;
    const unsigned li = lane & 3;
    const unsigned mk0 = li == 0 ? 0xffffu : (li == 1 ? 0xffff0000u : 0u), mk1 = li == 2 ? 0xffffu : (li == 3 ? 0xffff0000u : 0u);
    f32x16 acc[4] = {};
    bf16x8 xf[4];
    s16x4 wra[2], wrb[2];
    u32x4 gq, dg;
    u32x4 wf, wfn;
    f32x4 d0, d1;
    const f32x4 z4 = {0.f, 0.f, 0.f, 0.f};
#define RX(mt, ks) xf[mt] = *(const LAS bf16x8*)(offx[ks] + (mt) * 4096)
#define RW(R, tt, ks) R[tt] = __builtin_amdgcn_ds_read_tr16_b64_v4i16((LAS s16x4*)(offw[tt] + (ks) * 4096))
#define RG(o) gq = *(const LAS u32x4*)(offg + (o) * 32)
#define DG0() do { dg[0] = gq[0] & mk[0]; dg[1] = gq[1] & mk[1]; } while (0)
#define DG1() do { dg[2] = gq[2] & mk[0]; dg[3] = gq[3] & mk[1]; } while (0)
#define SM0(R) d0 = __builtin_amdgcn_mfma_f32_4x4x4bf16_1k(__builtin_bit_cast(s16x4, (u32x2){dg[0], dg[1]}), R[0], z4, 0, 0, 0)
#define SM1(R) d1 = __builtin_amdgcn_mfma_f32_4x4x4bf16_1k(__builtin_bit_cast(s16x4, (u32x2){dg[2], dg[3]}), R[1], z4, 0, 0, 0)
#define CV0(W) do { W[0] = cvt_pk(d0[0], d0[1]); W[1] = cvt_pk(d0[2], d0[3]); } while (0)
#define CV1(W) do { W[2] = cvt_pk(d1[0], d1[1]); W[3] = cvt_pk(d1[2], d1[3]); } while (0)
#define MM(mt, W) acc[mt] = __builtin_amdgcn_mfma_f32_32x32x16_bf16(__builtin_bit_cast(bf16x8, W), xf[mt], acc[mt], 0, 0, 0)
#define STEP(WC, WN, RA, RB, kx, kw, kg, EXTRA2, EXTRA3) do { \
      SM0(RA); SM1(RA); SCHED(); \
      MM(0, WC); SCHED(); RX(0, kx); RW(RB, 0, kw); DG0(); SCHED(); \
      MM(1, WC); SCHED(); RX(1, kx); RW(RB, 1, kw); DG1(); SCHED(); \
      MM(2, WC); SCHED(); RX(2, kx); RG(kg); CV0(WN); EXTRA2; SCHED(); \
      MM(3, WC); SCHED(); RX(3, kx); CV1(WN); EXTRA3; SCHED(); } while (0)
    unsigned mk[2] = {mk0, mk1};
    asm volatile("" : "+v"(acc[0]), "+v"(acc[1]), "+v"(acc[2]), "+v"(acc[3]));
    asm volatile("" : "+v"(offx[0]), "+v"(offx[1]), "+v"(offx[2]), "+v"(offx[3]), "+v"(offw[0]), "+v"(offw[1]), "+v"(offg), "+v"(mk[0]), "+v"(mk[1]));
    __builtin_amdgcn_s_waitcnt(0xC07F);
    STAMP(4);
    BARRIER_N();
    STAMP(5);
    RG(0); RW(wrb, 0, 0); RW(wrb, 1, 0); RX(0, 0); RW(wra, 0, 1); RW(wra, 1, 1); RX(1, 0); RX(2, 0); RX(3, 0);
    DG0(); DG1(); SCHED(); RG(1); SM0(wrb); SM1(wrb); SCHED();
    CV0(wf); CV1(wf); DG0(); DG1(); SCHED(); RG(2); SCHED();
#pragma unroll 1
    for (int s = 0; s < NST - 1; ++s) {
      const int dlt = ((s & (RING - 1)) == RING - 1) ? -(RING - 1) * SLOT : SLOT;
      STEP(wf, wfn, wra, wrb, 1, 2, 3, (void)0, (void)0);
      STEP(wfn, wf, wrb, wra, 2, 3, 4, (offx[0] += dlt, offw[0] += dlt), (offw[1] += dlt, offx[1] += dlt));
      BARRIER_N();
      STEP(wf, wfn, wra, wrb, 3, 0, 5, (offx[2] += dlt), (void)0);
      STEP(wfn, wf, wrb, wra, 0, 1, 6, (offx[3] += dlt), (offg += 128u));
    }
    f32x4 c1v[4], c2v[4];
    f32x2 rs[4];
    const unsigned tc = lds0 + LDS_TAB + 1024 + (unsigned)(wn * 32 + h * 16) * 4u, tr = lds0 + LDS_TAB + (unsigned)r * 8u;
#define RC(q) do { c1v[q] = *(const LAS f32x4*)(tc + (q) * 16); c2v[q] = *(const LAS f32x4*)(tc + 512 + (q) * 16); rs[q] = *(const LAS f32x2*)(tr + (q) * 256); } while (0)
    STEP(wf, wfn, wra, wrb, 1, 2, 3, (void)0, (void)0);
    STEP(wfn, wf, wrb, wra, 2, 3, 4, (void)0, (void)0);
    SM0(wra); SM1(wra); SCHED();
    MM(0, wf); SCHED(); RX(0, 3); SCHED();
    MM(1, wf); SCHED(); RX(1, 3); SCHED();
    MM(2, wf); SCHED(); RX(2, 3); CV0(wfn); SCHED();
    MM(3, wf); SCHED(); RX(3, 3); CV1(wfn); SCHED();
    MM(0, wfn); SCHED(); RC(0); SCHED(); MM(1, wfn); SCHED(); RC(1); SCHED(); MM(2, wfn); SCHED(); RC(2); SCHED(); MM(3, wfn); SCHED(); RC(3); SCHED();
    STAMP(20);
    const unsigned so = lds0 + (unsigned)r * 256u, sc0 = (unsigned)(((wn * 4 + h * 2) ^ (r & 7)) * 16), sc1 = sc0 ^ 16u;
#pragma unroll
    for (int mt = 0; mt < 4; ++mt) {
      const float nm = -rs[mt][0], rstd = rs[mt][1];
      f32x4 tq[4];
#pragma unroll
      for (int q = 0; q < 4; ++q) {
        const f32x4 av = {acc[mt][4 * q], acc[mt][4 * q + 1], acc[mt][4 * q + 2], acc[mt][4 * q + 3]};
        tq[q] = av + c1v[q] * nm;
      }
#pragma unroll
      for (int q = 0; q < 4; ++q) tq[q] = tq[q] * rstd + c2v[q];
      const u32x4 o0 = {cvt_pk(tq[0][0], tq[0][1]), cvt_pk(tq[0][2], tq[0][3]), cvt_pk(tq[1][0], tq[1][1]), cvt_pk(tq[1][2], tq[1][3])};
      const u32x4 o1 = {cvt_pk(tq[2][0], tq[2][1]), cvt_pk(tq[2][2], tq[2][3]), cvt_pk(tq[3][0], tq[3][1]), cvt_pk(tq[3][2], tq[3][3])};
      *(LAS u32x4*)(so + mt * 8192 + sc0) = o0;
      *(LAS u32x4*)(so + mt * 8192 + sc1) = o1;
      SCHED();
    }
  }
  STAMP(24);
  BARRIER_L();
  STAMP(25);
#pragma unroll
  for (int i = 0; i < 4; ++i) {
    const int row = (wave * 4 + i) * 4 + (lane >> 4), ch = lane & 15;
    const u32x4 v = *(const LAS u32x4*)(lds0 + row * 256 + ((ch ^ (row & 7)) * 16));
    *(u32x4*)(out + (size_t)(mi * 128 + row) * ND + ni * 128 + ch * 8) = v;
  }
  STAMP_FLUSH();
  asm volatile("lnlin_code_end:");
}

extern "C" void kernel_launch(void* const* d_in, const int* in_sizes, int n_in, void* d_out, int out_size, void* d_ws, size_t ws_size,
                              hipStream_t stream) {
  const u16* x = (const u16*)d_in[0]; const u16* g = (const u16*)d_in[1]; const u16* b = (const u16*)d_in[2]; const u16* w = (const u16*)d_in[3];
  (void)hipFuncSetAttribute((const void*)lnlin_kernel, hipFuncAttributeMaxDynamicSharedMemorySize, LDS_TOTAL);
  lnlin_kernel<<<256, 512, LDS_TOTAL, stream>>>(x, g, b, w, (u16*)d_out, (char*)d_ws);
}
```

```cpp
#define EXP_DELAY 800
#define EXP_DATA
#define EXP_CODE
#include <hip/hip_runtime.h>
#include <hip/hip_bf16.h>
#include <utility>
#pragma clang diagnostic ignored "-Wint-to-pointer-cast"

typedef short bf16x8 __attribute__((ext_vector_type(8)));
typedef short s16x4 __attribute__((ext_vector_type(4)));
typedef float f32x16 __attribute__((ext_vector_type(16)));
typedef float f32x4 __attribute__((ext_vector_type(4)));
typedef float f32x2 __attribute__((ext_vector_type(2)));
typedef unsigned u32x4 __attribute__((ext_vector_type(4)));
typedef unsigned u32x2 __attribute__((ext_vector_type(2)));
typedef __bf16 bf16x2_t __attribute__((ext_vector_type(2)));
typedef unsigned short u16;
typedef unsigned long long u64;
#define GAS __attribute__((address_space(1)))
#define LAS __attribute__((address_space(3)))

#define PRIO_M 2
#define PRIO_L 0

constexpr int KD = 1024, ND = 1024;
constexpr int NST = 16;
constexpr int RING = 4;
constexpr int SLOT = 32768;
constexpr int LDS_G = RING * SLOT;
constexpr int LDS_TAB = LDS_G + 2048 + 64;
constexpr int LDS_TAG = LDS_TAB + 2048;
constexpr int LDS_TOTAL = LDS_TAG + 16;

constexpr size_t WS_EPR = 0;
constexpr size_t WS_EPC = 32 * 128;
constexpr size_t WS_RS = 8192;
constexpr size_t WS_C = WS_RS + 4096 * 2 * 8;

#define STAMP_DECL
#define STAMP_RT(i)
#define STAMP(i)
#define STAMP_DYN(i)
#define STAMP_FLUSH()

template <int B, int E, class F> __device__ __forceinline__ void static_for(F&& f) {
  if constexpr (B < E) { f(std::integral_constant<int, B>{}); static_for<B + 1, E>(f); }
}
__device__ __forceinline__ float bf_lo(unsigned u) { return __uint_as_float(u << 16); }
__device__ __forceinline__ float bf_hi(unsigned u) { return __uint_as_float(u & 0xffff0000u); }
__device__ __forceinline__ unsigned cvt_pk(float lo, float hi) {
  f32x2 v = {lo, hi}; return __builtin_bit_cast(unsigned, __builtin_convertvector(v, bf16x2_t));
}
template <int CTRL> __device__ __forceinline__ float dpp_add(float x) {
  return x + __builtin_bit_cast(float, __builtin_amdgcn_update_dpp(0, __builtin_bit_cast(int, x), CTRL, 0xf, 0xf, true));
}
__device__ __forceinline__ float sum16(float x) {
  x = dpp_add<0x128>(x); x = dpp_add<0x124>(x); x = dpp_add<0x122>(x); x = dpp_add<0x121>(x);
  return x;
}
#define SCHED() __builtin_amdgcn_sched_barrier(0)
#define BARRIER_L() do { SCHED(); asm volatile("s_waitcnt lgkmcnt(0)\n\ts_barrier" ::: "memory"); SCHED(); } while (0)
#define BARRIER_N() do { SCHED(); asm volatile("s_barrier" ::: "memory"); SCHED(); } while (0)
template <int N> __device__ __forceinline__ void wait_vm() { asm volatile("s_waitcnt vmcnt(%0)" ::"n"(N) : "memory"); }

__device__ __forceinline__ void glds16x4(unsigned v0, unsigned v1, unsigned v2, unsigned v3, const void* sbase, unsigned lds_dst) {
  unsigned keep;
  asm volatile("s_mov_b32 %0, m0\n\ts_mov_b32 m0, %6\n\ts_nop 0\n\t"
               "global_load_lds_dwordx4 %1, %5\n\tglobal_load_lds_dwordx4 %2, %5 offset:1024\n\t"
               "global_load_lds_dwordx4 %3, %5 offset:2048\n\tglobal_load_lds_dwordx4 %4, %5 offset:3072\n\ts_mov_b32 m0, %0"
               : "=&s"(keep) : "v"(v0), "v"(v1), "v"(v2), "v"(v3), "s"(sbase), "s"(lds_dst) : "memory");
}
__device__ __forceinline__ void glds4x2(unsigned v0, const void* sbase, unsigned lds_dst) {
  unsigned keep;
  asm volatile("s_mov_b32 %0, m0\n\ts_mov_b32 m0, %3\n\ts_nop 0\n\t"
               "global_load_lds_dword %1, %2\n\tglobal_load_lds_dword %1, %2 offset:256\n\ts_mov_b32 m0, %0"
               : "=&s"(keep) : "v"(v0), "s"(sbase), "s"(lds_dst) : "memory");
}
template <int OFF> __device__ __forceinline__ u32x4 gload16_asm(unsigned voff, const void* sbase) {
  u32x4 r; asm volatile("global_load_dwordx4 %0, %1, %2 offset:%3" : "=v"(r) : "v"(voff), "s"(sbase), "n"(OFF) : "memory"); return r;
}
__device__ __forceinline__ unsigned gload_u32_sc1_asm(unsigned voff, const void* sbase) {
  unsigned r; asm volatile("global_load_dword %0, %1, %2 sc1" : "=v"(r) : "v"(voff), "s"(sbase) : "memory"); return r;
}
template <int OFF> __device__ __forceinline__ u32x2 gload_u64_sc1_asm(unsigned voff, const void* sbase) {
  u32x2 r; asm volatile("global_load_dwordx2 %0, %1, %2 offset:%3 sc1" : "=v"(r) : "v"(voff), "s"(sbase), "n"(OFF) : "memory"); return r;
}
__device__ __forceinline__ void store_granule(char* ws, size_t off, unsigned tag, float v) {
  __hip_atomic_store((GAS u64*)(ws + off), ((u64)tag << 32) | __float_as_uint(v), __ATOMIC_RELAXED, __HIP_MEMORY_SCOPE_AGENT);
}
__device__ __forceinline__ u64 load_granule(const char* ws, size_t off) {
  return __hip_atomic_load((GAS u64*)(ws + off), __ATOMIC_RELAXED, __HIP_MEMORY_SCOPE_AGENT);
}
__device__ __forceinline__ unsigned load_word(const char* ws, size_t off) {
  return __hip_atomic_load((GAS unsigned*)(ws + off), __ATOMIC_RELAXED, __HIP_MEMORY_SCOPE_AGENT);
}

__global__ __launch_bounds__(512, 3) void lnlin_kernel(const u16* __restrict__ x, const u16* __restrict__ g, const u16* __restrict__ b,
                                                       const u16* __restrict__ w, u16* __restrict__ out, char* ws) {
  u64 code_pc; unsigned code_len;
  asm volatile("s_getpc_b64 %0\nlnlin_code_begin:\n\ts_mov_b32 %1, lnlin_code_end-lnlin_code_begin" : "=s"(code_pc), "=s"(code_len));
  extern __shared__ __attribute__((aligned(16))) char smem_g[];
  const int bid = blockIdx.x, xcd = bid & 7, idx = bid >> 3;
  const int mi = xcd * 4 + (idx >> 3), ni = idx & 7;
  const int tid = threadIdx.x, lane = tid & 63;
  const int wave = __builtin_amdgcn_readfirstlane(tid >> 6);
  const unsigned lds0 = (unsigned)(size_t)(LAS char*)smem_g;
  STAMP_DECL STAMP_RT(0); STAMP(1);
  const u64 ts = __builtin_amdgcn_s_memrealtime();
  { u32x4 tq = {0u, 0u, 0u, 0u};
#pragma unroll
    for (int i = 0; i < 4; ++i) tq ^= *(const GAS u32x4*)((const char*)x + (size_t)(mi * 128 + ni * 16) * 2048 + (size_t)i * 8192 + tid * 16);
#pragma unroll
    for (int i = 0; i < 8; ++i) tq ^= *(const GAS u32x4*)((const char*)w + (size_t)idx * 65536 + (size_t)i * 8192 + tid * 16);
    asm volatile("" :: "v"(tq) : "memory"); }
  { const unsigned lim = (code_len & ~15u) - 16u;
    u32x4 tq = *(const GAS u32x4*)(code_pc + min((unsigned)(tid * 16), lim)) ^ *(const GAS u32x4*)(code_pc + min((unsigned)(tid * 16 + 8192), lim));
    asm volatile("" :: "v"(tq) : "memory"); }
  while (__builtin_amdgcn_s_memrealtime() - ts < EXP_DELAY) __builtin_amdgcn_s_sleep(2);

  if (wave >= 4) {
    const int d = wave - 4, t = tid - 256;
    const unsigned co = (unsigned)d * 3072u + (unsigned)lane * 16u, lim = (code_len & ~15u) - 16u;
    u32x4 t0 = gload16_asm<0>(min(co, lim), (const void*)code_pc), t1 = gload16_asm<0>(min(co + 1024u, lim), (const void*)code_pc),
          t2 = gload16_asm<0>(min(co + 2048u, lim), (const void*)code_pc);
    if (PRIO_L) __builtin_amdgcn_s_setprio(PRIO_L);
    unsigned xo[4], wo[4];
#pragma unroll
    for (int i = 0; i < 4; ++i) {
      const int row = i * 8 + (lane >> 3);
      xo[i] = (unsigned)row * 2048u + (unsigned)(((lane & 7) ^ ((row >> 1) & 7)) * 16) - (unsigned)i * 1024u;
      const int kr = i * 4 + (lane >> 4);
      wo[i] = (unsigned)kr * 2048u + (unsigned)(((lane & 15) ^ (((lane >> 4) << 2) | i)) * 16) - (unsigned)i * 1024u;
    }
    const unsigned dst = lds0 + (unsigned)d * 4096u;
    const char* xb = (const char*)x + (size_t)mi * 262144 + d * 65536;
    const char* wb = (const char*)w + d * 32768 + ni * 256;
    auto issue = [&](int st) {
      const unsigned ds_ = dst + (unsigned)(st & (RING - 1)) * SLOT;
      glds16x4(wo[0], wo[1], wo[2], wo[3], wb + (size_t)st * 131072, ds_ + 16384u);
      glds16x4(xo[0], xo[1], xo[2], xo[3], xb + st * 128, ds_);
    };
    glds4x2((unsigned)lane * 4u, (const char*)g + d * 512, lds0 + LDS_G + (unsigned)d * 512u);
    issue(0); issue(1);
    STAMP(2);
    asm volatile("s_waitcnt vmcnt(8)" : "+v"(t0), "+v"(t1), "+v"(t2)::"memory");
    STAMP(3);
    BARRIER_N();
    constexpr int GP = 11;
#pragma unroll 1
    for (int j = 0; j < GP; ++j) {
      issue(j + 2);
      wait_vm<8>();
      BARRIER_N();
      STAMP_DYN(j + 4);
    }
    u32x2 gr[9];
    issue(13);
    gr[0] = gload_u64_sc1_asm<0>((unsigned)t * 8u, ws + WS_RS + (size_t)mi * 2048);
    static_for<0, 8>([&](auto J) { constexpr int j = J;
      gr[1 + j] = gload_u64_sc1_asm<(j & 1) * 2048>((unsigned)t * 8u + (j >> 1) * 4096u, ws + WS_C + (size_t)ni * 16384); });
    wait_vm<17>(); BARRIER_N();
    STAMP(15);
    issue(14);
    wait_vm<17>(); BARRIER_N();
    STAMP(16);
    issue(15);
    asm volatile("s_waitcnt vmcnt(8)"
                 : "+v"(gr[0]), "+v"(gr[1]), "+v"(gr[2]), "+v"(gr[3]), "+v"(gr[4]), "+v"(gr[5]), "+v"(gr[6]), "+v"(gr[7]), "+v"(gr[8]) :: "memory");
    BARRIER_N();
    STAMP(17);
    const u32x2 tags = *(const LAS u32x2*)(lds0 + LDS_TAG);
    const unsigned tagr = tags[0], tagc = tags[1];
    bool ok = gr[0][1] == tagr;
#pragma unroll
    for (int j = 0; j < 8; ++j) ok &= gr[1 + j][1] == tagc;
    float vr = __uint_as_float(gr[0][0]), vc = 0.f;
#pragma unroll
    for (int j = 0; j < 8; ++j) vc += __uint_as_float(gr[1 + j][0]);
    if (!__all(ok)) {
#pragma unroll 1
      for (int it = 0; it < 20000; ++it) {
        u64 q[9];
        q[0] = load_granule(ws, WS_RS + (size_t)mi * 2048 + (size_t)t * 8);
#pragma unroll
        for (int j = 0; j < 8; ++j) q[1 + j] = load_granule(ws, WS_C + (size_t)ni * 16384 + (size_t)j * 2048 + (size_t)t * 8);
        ok = (unsigned)(q[0] >> 32) == tagr; vr = __uint_as_float((unsigned)q[0]); vc = 0.f;
#pragma unroll
        for (int j = 0; j < 8; ++j) { ok &= (unsigned)(q[1 + j] >> 32) == tagc; vc += __uint_as_float((unsigned)q[1 + j]); }
        if (__all(ok)) break;
        __builtin_amdgcn_s_sleep(4);
      }
      if (!__all(ok)) {
        const u16* xrow = x + (size_t)(mi * 128 + (t >> 1)) * KD;
        float sm = 0.f, sq = 0.f;
#pragma unroll 1
        for (int k = 0; k < KD; ++k) sm += bf_lo(xrow[k]);
        sm *= (1.f / 1024.f);
#pragma unroll 1
        for (int k = 0; k < KD; ++k) { const float dd = bf_lo(xrow[k]) - sm; sq = fmaf(dd, dd, sq); }
        vr = (t & 1) ? __builtin_amdgcn_rsqf(sq * (1.f / 1024.f) + 1e-5f) : sm;
        const u16* wc = w + ni * 128 + (t >> 1);
        const u16* gb = (t & 1) ? b : g;
        vc = 0.f;
#pragma unroll 1
        for (int k = 0; k < KD; ++k) {
          const float pr = bf_lo(gb[k]) * bf_lo(wc[(size_t)k * ND]);
          vc += (t & 1) ? pr : bf_lo(cvt_pk(pr, 0.f));
        }
      }
    }
    *(LAS float*)(lds0 + LDS_TAB + t * 4) = vr;
    *(LAS float*)(lds0 + LDS_TAB + 1024 + (t & 1) * 512 + (t >> 1) * 4) = vc;
    STAMP(18);
    wait_vm<0>();
    BARRIER_L();
    STAMP(19);
    if (t == 0) {
      __hip_atomic_fetch_add((GAS unsigned*)(ws + WS_EPR + mi * 128), 1u, __ATOMIC_RELAXED, __HIP_MEMORY_SCOPE_AGENT);
      __hip_atomic_fetch_add((GAS unsigned*)(ws + WS_EPC + ni * 128), 1u, __ATOMIC_RELAXED, __HIP_MEMORY_SCOPE_AGENT);
    }
  } else {
    const int wn = wave;
    {
      const int srow = mi * 128 + ni * 16 + wn * 4 + (lane >> 4);
      const char* xs = (const char*)x + (size_t)srow * 2048 + (lane & 15) * 16;
      u32x4 xr[8];
#pragma unroll
      for (int j = 0; j < 8; ++j) xr[j] = *(const GAS u32x4*)(xs + j * 256);
      const int sk = (mi >> 2) * 128 + lane, scol = (mi & 3) * 32 + wn * 8;
      const char* wsl = (const char*)w + (size_t)sk * 2048 + (ni * 128 + scol) * 2;
      const u32x4 wv0 = *(const GAS u32x4*)wsl, wv1 = *(const GAS u32x4*)(wsl + 64 * 2048);
      const unsigned gv0 = g[sk], gv1 = g[sk + 64], bv0 = b[sk], bv1 = b[sk + 64];
      const unsigned epr = load_word(ws, WS_EPR + mi * 128), epc = load_word(ws, WS_EPC + ni * 128);
      SCHED();
      float s0 = 0.f, s1 = 0.f, s2 = 0.f, s3 = 0.f;
#pragma unroll
      for (int j = 0; j < 8; ++j) {
        s0 += bf_lo(xr[j][0]); s1 += bf_hi(xr[j][0]); s2 += bf_lo(xr[j][1]); s3 += bf_hi(xr[j][1]);
        s0 += bf_lo(xr[j][2]); s1 += bf_hi(xr[j][2]); s2 += bf_lo(xr[j][3]); s3 += bf_hi(xr[j][3]);
      }
      const float mean = sum16((s0 + s1) + (s2 + s3)) * (1.f / 1024.f);
      float q0 = 0.f, q1 = 0.f, q2 = 0.f, q3 = 0.f;
#pragma unroll
      for (int j = 0; j < 8; ++j)
#pragma unroll
        for (int i = 0; i < 4; i += 2) {
          const float d0 = bf_lo(xr[j][i]) - mean, d1 = bf_hi(xr[j][i]) - mean, d2 = bf_lo(xr[j][i + 1]) - mean, d3 = bf_hi(xr[j][i + 1]) - mean;
          q0 = fmaf(d0, d0, q0); q1 = fmaf(d1, d1, q1); q2 = fmaf(d2, d2, q2); q3 = fmaf(d3, d3, q3);
        }
      const float var = sum16((q0 + q1) + (q2 + q3)) * (1.f / 1024.f);
      const float rstd = __builtin_amdgcn_rsqf(var + 1e-5f);
      STAMP(2);
      float p1[8], p2[8];
      const float ga = bf_lo(gv0), gb = bf_lo(gv1), ba = bf_lo(bv0), bb = bf_lo(bv1);
#pragma unroll
      for (int i = 0; i < 4; ++i) {
        const float l0 = bf_lo(wv0[i]), h0 = bf_hi(wv0[i]), l1 = bf_lo(wv1[i]), h1 = bf_hi(wv1[i]);
        const unsigned pa = cvt_pk(ga * l0, ga * h0), pb = cvt_pk(gb * l1, gb * h1);
        p1[2 * i] = bf_lo(pa) + bf_lo(pb); p1[2 * i + 1] = bf_hi(pa) + bf_hi(pb);
        p2[2 * i] = fmaf(ba, l0, bb * l1); p2[2 * i + 1] = fmaf(ba, h0, bb * h1);
      }
      float a[8];
#pragma unroll
      for (int i = 0; i < 8; ++i) {
        auto rr = __builtin_amdgcn_permlane32_swap(__float_as_uint(p1[i]), __float_as_uint(p2[i]), false, false);
        a[i] = __uint_as_float(rr[0]) + __uint_as_float(rr[1]);
      }
      float cs[4];
#pragma unroll
      for (int i = 0; i < 4; ++i) {
        auto rr = __builtin_amdgcn_permlane16_swap(__float_as_uint(a[i]), __float_as_uint(a[4 + i]), false, false);
        cs[i] = sum16(__uint_as_float(rr[0]) + __uint_as_float(rr[1]));
      }
      const int l4 = lane & 15;
      const float cv = l4 == 0 ? cs[0] : (l4 == 1 ? cs[1] : (l4 == 2 ? cs[2] : cs[3]));
      if (l4 < 2) store_granule(ws, WS_RS + ((size_t)srow * 2 + (lane & 1)) * 8, epr + 1u, (lane & 1) ? rstd : mean);
      if (l4 < 4) store_granule(ws, WS_C + ((size_t)((ni * 8 + (mi >> 2)) * 128 + scol + ((lane >> 4) & 1) * 4 + l4) * 2 + (lane >> 5)) * 8, epc + 1u, cv);
      if (tid == 0) *(LAS u32x2*)(lds0 + LDS_TAG) = (u32x2){epr + 1u, epc + 1u};
    }
    STAMP(3);
    if (PRIO_M) __builtin_amdgcn_s_setprio(PRIO_M);
    const int r = lane & 31, h = lane >> 5, sw = (r >> 1) & 7;
    unsigned offx[4], offw[2];
#pragma unroll
    for (int ks = 0; ks < 4; ++ks) offx[ks] = lds0 + (unsigned)r * 128u + (unsigned)(((2 * ks + h) ^ sw) * 16);
    {
      const int blk = (lane >> 4) & 1, q = (lane & 15) >> 2, p = lane & 3;
#pragma unroll
      for (int tt = 0; tt < 2; ++tt) {
        const int row = 8 * h + 4 * tt + q, ch = 4 * wn + blk + 2 * (p & 1);
        offw[tt] = lds0 + 16384u + (unsigned)(256 * row + 16 * (ch ^ ((q << 2) | ((2 * h + tt) & 3))) + 8 * (p >> 1));
      }
    }
    unsigned offg = lds0 + LDS_G + (unsigned)h * 16u;
    const unsigned li = lane & 3;
    const unsigned mk0 = li == 0 ? 0xffffu : (li == 1 ? 0xffff0000u : 0u), mk1 = li == 2 ? 0xffffu : (li == 3 ? 0xffff0000u : 0u);
    f32x16 acc[4] = {};
    bf16x8 xf[4];
    s16x4 wra[2], wrb[2];
    u32x4 gq, dg;
    u32x4 wf, wfn;
    f32x4 d0, d1;
    const f32x4 z4 = {0.f, 0.f, 0.f, 0.f};
#define RX(mt, ks) xf[mt] = *(const LAS bf16x8*)(offx[ks] + (mt) * 4096)
#define RW(R, tt, ks) R[tt] = __builtin_amdgcn_ds_read_tr16_b64_v4i16((LAS s16x4*)(offw[tt] + (ks) * 4096))
#define RG(o) gq = *(const LAS u32x4*)(offg + (o) * 32)
#define DG0() do { dg[0] = gq[0] & mk[0]; dg[1] = gq[1] & mk[1]; } while (0)
#define DG1() do { dg[2] = gq[2] & mk[0]; dg[3] = gq[3] & mk[1]; } while (0)
#define SM0(R) d0 = __builtin_amdgcn_mfma_f32_4x4x4bf16_1k(__builtin_bit_cast(s16x4, (u32x2){dg[0], dg[1]}), R[0], z4, 0, 0, 0)
#define SM1(R) d1 = __builtin_amdgcn_mfma_f32_4x4x4bf16_1k(__builtin_bit_cast(s16x4, (u32x2){dg[2], dg[3]}), R[1], z4, 0, 0, 0)
#define CV0(W) do { W[0] = cvt_pk(d0[0], d0[1]); W[1] = cvt_pk(d0[2], d0[3]); } while (0)
#define CV1(W) do { W[2] = cvt_pk(d1[0], d1[1]); W[3] = cvt_pk(d1[2], d1[3]); } while (0)
#define MM(mt, W) acc[mt] = __builtin_amdgcn_mfma_f32_32x32x16_bf16(__builtin_bit_cast(bf16x8, W), xf[mt], acc[mt], 0, 0, 0)
#define STEP(WC, WN, RA, RB, kx, kw, kg, EXTRA2, EXTRA3) do { \
      SM0(RA); SM1(RA); SCHED(); \
      MM(0, WC); SCHED(); RX(0, kx); RW(RB, 0, kw); DG0(); SCHED(); \
      MM(1, WC); SCHED(); RX(1, kx); RW(RB, 1, kw); DG1(); SCHED(); \
      MM(2, WC); SCHED(); RX(2, kx); RG(kg); CV0(WN); EXTRA2; SCHED(); \
      MM(3, WC); SCHED(); RX(3, kx); CV1(WN); EXTRA3; SCHED(); } while (0)
    unsigned mk[2] = {mk0, mk1};
    asm volatile("" : "+v"(acc[0]), "+v"(acc[1]), "+v"(acc[2]), "+v"(acc[3]));
    asm volatile("" : "+v"(offx[0]), "+v"(offx[1]), "+v"(offx[2]), "+v"(offx[3]), "+v"(offw[0]), "+v"(offw[1]), "+v"(offg), "+v"(mk[0]), "+v"(mk[1]));
    __builtin_amdgcn_s_waitcnt(0xC07F);
    STAMP(4);
    BARRIER_N();
    STAMP(5);
    RG(0); RW(wrb, 0, 0); RW(wrb, 1, 0); RX(0, 0); RW(wra, 0, 1); RW(wra, 1, 1); RX(1, 0); RX(2, 0); RX(3, 0);
    DG0(); DG1(); SCHED(); RG(1); SM0(wrb); SM1(wrb); SCHED();
    CV0(wf); CV1(wf); DG0(); DG1(); SCHED(); RG(2); SCHED();
#pragma unroll 1
    for (int s = 0; s < NST - 1; ++s) {
      const int dlt = ((s & (RING - 1)) == RING - 1) ? -(RING - 1) * SLOT : SLOT;
      STEP(wf, wfn, wra, wrb, 1, 2, 3, (void)0, (void)0);
      STEP(wfn, wf, wrb, wra, 2, 3, 4, (offx[0] += dlt, offw[0] += dlt), (offw[1] += dlt, offx[1] += dlt));
      BARRIER_N();
      STEP(wf, wfn, wra, wrb, 3, 0, 5, (offx[2] += dlt), (void)0);
      STEP(wfn, wf, wrb, wra, 0, 1, 6, (offx[3] += dlt), (offg += 128u));
    }
    f32x4 c1v[4], c2v[4];
    f32x2 rs[4];
    const unsigned tc = lds0 + LDS_TAB + 1024 + (unsigned)(wn * 32 + h * 16) * 4u, tr = lds0 + LDS_TAB + (unsigned)r * 8u;
#define RC(q) do { c1v[q] = *(const LAS f32x4*)(tc + (q) * 16); c2v[q] = *(const LAS f32x4*)(tc + 512 + (q) * 16); rs[q] = *(const LAS f32x2*)(tr + (q) * 256); } while (0)
    STEP(wf, wfn, wra, wrb, 1, 2, 3, (void)0, (void)0);
    STEP(wfn, wf, wrb, wra, 2, 3, 4, (void)0, (void)0);
    SM0(wra); SM1(wra); SCHED();
    MM(0, wf); SCHED(); RX(0, 3); SCHED();
    MM(1, wf); SCHED(); RX(1, 3); SCHED();
    MM(2, wf); SCHED(); RX(2, 3); CV0(wfn); SCHED();
    MM(3, wf); SCHED(); RX(3, 3); CV1(wfn); SCHED();
    MM(0, wfn); SCHED(); RC(0); SCHED(); MM(1, wfn); SCHED(); RC(1); SCHED(); MM(2, wfn); SCHED(); RC(2); SCHED(); MM(3, wfn); SCHED(); RC(3); SCHED();
    STAMP(20);
    const unsigned so = lds0 + (unsigned)r * 256u, sc0 = (unsigned)(((wn * 4 + h * 2) ^ (r & 7)) * 16), sc1 = sc0 ^ 16u;
#pragma unroll
    for (int mt = 0; mt < 4; ++mt) {
      const float nm = -rs[mt][0], rstd = rs[mt][1];
      f32x4 tq[4];
#pragma unroll
      for (int q = 0; q < 4; ++q) {
        const f32x4 av = {acc[mt][4 * q], acc[mt][4 * q + 1], acc[mt][4 * q + 2], acc[mt][4 * q + 3]};
        tq[q] = av + c1v[q] * nm;
      }
#pragma unroll
      for (int q = 0; q < 4; ++q) tq[q] = tq[q] * rstd + c2v[q];
      const u32x4 o0 = {cvt_pk(tq[0][0], tq[0][1]), cvt_pk(tq[0][2], tq[0][3]), cvt_pk(tq[1][0], tq[1][1]), cvt_pk(tq[1][2], tq[1][3])};
      const u32x4 o1 = {cvt_pk(tq[2][0], tq[2][1]), cvt_pk(tq[2][2], tq[2][3]), cvt_pk(tq[3][0], tq[3][1]), cvt_pk(tq[3][2], tq[3][3])};
      *(LAS u32x4*)(so + mt * 8192 + sc0) = o0;
      *(LAS u32x4*)(so + mt * 8192 + sc1) = o1;
      SCHED();
    }
  }
  STAMP(24);
  BARRIER_L();
  STAMP(25);
#pragma unroll
  for (int i = 0; i < 4; ++i) {
    const int row = (wave * 4 + i) * 4 + (lane >> 4), ch = lane & 15;
    const u32x4 v = *(const LAS u32x4*)(lds0 + row * 256 + ((ch ^ (row & 7)) * 16));
    *(u32x4*)(out + (size_t)(mi * 128 + row) * ND + ni * 128 + ch * 8) = v;
  }
  STAMP_FLUSH();
  asm volatile("lnlin_code_end:");
}

extern "C" void kernel_launch(void* const* d_in, const int* in_sizes, int n_in, void* d_out, int out_size, void* d_ws, size_t ws_size,
                              hipStream_t stream) {
  const u16* x = (const u16*)d_in[0]; const u16* g = (const u16*)d_in[1]; const u16* b = (const u16*)d_in[2]; const u16* w = (const u16*)d_in[3];
  (void)hipFuncSetAttribute((const void*)lnlin_kernel, hipFuncAttributeMaxDynamicSharedMemorySize, LDS_TOTAL);
  lnlin_kernel<<<256, 512, LDS_TOTAL, stream>>>(x, g, b, w, (u16*)d_out, (char*)d_ws);
}
```

```cpp
#define ST_FLAVOR "nt"
#include <hip/hip_runtime.h>
#include <hip/hip_bf16.h>
#include <utility>
#pragma clang diagnostic ignored "-Wint-to-pointer-cast"

typedef short bf16x8 __attribute__((ext_vector_type(8)));
typedef short s16x4 __attribute__((ext_vector_type(4)));
typedef float f32x16 __attribute__((ext_vector_type(16)));
typedef float f32x4 __attribute__((ext_vector_type(4)));
typedef float f32x2 __attribute__((ext_vector_type(2)));
typedef unsigned u32x4 __attribute__((ext_vector_type(4)));
typedef unsigned u32x2 __attribute__((ext_vector_type(2)));
typedef __bf16 bf16x2_t __attribute__((ext_vector_type(2)));
typedef unsigned short u16;
typedef unsigned long long u64;
#define GAS __attribute__((address_space(1)))
#define LAS __attribute__((address_space(3)))

#define PRIO_M 2
#define PRIO_L 0

constexpr int KD = 1024, ND = 1024;
constexpr int NST = 16;
constexpr int RING = 4;
constexpr int SLOT = 32768;
constexpr int LDS_G = RING * SLOT;
constexpr int LDS_TAB = LDS_G + 2048 + 64;
constexpr int LDS_TAG = LDS_TAB + 2048;
constexpr int LDS_TOTAL = LDS_TAG + 16;

constexpr size_t WS_EPR = 0;
constexpr size_t WS_EPC = 32 * 128;
constexpr size_t WS_RS = 8192;
constexpr size_t WS_C = WS_RS + 4096 * 2 * 8;

#define STAMP_DECL
#define STAMP_RT(i)
#define STAMP(i)
#define STAMP_DYN(i)
#define STAMP_FLUSH()

template <int B, int E, class F> __device__ __forceinline__ void static_for(F&& f) {
  if constexpr (B < E) { f(std::integral_constant<int, B>{}); static_for<B + 1, E>(f); }
}
__device__ __forceinline__ float bf_lo(unsigned u) { return __uint_as_float(u << 16); }
__device__ __forceinline__ float bf_hi(unsigned u) { return __uint_as_float(u & 0xffff0000u); }
__device__ __forceinline__ unsigned cvt_pk(float lo, float hi) {
  f32x2 v = {lo, hi}; return __builtin_bit_cast(unsigned, __builtin_convertvector(v, bf16x2_t));
}
template <int CTRL> __device__ __forceinline__ float dpp_add(float x) {
  return x + __builtin_bit_cast(float, __builtin_amdgcn_update_dpp(0, __builtin_bit_cast(int, x), CTRL, 0xf, 0xf, true));
}
__device__ __forceinline__ float sum16(float x) {
  x = dpp_add<0x128>(x); x = dpp_add<0x124>(x); x = dpp_add<0x122>(x); x = dpp_add<0x121>(x);
  return x;
}
#define SCHED() __builtin_amdgcn_sched_barrier(0)
#define BARRIER_L() do { SCHED(); asm volatile("s_waitcnt lgkmcnt(0)\n\ts_barrier" ::: "memory"); SCHED(); } while (0)
#define BARRIER_N() do { SCHED(); asm volatile("s_barrier" ::: "memory"); SCHED(); } while (0)
template <int N> __device__ __forceinline__ void wait_vm() { asm volatile("s_waitcnt vmcnt(%0)" ::"n"(N) : "memory"); }

__device__ __forceinline__ void glds16x4(unsigned v0, unsigned v1, unsigned v2, unsigned v3, const void* sbase, unsigned lds_dst) {
  unsigned keep;
  asm volatile("s_mov_b32 %0, m0\n\ts_mov_b32 m0, %6\n\ts_nop 0\n\t"
               "global_load_lds_dwordx4 %1, %5\n\tglobal_load_lds_dwordx4 %2, %5 offset:1024\n\t"
               "global_load_lds_dwordx4 %3, %5 offset:2048\n\tglobal_load_lds_dwordx4 %4, %5 offset:3072\n\ts_mov_b32 m0, %0"
               : "=&s"(keep) : "v"(v0), "v"(v1), "v"(v2), "v"(v3), "s"(sbase), "s"(lds_dst) : "memory");
}
__device__ __forceinline__ void glds4x2(unsigned v0, const void* sbase, unsigned lds_dst) {
  unsigned keep;
  asm volatile("s_mov_b32 %0, m0\n\ts_mov_b32 m0, %3\n\ts_nop 0\n\t"
               "global_load_lds_dword %1, %2\n\tglobal_load_lds_dword %1, %2 offset:256\n\ts_mov_b32 m0, %0"
               : "=&s"(keep) : "v"(v0), "s"(sbase), "s"(lds_dst) : "memory");
}
template <int OFF> __device__ __forceinline__ u32x4 gload16_asm(unsigned voff, const void* sbase) {
  u32x4 r; asm volatile("global_load_dwordx4 %0, %1, %2 offset:%3" : "=v"(r) : "v"(voff), "s"(sbase), "n"(OFF) : "memory"); return r;
}
__device__ __forceinline__ unsigned gload_u32_sc1_asm(unsigned voff, const void* sbase) {
  unsigned r; asm volatile("global_load_dword %0, %1, %2 sc1" : "=v"(r) : "v"(voff), "s"(sbase) : "memory"); return r;
}
template <int OFF> __device__ __forceinline__ u32x2 gload_u64_sc1_asm(unsigned voff, const void* sbase) {
  u32x2 r; asm volatile("global_load_dwordx2 %0, %1, %2 offset:%3 sc1" : "=v"(r) : "v"(voff), "s"(sbase), "n"(OFF) : "memory"); return r;
}
__device__ __forceinline__ void store_granule(char* ws, size_t off, unsigned tag, float v) {
  __hip_atomic_store((GAS u64*)(ws + off), ((u64)tag << 32) | __float_as_uint(v), __ATOMIC_RELAXED, __HIP_MEMORY_SCOPE_AGENT);
}
__device__ __forceinline__ u64 load_granule(const char* ws, size_t off) {
  return __hip_atomic_load((GAS u64*)(ws + off), __ATOMIC_RELAXED, __HIP_MEMORY_SCOPE_AGENT);
}
__device__ __forceinline__ unsigned load_word(const char* ws, size_t off) {
  return __hip_atomic_load((GAS unsigned*)(ws + off), __ATOMIC_RELAXED, __HIP_MEMORY_SCOPE_AGENT);
}

__global__ __launch_bounds__(512, 3) void lnlin_kernel(const u16* __restrict__ x, const u16* __restrict__ g, const u16* __restrict__ b,
                                                       const u16* __restrict__ w, u16* __restrict__ out, char* ws) {
  u64 code_pc; unsigned code_len;
  asm volatile("s_getpc_b64 %0\nlnlin_code_begin:\n\ts_mov_b32 %1, lnlin_code_end-lnlin_code_begin" : "=s"(code_pc), "=s"(code_len));
  extern __shared__ __attribute__((aligned(16))) char smem_g[];
  const int bid = blockIdx.x, xcd = bid & 7, idx = bid >> 3;
  const int mi = xcd * 4 + (idx >> 3), ni = idx & 7;
  const int tid = threadIdx.x, lane = tid & 63;
  const int wave = __builtin_amdgcn_readfirstlane(tid >> 6);
  const unsigned lds0 = (unsigned)(size_t)(LAS char*)smem_g;
  STAMP_DECL STAMP_RT(0); STAMP(1);

  if (wave >= 4) {
    const int d = wave - 4, t = tid - 256;
    const unsigned co = (unsigned)d * 3072u + (unsigned)lane * 16u, lim = (code_len & ~15u) - 16u;
    u32x4 t0 = gload16_asm<0>(min(co, lim), (const void*)code_pc), t1 = gload16_asm<0>(min(co + 1024u, lim), (const void*)code_pc),
          t2 = gload16_asm<0>(min(co + 2048u, lim), (const void*)code_pc);
    if (PRIO_L) __builtin_amdgcn_s_setprio(PRIO_L);
    unsigned xo[4], wo[4];
#pragma unroll
    for (int i = 0; i < 4; ++i) {
      const int row = i * 8 + (lane >> 3);
      xo[i] = (unsigned)row * 2048u + (unsigned)(((lane & 7) ^ ((row >> 1) & 7)) * 16) - (unsigned)i * 1024u;
      const int kr = i * 4 + (lane >> 4);
      wo[i] = (unsigned)kr * 2048u + (unsigned)(((lane & 15) ^ (((lane >> 4) << 2) | i)) * 16) - (unsigned)i * 1024u;
    }
    const unsigned dst = lds0 + (unsigned)d * 4096u;
    const char* xb = (const char*)x + (size_t)mi * 262144 + d * 65536;
    const char* wb = (const char*)w + d * 32768 + ni * 256;
    auto issue = [&](int st) {
      const unsigned ds_ = dst + (unsigned)(st & (RING - 1)) * SLOT;
      glds16x4(wo[0], wo[1], wo[2], wo[3], wb + (size_t)st * 131072, ds_ + 16384u);
      glds16x4(xo[0], xo[1], xo[2], xo[3], xb + st * 128, ds_);
    };
    glds4x2((unsigned)lane * 4u, (const char*)g + d * 512, lds0 + LDS_G + (unsigned)d * 512u);
    issue(0); issue(1);
    STAMP(2);
    asm volatile("s_waitcnt vmcnt(8)" : "+v"(t0), "+v"(t1), "+v"(t2)::"memory");
    STAMP(3);
    BARRIER_N();
    constexpr int GP = 11;
#pragma unroll 1
    for (int j = 0; j < GP; ++j) {
      issue(j + 2);
      wait_vm<8>();
      BARRIER_N();
      STAMP_DYN(j + 4);
    }
    u32x2 gr[9];
    issue(13);
    gr[0] = gload_u64_sc1_asm<0>((unsigned)t * 8u, ws + WS_RS + (size_t)mi * 2048);
    static_for<0, 8>([&](auto J) { constexpr int j = J;
      gr[1 + j] = gload_u64_sc1_asm<(j & 1) * 2048>((unsigned)t * 8u + (j >> 1) * 4096u, ws + WS_C + (size_t)ni * 16384); });
    wait_vm<17>(); BARRIER_N();
    STAMP(15);
    issue(14);
    wait_vm<17>(); BARRIER_N();
    STAMP(16);
    issue(15);
    asm volatile("s_waitcnt vmcnt(8)"
                 : "+v"(gr[0]), "+v"(gr[1]), "+v"(gr[2]), "+v"(gr[3]), "+v"(gr[4]), "+v"(gr[5]), "+v"(gr[6]), "+v"(gr[7]), "+v"(gr[8]) :: "memory");
    BARRIER_N();
    STAMP(17);
    const u32x2 tags = *(const LAS u32x2*)(lds0 + LDS_TAG);
    const unsigned tagr = tags[0], tagc = tags[1];
    bool ok = gr[0][1] == tagr;
#pragma unroll
    for (int j = 0; j < 8; ++j) ok &= gr[1 + j][1] == tagc;
    float vr = __uint_as_float(gr[0][0]), vc = 0.f;
#pragma unroll
    for (int j = 0; j < 8; ++j) vc += __uint_as_float(gr[1 + j][0]);
    if (!__all(ok)) {
#pragma unroll 1
      for (int it = 0; it < 20000; ++it) {
        u64 q[9];
        q[0] = load_granule(ws, WS_RS + (size_t)mi * 2048 + (size_t)t * 8);
#pragma unroll
        for (int j = 0; j < 8; ++j) q[1 + j] = load_granule(ws, WS_C + (size_t)ni * 16384 + (size_t)j * 2048 + (size_t)t * 8);
        ok = (unsigned)(q[0] >> 32) == tagr; vr = __uint_as_float((unsigned)q[0]); vc = 0.f;
#pragma unroll
        for (int j = 0; j < 8; ++j) { ok &= (unsigned)(q[1 + j] >> 32) == tagc; vc += __uint_as_float((unsigned)q[1 + j]); }
        if (__all(ok)) break;
        __builtin_amdgcn_s_sleep(4);
      }
      if (!__all(ok)) {
        const u16* xrow = x + (size_t)(mi * 128 + (t >> 1)) * KD;
        float sm = 0.f, sq = 0.f;
#pragma unroll 1
        for (int k = 0; k < KD; ++k) sm += bf_lo(xrow[k]);
        sm *= (1.f / 1024.f);
#pragma unroll 1
        for (int k = 0; k < KD; ++k) { const float dd = bf_lo(xrow[k]) - sm; sq = fmaf(dd, dd, sq); }
        vr = (t & 1) ? __builtin_amdgcn_rsqf(sq * (1.f / 1024.f) + 1e-5f) : sm;
        const u16* wc = w + ni * 128 + (t >> 1);
        const u16* gb = (t & 1) ? b : g;
        vc = 0.f;
#pragma unroll 1
        for (int k = 0; k < KD; ++k) {
          const float pr = bf_lo(gb[k]) * bf_lo(wc[(size_t)k * ND]);
          vc += (t & 1) ? pr : bf_lo(cvt_pk(pr, 0.f));
        }
      }
    }
    *(LAS float*)(lds0 + LDS_TAB + t * 4) = vr;
    *(LAS float*)(lds0 + LDS_TAB + 1024 + (t & 1) * 512 + (t >> 1) * 4) = vc;
    STAMP(18);
    wait_vm<0>();
    BARRIER_L();
    STAMP(19);
    if (t == 0) {
      __hip_atomic_fetch_add((GAS unsigned*)(ws + WS_EPR + mi * 128), 1u, __ATOMIC_RELAXED, __HIP_MEMORY_SCOPE_AGENT);
      __hip_atomic_fetch_add((GAS unsigned*)(ws + WS_EPC + ni * 128), 1u, __ATOMIC_RELAXED, __HIP_MEMORY_SCOPE_AGENT);
    }
  } else {
    const int wn = wave;
    {
      const int srow = mi * 128 + ni * 16 + wn * 4 + (lane >> 4);
      const char* xs = (const char*)x + (size_t)srow * 2048 + (lane & 15) * 16;
      u32x4 xr[8];
#pragma unroll
      for (int j = 0; j < 8; ++j) xr[j] = *(const GAS u32x4*)(xs + j * 256);
      const int sk = (mi >> 2) * 128 + lane, scol = (mi & 3) * 32 + wn * 8;
      const char* wsl = (const char*)w + (size_t)sk * 2048 + (ni * 128 + scol) * 2;
      const u32x4 wv0 = *(const GAS u32x4*)wsl, wv1 = *(const GAS u32x4*)(wsl + 64 * 2048);
      const unsigned gv0 = g[sk], gv1 = g[sk + 64], bv0 = b[sk], bv1 = b[sk + 64];
      const unsigned epr = load_word(ws, WS_EPR + mi * 128), epc = load_word(ws, WS_EPC + ni * 128);
      SCHED();
      float s0 = 0.f, s1 = 0.f, s2 = 0.f, s3 = 0.f;
#pragma unroll
      for (int j = 0; j < 8; ++j) {
        s0 += bf_lo(xr[j][0]); s1 += bf_hi(xr[j][0]); s2 += bf_lo(xr[j][1]); s3 += bf_hi(xr[j][1]);
        s0 += bf_lo(xr[j][2]); s1 += bf_hi(xr[j][2]); s2 += bf_lo(xr[j][3]); s3 += bf_hi(xr[j][3]);
      }
      const float mean = sum16((s0 + s1) + (s2 + s3)) * (1.f / 1024.f);
      float q0 = 0.f, q1 = 0.f, q2 = 0.f, q3 = 0.f;
#pragma unroll
      for (int j = 0; j < 8; ++j)
#pragma unroll
        for (int i = 0; i < 4; i += 2) {
          const float d0 = bf_lo(xr[j][i]) - mean, d1 = bf_hi(xr[j][i]) - mean, d2 = bf_lo(xr[j][i + 1]) - mean, d3 = bf_hi(xr[j][i + 1]) - mean;
          q0 = fmaf(d0, d0, q0); q1 = fmaf(d1, d1, q1); q2 = fmaf(d2, d2, q2); q3 = fmaf(d3, d3, q3);
        }
      const float var = sum16((q0 + q1) + (q2 + q3)) * (1.f / 1024.f);
      const float rstd = __builtin_amdgcn_rsqf(var + 1e-5f);
      STAMP(2);
      float p1[8], p2[8];
      const float ga = bf_lo(gv0), gb = bf_lo(gv1), ba = bf_lo(bv0), bb = bf_lo(bv1);
#pragma unroll
      for (int i = 0; i < 4; ++i) {
        const float l0 = bf_lo(wv0[i]), h0 = bf_hi(wv0[i]), l1 = bf_lo(wv1[i]), h1 = bf_hi(wv1[i]);
        const unsigned pa = cvt_pk(ga * l0, ga * h0), pb = cvt_pk(gb * l1, gb * h1);
        p1[2 * i] = bf_lo(pa) + bf_lo(pb); p1[2 * i + 1] = bf_hi(pa) + bf_hi(pb);
        p2[2 * i] = fmaf(ba, l0, bb * l1); p2[2 * i + 1] = fmaf(ba, h0, bb * h1);
      }
      float a[8];
#pragma unroll
      for (int i = 0; i < 8; ++i) {
        auto rr = __builtin_amdgcn_permlane32_swap(__float_as_uint(p1[i]), __float_as_uint(p2[i]), false, false);
        a[i] = __uint_as_float(rr[0]) + __uint_as_float(rr[1]);
      }
      float cs[4];
#pragma unroll
      for (int i = 0; i < 4; ++i) {
        auto rr = __builtin_amdgcn_permlane16_swap(__float_as_uint(a[i]), __float_as_uint(a[4 + i]), false, false);
        cs[i] = sum16(__uint_as_float(rr[0]) + __uint_as_float(rr[1]));
      }
      const int l4 = lane & 15;
      const float cv = l4 == 0 ? cs[0] : (l4 == 1 ? cs[1] : (l4 == 2 ? cs[2] : cs[3]));
      if (l4 < 2) store_granule(ws, WS_RS + ((size_t)srow * 2 + (lane & 1)) * 8, epr + 1u, (lane & 1) ? rstd : mean);
      if (l4 < 4) store_granule(ws, WS_C + ((size_t)((ni * 8 + (mi >> 2)) * 128 + scol + ((lane >> 4) & 1) * 4 + l4) * 2 + (lane >> 5)) * 8, epc + 1u, cv);
      if (tid == 0) *(LAS u32x2*)(lds0 + LDS_TAG) = (u32x2){epr + 1u, epc + 1u};
    }
    STAMP(3);
    if (PRIO_M) __builtin_amdgcn_s_setprio(PRIO_M);
    const int r = lane & 31, h = lane >> 5, sw = (r >> 1) & 7;
    unsigned offx[4], offw[2];
#pragma unroll
    for (int ks = 0; ks < 4; ++ks) offx[ks] = lds0 + (unsigned)r * 128u + (unsigned)(((2 * ks + h) ^ sw) * 16);
    {
      const int blk = (lane >> 4) & 1, q = (lane & 15) >> 2, p = lane & 3;
#pragma unroll
      for (int tt = 0; tt < 2; ++tt) {
        const int row = 8 * h + 4 * tt + q, ch = 4 * wn + blk + 2 * (p & 1);
        offw[tt] = lds0 + 16384u + (unsigned)(256 * row + 16 * (ch ^ ((q << 2) | ((2 * h + tt) & 3))) + 8 * (p >> 1));
      }
    }
    unsigned offg = lds0 + LDS_G + (unsigned)h * 16u;
    const unsigned li = lane & 3;
    const unsigned mk0 = li == 0 ? 0xffffu : (li == 1 ? 0xffff0000u : 0u), mk1 = li == 2 ? 0xffffu : (li == 3 ? 0xffff0000u : 0u);
    f32x16 acc[4] = {};
    bf16x8 xf[4];
    s16x4 wra[2], wrb[2];
    u32x4 gq, dg;
    u32x4 wf, wfn;
    f32x4 d0, d1;
    const f32x4 z4 = {0.f, 0.f, 0.f, 0.f};
#define RX(mt, ks) xf[mt] = *(const LAS bf16x8*)(offx[ks] + (mt) * 4096)
#define RW(R, tt, ks) R[tt] = __builtin_amdgcn_ds_read_tr16_b64_v4i16((LAS s16x4*)(offw[tt] + (ks) * 4096))
#define RG(o) gq = *(const LAS u32x4*)(offg + (o) * 32)
#define DG0() do { dg[0] = gq[0] & mk[0]; dg[1] = gq[1] & mk[1]; } while (0)
#define DG1() do { dg[2] = gq[2] & mk[0]; dg[3] = gq[3] & mk[1]; } while (0)
#define SM0(R) d0 = __builtin_amdgcn_mfma_f32_4x4x4bf16_1k(__builtin_bit_cast(s16x4, (u32x2){dg[0], dg[1]}), R[0], z4, 0, 0, 0)
#define SM1(R) d1 = __builtin_amdgcn_mfma_f32_4x4x4bf16_1k(__builtin_bit_cast(s16x4, (u32x2){dg[2], dg[3]}), R[1], z4, 0, 0, 0)
#define CV0(W) do { W[0] = cvt_pk(d0[0], d0[1]); W[1] = cvt_pk(d0[2], d0[3]); } while (0)
#define CV1(W) do { W[2] = cvt_pk(d1[0], d1[1]); W[3] = cvt_pk(d1[2], d1[3]); } while (0)
#define MM(mt, W) acc[mt] = __builtin_amdgcn_mfma_f32_32x32x16_bf16(__builtin_bit_cast(bf16x8, W), xf[mt], acc[mt], 0, 0, 0)
#define STEP(WC, WN, RA, RB, kx, kw, kg, EXTRA2, EXTRA3) do { \
      SM0(RA); SM1(RA); SCHED(); \
      MM(0, WC); SCHED(); RX(0, kx); RW(RB, 0, kw); DG0(); SCHED(); \
      MM(1, WC); SCHED(); RX(1, kx); RW(RB, 1, kw); DG1(); SCHED(); \
      MM(2, WC); SCHED(); RX(2, kx); RG(kg); CV0(WN); EXTRA2; SCHED(); \
      MM(3, WC); SCHED(); RX(3, kx); CV1(WN); EXTRA3; SCHED(); } while (0)
    unsigned mk[2] = {mk0, mk1};
    asm volatile("" : "+v"(acc[0]), "+v"(acc[1]), "+v"(acc[2]), "+v"(acc[3]));
    asm volatile("" : "+v"(offx[0]), "+v"(offx[1]), "+v"(offx[2]), "+v"(offx[3]), "+v"(offw[0]), "+v"(offw[1]), "+v"(offg), "+v"(mk[0]), "+v"(mk[1]));
    __builtin_amdgcn_s_waitcnt(0xC07F);
    STAMP(4);
    BARRIER_N();
    STAMP(5);
    RG(0); RW(wrb, 0, 0); RW(wrb, 1, 0); RX(0, 0); RW(wra, 0, 1); RW(wra, 1, 1); RX(1, 0); RX(2, 0); RX(3, 0);
    DG0(); DG1(); SCHED(); RG(1); SM0(wrb); SM1(wrb); SCHED();
    CV0(wf); CV1(wf); DG0(); DG1(); SCHED(); RG(2); SCHED();
#pragma unroll 1
    for (int s = 0; s < NST - 1; ++s) {
      const int dlt = ((s & (RING - 1)) == RING - 1) ? -(RING - 1) * SLOT : SLOT;
      STEP(wf, wfn, wra, wrb, 1, 2, 3, (void)0, (void)0);
      STEP(wfn, wf, wrb, wra, 2, 3, 4, (offx[0] += dlt, offw[0] += dlt), (offw[1] += dlt, offx[1] += dlt));
      BARRIER_N();
      STEP(wf, wfn, wra, wrb, 3, 0, 5, (offx[2] += dlt), (void)0);
      STEP(wfn, wf, wrb, wra, 0, 1, 6, (offx[3] += dlt), (offg += 128u));
    }
    f32x4 c1v[4], c2v[4];
    f32x2 rs[4];
    const unsigned tc = lds0 + LDS_TAB + 1024 + (unsigned)(wn * 32 + h * 16) * 4u, tr = lds0 + LDS_TAB + (unsigned)r * 8u;
#define RC(q) do { c1v[q] = *(const LAS f32x4*)(tc + (q) * 16); c2v[q] = *(const LAS f32x4*)(tc + 512 + (q) * 16); rs[q] = *(const LAS f32x2*)(tr + (q) * 256); } while (0)
    STEP(wf, wfn, wra, wrb, 1, 2, 3, (void)0, (void)0);
    STEP(wfn, wf, wrb, wra, 2, 3, 4, (void)0, (void)0);
    SM0(wra); SM1(wra); SCHED();
    MM(0, wf); SCHED(); RX(0, 3); SCHED();
    MM(1, wf); SCHED(); RX(1, 3); SCHED();
    MM(2, wf); SCHED(); RX(2, 3); CV0(wfn); SCHED();
    MM(3, wf); SCHED(); RX(3, 3); CV1(wfn); SCHED();
    MM(0, wfn); SCHED(); RC(0); SCHED(); MM(1, wfn); SCHED(); RC(1); SCHED(); MM(2, wfn); SCHED(); RC(2); SCHED(); MM(3, wfn); SCHED(); RC(3); SCHED();
    STAMP(20);
    const unsigned so = lds0 + (unsigned)r * 256u, sc0 = (unsigned)(((wn * 4 + h * 2) ^ (r & 7)) * 16), sc1 = sc0 ^ 16u;
#pragma unroll
    for (int mt = 0; mt < 4; ++mt) {
      const float nm = -rs[mt][0], rstd = rs[mt][1];
      f32x4 tq[4];
#pragma unroll
      for (int q = 0; q < 4; ++q) {
        const f32x4 av = {acc[mt][4 * q], acc[mt][4 * q + 1], acc[mt][4 * q + 2], acc[mt][4 * q + 3]};
        tq[q] = av + c1v[q] * nm;
      }
#pragma unroll
      for (int q = 0; q < 4; ++q) tq[q] = tq[q] * rstd + c2v[q];
      const u32x4 o0 = {cvt_pk(tq[0][0], tq[0][1]), cvt_pk(tq[0][2], tq[0][3]), cvt_pk(tq[1][0], tq[1][1]), cvt_pk(tq[1][2], tq[1][3])};
      const u32x4 o1 = {cvt_pk(tq[2][0], tq[2][1]), cvt_pk(tq[2][2], tq[2][3]), cvt_pk(tq[3][0], tq[3][1]), cvt_pk(tq[3][2], tq[3][3])};
      *(LAS u32x4*)(so + mt * 8192 + sc0) = o0;
      *(LAS u32x4*)(so + mt * 8192 + sc1) = o1;
      SCHED();
    }
  }
  STAMP(24);
  BARRIER_L();
  STAMP(25);
  {
    const int row0 = wave * 16 + (lane >> 4), ch = lane & 15;
    u32x4 v[4];
#pragma unroll
    for (int i = 0; i < 4; ++i) { const int row = row0 + i * 4; v[i] = *(const LAS u32x4*)(lds0 + row * 256 + ((ch ^ (row & 7)) * 16)); }
    const unsigned oo = (unsigned)row0 * 2048u + (unsigned)ch * 16u;
    char* ob = (char*)out + (size_t)mi * 262144 + ni * 256;
#pragma unroll
    for (int i = 0; i < 4; ++i) {
      asm volatile("global_store_dwordx4 %0, %1, %2 " ST_FLAVOR "\n\ts_nop 1" :: "v"(oo + i * 8192u), "v"(v[i]), "s"(ob) : "memory");
    }
  }
  STAMP_FLUSH();
  asm volatile("lnlin_code_end:");
}

extern "C" void kernel_launch(void* const* d_in, const int* in_sizes, int n_in, void* d_out, int out_size, void* d_ws, size_t ws_size,
                              hipStream_t stream) {
  const u16* x = (const u16*)d_in[0]; const u16* g = (const u16*)d_in[1]; const u16* b = (const u16*)d_in[2]; const u16* w = (const u16*)d_in[3];
  (void)hipFuncSetAttribute((const void*)lnlin_kernel, hipFuncAttributeMaxDynamicSharedMemorySize, LDS_TOTAL);
  lnlin_kernel<<<256, 512, LDS_TOTAL, stream>>>(x, g, b, w, (u16*)d_out, (char*)d_ws);
}
```

```cpp
#include <hip/hip_runtime.h>
#include <hip/hip_bf16.h>
#include <utility>
#pragma clang diagnostic ignored "-Wint-to-pointer-cast"

typedef short bf16x8 __attribute__((ext_vector_type(8)));
typedef short s16x4 __attribute__((ext_vector_type(4)));
typedef float f32x16 __attribute__((ext_vector_type(16)));
typedef float f32x4 __attribute__((ext_vector_type(4)));
typedef float f32x2 __attribute__((ext_vector_type(2)));
typedef unsigned u32x4 __attribute__((ext_vector_type(4)));
typedef unsigned u32x2 __attribute__((ext_vector_type(2)));
typedef __bf16 bf16x2_t __attribute__((ext_vector_type(2)));
typedef unsigned short u16;
typedef unsigned long long u64;
#define GAS __attribute__((address_space(1)))
#define LAS __attribute__((address_space(3)))

#define ST_FLAVOR "nt"
#define SIDE_START 2
#define PRIO_M 2
#define PRIO_L 0

constexpr int KD = 1024, ND = 1024;
constexpr int NST = 16;
constexpr int RING = 4;
constexpr int SLOT = 32768;
constexpr int LDS_G = RING * SLOT;
constexpr int LDS_B = LDS_G + 2048 + 64;
constexpr int LDS_TAB = LDS_B + 256;
constexpr int LDS_TOTAL = LDS_TAB + 2048;

constexpr size_t WS_EPR = 0;
constexpr size_t WS_EPC = 32 * 128;
constexpr size_t WS_RS = 8192;
constexpr size_t WS_C = WS_RS + 4096 * 2 * 8;

#define STAMP_DECL
#define STAMP_RT(i)
#define STAMP(i)
#define STAMP_DYN(i)
#define STAMP_FLUSH()

template <int B, int E, class F> __device__ __forceinline__ void static_for(F&& f) {
  if constexpr (B < E) { f(std::integral_constant<int, B>{}); static_for<B + 1, E>(f); }
}
__device__ __forceinline__ float bf_lo(unsigned u) { return __uint_as_float(u << 16); }
__device__ __forceinline__ float bf_hi(unsigned u) { return __uint_as_float(u & 0xffff0000u); }
__device__ __forceinline__ unsigned cvt_pk(float lo, float hi) {
  f32x2 v = {lo, hi}; return __builtin_bit_cast(unsigned, __builtin_convertvector(v, bf16x2_t));
}
template <int CTRL> __device__ __forceinline__ float dpp_add(float x) {
  return x + __builtin_bit_cast(float, __builtin_amdgcn_update_dpp(0, __builtin_bit_cast(int, x), CTRL, 0xf, 0xf, true));
}
__device__ __forceinline__ float sum16(float x) {
  x = dpp_add<0x128>(x); x = dpp_add<0x124>(x); x = dpp_add<0x122>(x); x = dpp_add<0x121>(x);
  return x;
}
#define SCHED() __builtin_amdgcn_sched_barrier(0)
#define BARRIER_L() do { SCHED(); asm volatile("s_waitcnt lgkmcnt(0)\n\ts_barrier" ::: "memory"); SCHED(); } while (0)
#define BARRIER_N() do { SCHED(); asm volatile("s_barrier" ::: "memory"); SCHED(); } while (0)
template <int N> __device__ __forceinline__ void wait_vm() { asm volatile("s_waitcnt vmcnt(%0)" ::"n"(N) : "memory"); }

__device__ __forceinline__ void glds16x4(unsigned v0, unsigned v1, unsigned v2, unsigned v3, const void* sbase, unsigned lds_dst) {
  unsigned keep;
  asm volatile("s_mov_b32 %0, m0\n\ts_mov_b32 m0, %6\n\ts_nop 0\n\t"
               "global_load_lds_dwordx4 %1, %5\n\tglobal_load_lds_dwordx4 %2, %5 offset:1024\n\t"
               "global_load_lds_dwordx4 %3, %5 offset:2048\n\tglobal_load_lds_dwordx4 %4, %5 offset:3072\n\ts_mov_b32 m0, %0"
               : "=&s"(keep) : "v"(v0), "v"(v1), "v"(v2), "v"(v3), "s"(sbase), "s"(lds_dst) : "memory");
}
__device__ __forceinline__ void glds4(unsigned v0, const void* sbase, unsigned lds_dst) {
  unsigned keep;
  asm volatile("s_mov_b32 %0, m0\n\ts_mov_b32 m0, %3\n\ts_nop 0\n\tglobal_load_lds_dword %1, %2\n\ts_mov_b32 m0, %0"
               : "=&s"(keep) : "v"(v0), "s"(sbase), "s"(lds_dst) : "memory");
}
template <int OFF> __device__ __forceinline__ u32x4 gload16_asm(unsigned voff, const void* sbase) {
  u32x4 r; asm volatile("global_load_dwordx4 %0, %1, %2 offset:%3" : "=v"(r) : "v"(voff), "s"(sbase), "n"(OFF) : "memory"); return r;
}
__device__ __forceinline__ unsigned gload_u32_sc1_asm(unsigned voff, const void* sbase) {
  unsigned r; asm volatile("global_load_dword %0, %1, %2 sc1" : "=v"(r) : "v"(voff), "s"(sbase) : "memory"); return r;
}
template <int OFF> __device__ __forceinline__ u32x2 gload_u64_sc1_asm(unsigned voff, const void* sbase) {
  u32x2 r; asm volatile("global_load_dwordx2 %0, %1, %2 offset:%3 sc1" : "=v"(r) : "v"(voff), "s"(sbase), "n"(OFF) : "memory"); return r;
}
__device__ __forceinline__ void store_granule(char* ws, size_t off, unsigned tag, float v) {
  __hip_atomic_store((GAS u64*)(ws + off), ((u64)tag << 32) | __float_as_uint(v), __ATOMIC_RELAXED, __HIP_MEMORY_SCOPE_AGENT);
}
__device__ __forceinline__ u64 load_granule(const char* ws, size_t off) {
  return __hip_atomic_load((GAS u64*)(ws + off), __ATOMIC_RELAXED, __HIP_MEMORY_SCOPE_AGENT);
}
__device__ __forceinline__ unsigned load_word(const char* ws, size_t off) {
  return __hip_atomic_load((GAS unsigned*)(ws + off), __ATOMIC_RELAXED, __HIP_MEMORY_SCOPE_AGENT);
}

template <int N> __device__ __forceinline__ void store_rows(unsigned lds0, int rowb, int lane, char* ob) {
  const int row0 = rowb + (lane >> 4), ch = lane & 15;
  u32x4 v[N];
#pragma unroll
  for (int i = 0; i < N; ++i) { const int row = row0 + i * 4; v[i] = *(const LAS u32x4*)(lds0 + row * 256 + ((ch ^ (row & 7)) * 16)); }
  const unsigned oo = (unsigned)row0 * 2048u + (unsigned)ch * 16u;
#pragma unroll
  for (int i = 0; i < N; ++i)
    asm volatile("global_store_dwordx4 %0, %1, %2 " ST_FLAVOR "\n\ts_nop 1" :: "v"(oo + i * 8192u), "v"(v[i]), "s"(ob) : "memory");
}

__global__ __launch_bounds__(768, 3) void lnlin_kernel(const u16* __restrict__ x, const u16* __restrict__ g, const u16* __restrict__ b,
                                                       const u16* __restrict__ w, u16* __restrict__ out, char* ws) {
  u64 code_pc; unsigned code_len;
  asm volatile("s_getpc_b64 %0\nlnlin_code_begin:\n\ts_mov_b32 %1, lnlin_code_end-lnlin_code_begin" : "=s"(code_pc), "=s"(code_len));
  extern __shared__ __attribute__((aligned(16))) char smem_g[];
  const int bid = blockIdx.x, xcd = bid & 7, idx = bid >> 3;
  const int mi = xcd * 4 + (idx >> 3), ni = idx & 7;
  const int rot = (mi >> 2) * 2;
  const int tid = threadIdx.x, lane = tid & 63;
  const int wave = __builtin_amdgcn_readfirstlane(tid >> 6);
  const unsigned lds0 = (unsigned)(size_t)(LAS char*)smem_g;
  STAMP_DECL STAMP_RT(0); STAMP(1);

  if (wave >= 8) {
    const int d = wave - 8, t = tid - 512;
    const int scol = (mi & 3) * 32 + d * 8;
    const unsigned cw = lds0 + 16384u + (unsigned)(256 * lane + 16 * ((scol >> 3) ^ (((lane & 3) << 2) | ((lane >> 2) & 3))));
    const int srow = mi * 128 + ni * 16 + d * 4 + (lane >> 4), l4 = lane & 15;
    const char* xs = (const char*)x + (size_t)srow * 2048 + l4 * 16;
    u32x4 xr[8];
    unsigned ep;
    float p1[8], p2[8], part = 0.f, mean = 0.f, pub = 0.f, v0 = 0.f, v1 = 0.f;
    unsigned bad = 0u;
    u32x4 q[8];
    const char* gsrc = d < 2 ? ws + WS_C + (size_t)ni * 16384 + (size_t)t * 16 : ws + WS_RS + (size_t)mi * 2048 + (size_t)(t - 128) * 16;
    auto gather = [&](int j0, int j1) {
#pragma unroll
      for (int j = j0; j < j1; ++j)
        if (j == 0 || d < 2) q[j] = __builtin_amdgcn_raw_buffer_load_b128(__builtin_amdgcn_make_buffer_rsrc((void*)ws, 0, 0x40000, 0x00020000),
                                                                         (unsigned)(gsrc - ws) + j * 2048u, 0, 16  );
    };
    ep = load_word(ws, (lane & 1) ? WS_EPC + ni * 128 : WS_EPR + mi * 128);
    STAMP(2);
    BARRIER_N();
    unsigned epr = 0, epc = 0;
    static_for<0, NST - 1>([&](auto S) {
      constexpr int st = S;
      if constexpr (st <= 1) {
        const u32x4 wv = *(const LAS u32x4*)(cw + st * SLOT);
        const float ga = bf_lo(*(const LAS u16*)(lds0 + LDS_G + st * 128 + lane * 2)), ba = bf_lo(*(const LAS u16*)(lds0 + LDS_B + st * 128 + lane * 2));
#pragma unroll
        for (int i = 0; i < 4; ++i) {
          const float l0 = bf_lo(wv[i]), h0 = bf_hi(wv[i]);
          const unsigned pa = cvt_pk(ga * l0, ga * h0);
          if constexpr (st == 0) { p1[2 * i] = bf_lo(pa); p1[2 * i + 1] = bf_hi(pa); p2[2 * i] = ba * l0; p2[2 * i + 1] = ba * h0; }
          else { p1[2 * i] += bf_lo(pa); p1[2 * i + 1] += bf_hi(pa); p2[2 * i] = fmaf(ba, l0, p2[2 * i]); p2[2 * i + 1] = fmaf(ba, h0, p2[2 * i + 1]); }
        }
      }
      if constexpr (st == 2) {
        float a[8];
#pragma unroll
        for (int i = 0; i < 8; ++i) {
          auto rr = __builtin_amdgcn_permlane32_swap(__float_as_uint(p1[i]), __float_as_uint(p2[i]), false, false);
          a[i] = __uint_as_float(rr[0]) + __uint_as_float(rr[1]);
        }
        float cs[4];
#pragma unroll
        for (int i = 0; i < 4; ++i) {
          auto rr = __builtin_amdgcn_permlane16_swap(__float_as_uint(a[i]), __float_as_uint(a[4 + i]), false, false);
          cs[i] = sum16(__uint_as_float(rr[0]) + __uint_as_float(rr[1]));
        }
        pub = l4 == 0 ? cs[0] : (l4 == 1 ? cs[1] : (l4 == 2 ? cs[2] : cs[3]));
        epr = (unsigned)__builtin_amdgcn_readlane((int)ep, 0); epc = (unsigned)__builtin_amdgcn_readlane((int)ep, 1);
      }
      if constexpr (st >= 3 && st <= 5) {
        constexpr int j0 = st == 3 ? 0 : (st == 4 ? 2 : 5), j1 = st == 3 ? 2 : (st == 4 ? 5 : 8);
        float s0 = part, s1 = 0.f, s2 = 0.f, s3 = 0.f;
#pragma unroll
        for (int j = j0; j < j1; ++j) {
          s0 += bf_lo(xr[j][0]); s1 += bf_hi(xr[j][0]); s2 += bf_lo(xr[j][1]); s3 += bf_hi(xr[j][1]);
          s0 += bf_lo(xr[j][2]); s1 += bf_hi(xr[j][2]); s2 += bf_lo(xr[j][3]); s3 += bf_hi(xr[j][3]);
        }
        part = (s0 + s1) + (s2 + s3);
        if constexpr (st == 5) { mean = sum16(part) * (1.f / 1024.f); part = 0.f; }
      }
      if constexpr (st >= 6 && st <= 8) {
        constexpr int j0 = (st - 6) * 3, j1 = st == 8 ? 8 : j0 + 3;
        float q0 = part, q1 = 0.f, q2 = 0.f, q3 = 0.f;
#pragma unroll
        for (int j = j0; j < j1; ++j)
#pragma unroll
          for (int i = 0; i < 4; i += 2) {
            const float d0 = bf_lo(xr[j][i]) - mean, d1 = bf_hi(xr[j][i]) - mean, d2 = bf_lo(xr[j][i + 1]) - mean, d3 = bf_hi(xr[j][i + 1]) - mean;
            q0 = fmaf(d0, d0, q0); q1 = fmaf(d1, d1, q1); q2 = fmaf(d2, d2, q2); q3 = fmaf(d3, d3, q3);
          }
        part = (q0 + q1) + (q2 + q3);
        if constexpr (st == 8) {
          const float var = sum16(part) * (1.f / 1024.f);
          const float rstd = __builtin_amdgcn_rsqf(var + 1e-5f);
          if (l4 < 2) store_granule(ws, WS_RS + ((size_t)srow * 2 + (lane & 1)) * 8, epr + 1u, (lane & 1) ? rstd : mean);
        }
      }
      SCHED();
      if constexpr (st <= 3) {
#pragma unroll
        for (int j = 2 * st; j < 2 * st + 2; ++j) xr[j] = *(const GAS u32x4*)(xs + (((j + 2 + (rot >> 1)) & 7) * 256));
      }
      if constexpr (st == 3) {
        if (l4 < 4) store_granule(ws, WS_C + ((size_t)((ni * 8 + (mi >> 2)) * 128 + scol + ((lane >> 4) & 1) * 4 + l4) * 2 + (lane >> 5)) * 8, epc + 1u, pub);
      }
      if constexpr (st == 9) { if (d < 2) gather(4, 8); }
      if constexpr (st == 10) { if (d < 2) gather(0, 4); }
      if constexpr (st == 12) { if (d >= 2) gather(0, 1); }
      if constexpr (st == 12 || st == 13) {
        if (d < 2) {
          const unsigned tag = epc + 1u;
#pragma unroll
          for (int j = (st == 12 ? 4 : 0); j < (st == 12 ? 8 : 4); ++j) {
            bad |= (q[j][1] ^ tag) | (q[j][3] ^ tag); v0 += __uint_as_float(q[j][0]); v1 += __uint_as_float(q[j][2]);
          }
        }
      }
      if constexpr (st == 14) {
        const unsigned tag = (d < 2 ? epc : epr) + 1u;
        if (d >= 2) { bad = (q[0][1] ^ tag) | (q[0][3] ^ tag); v0 = __uint_as_float(q[0][0]); v1 = __uint_as_float(q[0][2]); }
        bool ok = bad == 0u;
        if (__builtin_expect(!__all(ok), 0)) {
#pragma unroll 1
          for (int it = 0; it < 20000; ++it) {
            asm volatile("" ::: "memory");
            gather(0, 8);
            bad = (q[0][1] ^ tag) | (q[0][3] ^ tag); v0 = __uint_as_float(q[0][0]); v1 = __uint_as_float(q[0][2]);
            if (d < 2) {
              v0 = 0.f; v1 = 0.f;
#pragma unroll
              for (int jj = 0; jj < 8; ++jj) { const int j = jj ^ 4; bad |= (q[j][1] ^ tag) | (q[j][3] ^ tag); v0 += __uint_as_float(q[j][0]); v1 += __uint_as_float(q[j][2]); }
            }
            ok = bad == 0u;
            if (__all(ok)) break;
            __builtin_amdgcn_s_sleep(2);
          }
        }
        if (__builtin_expect(!__all(ok), 0)) {
          v0 = 0.f; v1 = 0.f;
          if (d < 2) {
            const u16* wc = w + ni * 128 + t;
#pragma unroll 1
            for (int k = 0; k < KD; ++k) {
              const float wv = bf_lo(wc[(size_t)k * ND]);
              v0 += bf_lo(cvt_pk(bf_lo(g[k]) * wv, 0.f)); v1 = fmaf(bf_lo(b[k]), wv, v1);
            }
          } else {
            const u16* xrow = x + (size_t)(mi * 128 + (t - 128)) * KD;
#pragma unroll 1
            for (int k = 0; k < KD; ++k) v0 += bf_lo(xrow[k]);
            v0 *= (1.f / 1024.f);
#pragma unroll 1
            for (int k = 0; k < KD; ++k) { const float dd = bf_lo(xrow[k]) - v0; v1 = fmaf(dd, dd, v1); }
            v1 = __builtin_amdgcn_rsqf(v1 * (1.f / 1024.f) + 1e-5f);
          }
        }
        if (d < 2) { *(LAS float*)(lds0 + LDS_TAB + 1024 + t * 4) = v0; *(LAS float*)(lds0 + LDS_TAB + 1536 + t * 4) = v1; }
        else *(LAS f32x2*)(lds0 + LDS_TAB + (t - 128) * 8) = (f32x2){v0, v1};
      }
      if constexpr (st == 14) { STAMP(23); BARRIER_L(); } else BARRIER_N();
      STAMP(3 + st);
    });
    if (t == 0) {
      __hip_atomic_fetch_add((GAS unsigned*)(ws + WS_EPR + mi * 128), 1u, __ATOMIC_RELAXED, __HIP_MEMORY_SCOPE_AGENT);
      __hip_atomic_fetch_add((GAS unsigned*)(ws + WS_EPC + ni * 128), 1u, __ATOMIC_RELAXED, __HIP_MEMORY_SCOPE_AGENT);
    }
  } else if (wave >= 4) {
    const int d = wave - 4;
    if (PRIO_L) __builtin_amdgcn_s_setprio(PRIO_L);
    unsigned xo[4], wo[4];
#pragma unroll
    for (int i = 0; i < 4; ++i) {
      const int row = i * 8 + (lane >> 3);
      xo[i] = (unsigned)row * 2048u + (unsigned)(((lane & 7) ^ ((row >> 1) & 7)) * 16) - (unsigned)i * 1024u;
      const int kr = i * 4 + (lane >> 4);
      wo[i] = (unsigned)kr * 2048u + (unsigned)(((lane & 15) ^ (((lane >> 4) << 2) | i)) * 16) - (unsigned)i * 1024u;
    }
    const unsigned dst = lds0 + (unsigned)d * 4096u;
    const char* xb = (const char*)x + (size_t)mi * 262144 + d * 65536;
    const char* wb = (const char*)w + d * 32768 + ni * 256;
    auto issue = [&](int st) {
      const unsigned ds_ = dst + (unsigned)(st & (RING - 1)) * SLOT;
      const int ks_ = (st + rot) & (NST - 1);
      glds16x4(wo[0], wo[1], wo[2], wo[3], wb + (size_t)ks_ * 131072, ds_ + 16384u);
      glds16x4(xo[0], xo[1], xo[2], xo[3], xb + ks_ * 128, ds_);
    };
    glds4((unsigned)lane * 4u, (const char*)g + ((d * 512 + rot * 128) & 2047), lds0 + LDS_G + (unsigned)d * 512u);
    glds4((unsigned)lane * 4u, (const char*)g + ((d * 512 + 256 + rot * 128) & 2047), lds0 + LDS_G + (unsigned)d * 512u + 256u);
    if (d == 0) glds4((unsigned)lane * 4u, (const char*)b + rot * 128, lds0 + LDS_B);
    issue(0); issue(1);
    STAMP(2);
    wait_vm<8>();
    STAMP(3);
    BARRIER_N();
#pragma unroll 1
    for (int j = 0; j < NST - 2; ++j) {
      issue(j + 2);
      wait_vm<8>();
      BARRIER_N();
      STAMP_DYN(j + 4);
    }
    wait_vm<0>();
    BARRIER_N();
    STAMP(18);
  } else {
    const int wn = wave;
    {
      const unsigned co = (unsigned)wn * 3072u + (unsigned)lane * 16u, lim = (code_len & ~15u) - 16u;
      const u32x4 t0 = *(const GAS u32x4*)(code_pc + min(co, lim)) ^ *(const GAS u32x4*)(code_pc + min(co + 1024u, lim)) ^ *(const GAS u32x4*)(code_pc + min(co + 2048u, lim));
      asm volatile("" :: "v"(t0));
    }
    STAMP(3);
    if (PRIO_M) __builtin_amdgcn_s_setprio(PRIO_M);
    const int r = lane & 31, h = lane >> 5, sw = (r >> 1) & 7;
    unsigned offx[4], offw[2];
#pragma unroll
    for (int ks = 0; ks < 4; ++ks) offx[ks] = lds0 + (unsigned)r * 128u + (unsigned)(((2 * ks + h) ^ sw) * 16);
    {
      const int blk = (lane >> 4) & 1, q = (lane & 15) >> 2, p = lane & 3;
#pragma unroll
      for (int tt = 0; tt < 2; ++tt) {
        const int row = 8 * h + 4 * tt + q, ch = 4 * wn + blk + 2 * (p & 1);
        offw[tt] = lds0 + 16384u + (unsigned)(256 * row + 16 * (ch ^ ((q << 2) | ((2 * h + tt) & 3))) + 8 * (p >> 1));
      }
    }
    unsigned offg = lds0 + LDS_G + (unsigned)h * 16u;
    const unsigned li = lane & 3;
    const unsigned mk0 = li == 0 ? 0xffffu : (li == 1 ? 0xffff0000u : 0u), mk1 = li == 2 ? 0xffffu : (li == 3 ? 0xffff0000u : 0u);
    f32x16 acc[4] = {};
    bf16x8 xf[4];
    s16x4 wra[2], wrb[2];
    u32x4 gq, dg;
    u32x4 wf, wfn;
    f32x4 d0, d1;
    const f32x4 z4 = {0.f, 0.f, 0.f, 0.f};
#define RX(mt, ks) xf[mt] = *(const LAS bf16x8*)(offx[ks] + (mt) * 4096)
#define RW(R, tt, ks) R[tt] = __builtin_amdgcn_ds_read_tr16_b64_v4i16((LAS s16x4*)(offw[tt] + (ks) * 4096))
#define RG(o) gq = *(const LAS u32x4*)(offg + (o) * 32)
#define DG0() do { dg[0] = gq[0] & mk[0]; dg[1] = gq[1] & mk[1]; } while (0)
#define DG1() do { dg[2] = gq[2] & mk[0]; dg[3] = gq[3] & mk[1]; } while (0)
#define SM0(R) d0 = __builtin_amdgcn_mfma_f32_4x4x4bf16_1k(__builtin_bit_cast(s16x4, (u32x2){dg[0], dg[1]}), R[0], z4, 0, 0, 0)
#define SM1(R) d1 = __builtin_amdgcn_mfma_f32_4x4x4bf16_1k(__builtin_bit_cast(s16x4, (u32x2){dg[2], dg[3]}), R[1], z4, 0, 0, 0)
#define CV0(W) do { W[0] = cvt_pk(d0[0], d0[1]); W[1] = cvt_pk(d0[2], d0[3]); } while (0)
#define CV1(W) do { W[2] = cvt_pk(d1[0], d1[1]); W[3] = cvt_pk(d1[2], d1[3]); } while (0)
#define MM(mt, W) acc[mt] = __builtin_amdgcn_mfma_f32_32x32x16_bf16(__builtin_bit_cast(bf16x8, W), xf[mt], acc[mt], 0, 0, 0)
#define STEP(WC, WN, RA, RB, kx, kw, kg, EXTRA2, EXTRA3) do { \
      SM0(RA); SM1(RA); SCHED(); \
      MM(0, WC); SCHED(); RX(0, kx); RW(RB, 0, kw); DG0(); SCHED(); \
      MM(1, WC); SCHED(); RX(1, kx); RW(RB, 1, kw); DG1(); SCHED(); \
      MM(2, WC); SCHED(); RX(2, kx); RG(kg); CV0(WN); EXTRA2; SCHED(); \
      MM(3, WC); SCHED(); RX(3, kx); CV1(WN); EXTRA3; SCHED(); } while (0)
    unsigned mk[2] = {mk0, mk1};
    asm volatile("" : "+v"(acc[0]), "+v"(acc[1]), "+v"(acc[2]), "+v"(acc[3]));
    asm volatile("" : "+v"(offx[0]), "+v"(offx[1]), "+v"(offx[2]), "+v"(offx[3]), "+v"(offw[0]), "+v"(offw[1]), "+v"(offg), "+v"(mk[0]), "+v"(mk[1]));
    __builtin_amdgcn_s_waitcnt(0xC07F);
    STAMP(4);
    BARRIER_N();
    STAMP(5);
    RG(0); RW(wrb, 0, 0); RW(wrb, 1, 0); RX(0, 0); RW(wra, 0, 1); RW(wra, 1, 1); RX(1, 0); RX(2, 0); RX(3, 0);
    DG0(); DG1(); SCHED(); RG(1); SM0(wrb); SM1(wrb); SCHED();
    CV0(wf); CV1(wf); DG0(); DG1(); SCHED(); RG(2); SCHED();
#pragma unroll 1
    for (int s = 0; s < NST - 1; ++s) {
      const int dlt = ((s & (RING - 1)) == RING - 1) ? -(RING - 1) * SLOT : SLOT;
      STEP(wf, wfn, wra, wrb, 1, 2, 3, (void)0, (void)0);
      STEP(wfn, wf, wrb, wra, 2, 3, 4, (offx[0] += dlt, offw[0] += dlt), (offw[1] += dlt, offx[1] += dlt));
      BARRIER_N();
      STEP(wf, wfn, wra, wrb, 3, 0, 5, (offx[2] += dlt), (void)0);
      STEP(wfn, wf, wrb, wra, 0, 1, 6, (offx[3] += dlt), (offg += 128u));
    }
    f32x4 c1v[4], c2v[4];
    f32x2 rs[4];
    const unsigned tc = lds0 + LDS_TAB + 1024 + (unsigned)(wn * 32 + h * 16) * 4u, tr = lds0 + LDS_TAB + (unsigned)r * 8u;
#define RC(q) do { c1v[q] = *(const LAS f32x4*)(tc + (q) * 16); c2v[q] = *(const LAS f32x4*)(tc + 512 + (q) * 16); rs[q] = *(const LAS f32x2*)(tr + (q) * 256); } while (0)
    STEP(wf, wfn, wra, wrb, 1, 2, 3, (void)0, (void)0);
    STEP(wfn, wf, wrb, wra, 2, 3, 4, (void)0, (void)0);
    SM0(wra); SM1(wra); SCHED();
    MM(0, wf); SCHED(); RX(0, 3); SCHED();
    MM(1, wf); SCHED(); RX(1, 3); SCHED();
    MM(2, wf); SCHED(); RX(2, 3); CV0(wfn); SCHED();
    MM(3, wf); SCHED(); RX(3, 3); CV1(wfn); SCHED();
    MM(0, wfn); SCHED(); RC(0); SCHED(); MM(1, wfn); SCHED(); RC(1); SCHED(); MM(2, wfn); SCHED(); RC(2); SCHED(); MM(3, wfn); SCHED(); RC(3); SCHED();
    STAMP(20);
    const unsigned so = lds0 + (unsigned)r * 256u, sc0 = (unsigned)(((wn * 4 + h * 2) ^ (r & 7)) * 16), sc1 = sc0 ^ 16u;
#pragma unroll
    for (int mt = 0; mt < 4; ++mt) {
      const float nm = -rs[mt][0], rstd = rs[mt][1];
      f32x4 tq[4];
#pragma unroll
      for (int q = 0; q < 4; ++q) {
        const f32x4 av = {acc[mt][4 * q], acc[mt][4 * q + 1], acc[mt][4 * q + 2], acc[mt][4 * q + 3]};
        tq[q] = av + c1v[q] * nm;
      }
#pragma unroll
      for (int q = 0; q < 4; ++q) tq[q] = tq[q] * rstd + c2v[q];
      const u32x4 o0 = {cvt_pk(tq[0][0], tq[0][1]), cvt_pk(tq[0][2], tq[0][3]), cvt_pk(tq[1][0], tq[1][1]), cvt_pk(tq[1][2], tq[1][3])};
      const u32x4 o1 = {cvt_pk(tq[2][0], tq[2][1]), cvt_pk(tq[2][2], tq[2][3]), cvt_pk(tq[3][0], tq[3][1]), cvt_pk(tq[3][2], tq[3][3])};
      *(LAS u32x4*)(so + mt * 8192 + sc0) = o0;
      *(LAS u32x4*)(so + mt * 8192 + sc1) = o1;
      SCHED();
      if (mt == 1) BARRIER_L();
    }
  }
  if (wave >= 4) {
    BARRIER_N();
    store_rows<2>(lds0, (wave - 4) * 8, lane, (char*)out + (size_t)mi * 262144 + ni * 256);
  }
  STAMP(24);
  BARRIER_L();
  STAMP(25);
  if (wave < 8) store_rows<2>(lds0, 64 + wave * 8, lane, (char*)out + (size_t)mi * 262144 + ni * 256);
  STAMP_FLUSH();
  asm volatile("lnlin_code_end:");
}

extern "C" void kernel_launch(void* const* d_in, const int* in_sizes, int n_in, void* d_out, int out_size, void* d_ws, size_t ws_size,
                              hipStream_t stream) {
  const u16* x = (const u16*)d_in[0]; const u16* g = (const u16*)d_in[1]; const u16* b = (const u16*)d_in[2]; const u16* w = (const u16*)d_in[3];
  (void)hipFuncSetAttribute((const void*)lnlin_kernel, hipFuncAttributeMaxDynamicSharedMemorySize, LDS_TOTAL);
  lnlin_kernel<<<256, 768, LDS_TOTAL, stream>>>(x, g, b, w, (u16*)d_out, (char*)d_ws);
}
```

```cpp
#include <hip/hip_runtime.h>
#include <hip/hip_bf16.h>
#include <utility>
#pragma clang diagnostic ignored "-Wint-to-pointer-cast"

typedef short bf16x8 __attribute__((ext_vector_type(8)));
typedef short s16x4 __attribute__((ext_vector_type(4)));
typedef float f32x16 __attribute__((ext_vector_type(16)));
typedef float f32x4 __attribute__((ext_vector_type(4)));
typedef float f32x2 __attribute__((ext_vector_type(2)));
typedef unsigned u32x4 __attribute__((ext_vector_type(4)));
typedef unsigned u32x2 __attribute__((ext_vector_type(2)));
typedef __bf16 bf16x2_t __attribute__((ext_vector_type(2)));
typedef unsigned short u16;
typedef unsigned long long u64;
#define GAS __attribute__((address_space(1)))
#define LAS __attribute__((address_space(3)))

#define ST_FLAVOR "nt"
#define SIDE_START 2
#define PRIO_M 2
#define PRIO_L 0

constexpr int KD = 1024, ND = 1024;
constexpr int NST = 16;
constexpr int RING = 4;
constexpr int SLOT = 32768;
constexpr int LDS_G = RING * SLOT;
constexpr int LDS_B = LDS_G + 2048 + 64;
constexpr int LDS_TAB = LDS_B + 256;
constexpr int LDS_TOTAL = LDS_TAB + 2048;

constexpr size_t WS_EPR = 0;
constexpr size_t WS_EPC = 32 * 128;
constexpr size_t WS_C = 8192;
constexpr size_t WS_RS = WS_C + 8 * 16384;

#define STAMP_DECL
#define STAMP_RT(i)
#define STAMP(i)
#define STAMP_DYN(i)
#define STAMP_FLUSH()

template <int B, int E, class F> __device__ __forceinline__ void static_for(F&& f) {
  if constexpr (B < E) { f(std::integral_constant<int, B>{}); static_for<B + 1, E>(f); }
}
__device__ __forceinline__ float bf_lo(unsigned u) { return __uint_as_float(u << 16); }
__device__ __forceinline__ float bf_hi(unsigned u) { return __uint_as_float(u & 0xffff0000u); }
__device__ __forceinline__ unsigned cvt_pk(float lo, float hi) {
  f32x2 v = {lo, hi}; return __builtin_bit_cast(unsigned, __builtin_convertvector(v, bf16x2_t));
}
template <int CTRL> __device__ __forceinline__ float dpp_add(float x) {
  return x + __builtin_bit_cast(float, __builtin_amdgcn_update_dpp(0, __builtin_bit_cast(int, x), CTRL, 0xf, 0xf, true));
}
__device__ __forceinline__ float sum16(float x) {
  x = dpp_add<0x128>(x); x = dpp_add<0x124>(x); x = dpp_add<0x122>(x); x = dpp_add<0x121>(x);
  return x;
}
__device__ __forceinline__ float add1(float a, float b) { float r; asm("v_add_f32 %0, %1, %2" : "=v"(r) : "v"(a), "v"(b)); return r; }
__device__ __forceinline__ float mul1(float a, float b) { float r; asm("v_mul_f32 %0, %1, %2" : "=v"(r) : "v"(a), "v"(b)); return r; }
__device__ __forceinline__ float fma1(float a, float b, float c) { float r; asm("v_fma_f32 %0, %1, %2, %3" : "=v"(r) : "v"(a), "v"(b), "v"(c)); return r; }
#define PIN8(a) asm volatile("" : "+v"(a[0]), "+v"(a[1]), "+v"(a[2]), "+v"(a[3]), "+v"(a[4]), "+v"(a[5]), "+v"(a[6]), "+v"(a[7]))
#define SCHED() __builtin_amdgcn_sched_barrier(0)
#define BARRIER_L() do { SCHED(); asm volatile("s_waitcnt lgkmcnt(0)\n\ts_barrier" ::: "memory"); SCHED(); } while (0)
#define BARRIER_N() do { SCHED(); asm volatile("s_barrier" ::: "memory"); SCHED(); } while (0)
template <int N> __device__ __forceinline__ void wait_vm() { asm volatile("s_waitcnt vmcnt(%0)" ::"n"(N) : "memory"); }

__device__ __forceinline__ void glds16x4(unsigned v0, unsigned v1, unsigned v2, unsigned v3, const void* sbase, unsigned lds_dst) {
  unsigned keep;
  asm volatile("s_mov_b32 %0, m0\n\ts_mov_b32 m0, %6\n\ts_nop 0\n\t"
               "global_load_lds_dwordx4 %1, %5\n\tglobal_load_lds_dwordx4 %2, %5 offset:1024\n\t"
               "global_load_lds_dwordx4 %3, %5 offset:2048\n\tglobal_load_lds_dwordx4 %4, %5 offset:3072\n\ts_mov_b32 m0, %0"
               : "=&s"(keep) : "v"(v0), "v"(v1), "v"(v2), "v"(v3), "s"(sbase), "s"(lds_dst) : "memory");
}
__device__ __forceinline__ void glds4(unsigned v0, const void* sbase, unsigned lds_dst) {
  unsigned keep;
  asm volatile("s_mov_b32 %0, m0\n\ts_mov_b32 m0, %3\n\ts_nop 0\n\tglobal_load_lds_dword %1, %2\n\ts_mov_b32 m0, %0"
               : "=&s"(keep) : "v"(v0), "s"(sbase), "s"(lds_dst) : "memory");
}
template <int OFF> __device__ __forceinline__ u32x4 gload16_asm(unsigned voff, const void* sbase) {
  u32x4 r; asm volatile("global_load_dwordx4 %0, %1, %2 offset:%3" : "=v"(r) : "v"(voff), "s"(sbase), "n"(OFF) : "memory"); return r;
}
__device__ __forceinline__ unsigned gload_u32_sc1_asm(unsigned voff, const void* sbase) {
  unsigned r; asm volatile("global_load_dword %0, %1, %2 sc1" : "=v"(r) : "v"(voff), "s"(sbase) : "memory"); return r;
}
template <int OFF> __device__ __forceinline__ u32x2 gload_u64_sc1_asm(unsigned voff, const void* sbase) {
  u32x2 r; asm volatile("global_load_dwordx2 %0, %1, %2 offset:%3 sc1" : "=v"(r) : "v"(voff), "s"(sbase), "n"(OFF) : "memory"); return r;
}
__device__ __forceinline__ void store_granule(char* ws, size_t off, unsigned tag, float v) {
  __hip_atomic_store((GAS u64*)(ws + off), ((u64)tag << 32) | __float_as_uint(v), __ATOMIC_RELAXED, __HIP_MEMORY_SCOPE_AGENT);
}
__device__ __forceinline__ u64 load_granule(const char* ws, size_t off) {
  return __hip_atomic_load((GAS u64*)(ws + off), __ATOMIC_RELAXED, __HIP_MEMORY_SCOPE_AGENT);
}
__device__ __forceinline__ unsigned load_word(const char* ws, size_t off) {
  return __hip_atomic_load((GAS unsigned*)(ws + off), __ATOMIC_RELAXED, __HIP_MEMORY_SCOPE_AGENT);
}

template <int N> __device__ __forceinline__ void store_rows(unsigned lds0, int rowb, int lane, char* ob) {
  const int row0 = rowb + (lane >> 4), ch = lane & 15;
  u32x4 v[N];
#pragma unroll
  for (int i = 0; i < N; ++i) { const int row = row0 + i * 4; v[i] = *(const LAS u32x4*)(lds0 + row * 256 + ((ch ^ (row & 7)) * 16)); }
  const unsigned oo = (unsigned)row0 * 2048u + (unsigned)ch * 16u;
#pragma unroll
  for (int i = 0; i < N; ++i)
    asm volatile("global_store_dwordx4 %0, %1, %2 " ST_FLAVOR "\n\ts_nop 1" :: "v"(oo + i * 8192u), "v"(v[i]), "s"(ob) : "memory");
}

__global__ __launch_bounds__(768, 3) void lnlin_kernel(const u16* __restrict__ x, const u16* __restrict__ g, const u16* __restrict__ b,
                                                       const u16* __restrict__ w, u16* __restrict__ out, char* ws) {
  u64 code_pc; unsigned code_len;
  asm volatile("s_getpc_b64 %0\nlnlin_code_begin:\n\ts_mov_b32 %1, lnlin_code_end-lnlin_code_begin" : "=s"(code_pc), "=s"(code_len));
  extern __shared__ __attribute__((aligned(16))) char smem_g[];
  const int bid = blockIdx.x, xcd = bid & 7, idx = bid >> 3;
  const int mi = xcd * 4 + (idx >> 3), ni = idx & 7;
  const int slab = (ni + (mi >> 2)) & 7, rot = slab * 2;
  const int tid = threadIdx.x, lane = tid & 63;
  const int wave = __builtin_amdgcn_readfirstlane(tid >> 6);
  const unsigned lds0 = (unsigned)(size_t)(LAS char*)smem_g;
  STAMP_DECL STAMP_RT(0); STAMP(1);

  if (wave >= 8) {
    const int d = wave - 8, t = tid - 512;
    const int scol = (mi & 3) * 32 + d * 8;
    const unsigned cw = lds0 + 16384u + (unsigned)(256 * lane + 16 * ((scol >> 3) ^ (((lane & 3) << 2) | ((lane >> 2) & 3))));
    const int srow = t >> 1, l4 = lane & 15;
    const unsigned sx = lds0 + (unsigned)srow * 128u, ssw = (unsigned)(((t & 1) * 4) ^ ((srow >> 1) & 7));
    u32x4 xr[8];
    f32x4 f[16];
    unsigned ep;
    float p1[8], p2[8], part = 0.f, mean = 0.f, pub = 0.f, v0 = 0.f, v1 = 0.f;
    unsigned bad = 0u;
    u32x4 q[8];
    const unsigned gsrc = d < 2 ? (unsigned)WS_C + (unsigned)ni * 16384u + (unsigned)t * 16u : (unsigned)WS_RS + (unsigned)mi * 16384u + (unsigned)(t - 128) * 16u;
    auto gather = [&](int j0, int j1) __attribute__((always_inline)) {
#pragma unroll
      for (int j = j0; j < j1; ++j)
        q[j] = __builtin_amdgcn_raw_buffer_load_b128(__builtin_amdgcn_make_buffer_rsrc((void*)ws, 0, 0x100000, 0x00020000), gsrc + j * 2048u, 0, 16  );
    };
    auto combine = [&](unsigned tag) __attribute__((always_inline)) {
      bad = 0u; v0 = 0.f; v1 = 0.f;
#pragma unroll
      for (int j = 0; j < 8; ++j) { bad |= (q[j][1] ^ tag) | (q[j][3] ^ tag); v0 = add1(v0, __uint_as_float(q[j][0])); v1 = add1(v1, __uint_as_float(q[j][2])); }
      if (d >= 2) {
        v0 *= 0.125f;
        float dv = 0.f;
#pragma unroll
        for (int j = 0; j < 8; ++j) { const float e = __uint_as_float(q[j][0]) - v0; dv = fmaf(e, e, dv); }
        v1 = __builtin_amdgcn_rsqf(fmaf(dv, 128.f, v1) * (1.f / 1024.f) + 1e-5f);
      }
    };
    ep = load_word(ws, (lane & 1) ? WS_EPC + ni * 128 : WS_EPR + mi * 128);
    STAMP(2);
    BARRIER_N();
    unsigned epr = 0, epc = 0;
    static_for<0, NST - 1>([&](auto S) {
      constexpr int st = S;
      if constexpr (st <= 1) {
        const u32x4 wv = *(const LAS u32x4*)(cw + st * SLOT);
        const float ga = bf_lo(*(const LAS u16*)(lds0 + LDS_G + st * 128 + lane * 2)), ba = bf_lo(*(const LAS u16*)(lds0 + LDS_B + st * 128 + lane * 2));
#pragma unroll
        for (int i = 0; i < 4; ++i) xr[st * 4 + i] = *(const LAS u32x4*)(sx + st * SLOT + ((ssw ^ i) * 16u));
#pragma unroll
        for (int i = 0; i < 4; ++i) {
          const float l0 = bf_lo(wv[i]), h0 = bf_hi(wv[i]);
          const unsigned pa = cvt_pk(mul1(ga, l0), mul1(ga, h0));
          if constexpr (st == 0) { p1[2 * i] = bf_lo(pa); p1[2 * i + 1] = bf_hi(pa); p2[2 * i] = mul1(ba, l0); p2[2 * i + 1] = mul1(ba, h0); }
          else { p1[2 * i] = add1(p1[2 * i], bf_lo(pa)); p1[2 * i + 1] = add1(p1[2 * i + 1], bf_hi(pa)); p2[2 * i] = fma1(ba, l0, p2[2 * i]); p2[2 * i + 1] = fma1(ba, h0, p2[2 * i + 1]); }
        }
      }
      if constexpr (st == 2) {
        float a[8];
#pragma unroll
        for (int i = 0; i < 8; ++i) {
          auto rr = __builtin_amdgcn_permlane32_swap(__float_as_uint(p1[i]), __float_as_uint(p2[i]), false, false);
          a[i] = __uint_as_float(rr[0]) + __uint_as_float(rr[1]);
        }
        float cs[4];
#pragma unroll
        for (int i = 0; i < 4; ++i) {
          auto rr = __builtin_amdgcn_permlane16_swap(__float_as_uint(a[i]), __float_as_uint(a[4 + i]), false, false);
          cs[i] = sum16(__uint_as_float(rr[0]) + __uint_as_float(rr[1]));
        }
        pub = l4 == 0 ? cs[0] : (l4 == 1 ? cs[1] : (l4 == 2 ? cs[2] : cs[3]));
        epr = (unsigned)__builtin_amdgcn_readlane((int)ep, 0); epc = (unsigned)__builtin_amdgcn_readlane((int)ep, 1);
      }
      if constexpr (st <= 1) { PIN8(p1); PIN8(p2); }
      if constexpr (st == 2) asm volatile("" : "+v"(pub));
      if constexpr (st == 3 || st == 4) {
        constexpr int j0 = (st - 3) * 4;
        f32x4 s4 = {part, 0.f, 0.f, 0.f};
#pragma unroll
        for (int j = j0; j < j0 + 4; ++j) {
          f[2 * j] = (f32x4){bf_lo(xr[j][0]), bf_hi(xr[j][0]), bf_lo(xr[j][1]), bf_hi(xr[j][1])};
          f[2 * j + 1] = (f32x4){bf_lo(xr[j][2]), bf_hi(xr[j][2]), bf_lo(xr[j][3]), bf_hi(xr[j][3])};
#pragma unroll
          for (int e = 0; e < 4; ++e) { s4[e] = add1(s4[e], f[2 * j][e]); s4[e] = add1(s4[e], f[2 * j + 1][e]); }
        }
        part = add1(add1(s4[0], s4[1]), add1(s4[2], s4[3]));
        if constexpr (st == 4) { mean = dpp_add<0xB1>(part) * (1.f / 128.f); part = 0.f; }
        asm volatile("" : "+v"(part), "+v"(mean), "+v"(f[2 * j0]), "+v"(f[2 * j0 + 1]), "+v"(f[2 * j0 + 2]), "+v"(f[2 * j0 + 3]),
                          "+v"(f[2 * j0 + 4]), "+v"(f[2 * j0 + 5]), "+v"(f[2 * j0 + 6]), "+v"(f[2 * j0 + 7]));
      }
      if constexpr (st == 5 || st == 6) {
        constexpr int j0 = (st - 5) * 8;
        f32x4 s4 = {part, 0.f, 0.f, 0.f};
#pragma unroll
        for (int j = j0; j < j0 + 8; ++j)
#pragma unroll
          for (int e = 0; e < 4; ++e) { const float dd = add1(f[j][e], -mean); s4[e] = fma1(dd, dd, s4[e]); }
        part = add1(add1(s4[0], s4[1]), add1(s4[2], s4[3]));
        asm volatile("" : "+v"(part));
      }
      SCHED();
      if constexpr (st == 3) {
        if (l4 < 4) store_granule(ws, WS_C + ((size_t)((ni * 8 + slab) * 128 + scol + ((lane >> 4) & 1) * 4 + l4) * 2 + (lane >> 5)) * 8, epc + 1u, pub);
      }
      if constexpr (st == 7) {
        const float m2 = dpp_add<0xB1>(part);
        store_granule(ws, WS_RS + ((size_t)(mi * 8 + slab) * 256 + t) * 8, epr + 1u, (t & 1) ? m2 : mean);
      }
      if constexpr (st == 8) { if (d < 2) gather(0, 4); }
      if constexpr (st == 9) { if (d < 2) gather(4, 8); }
      if constexpr (st == 11) { if (d >= 2) gather(0, 4); }
      if constexpr (st == 12) { if (d >= 2) gather(4, 8); }
      if constexpr (st == 11) { if (d < 2) { combine(epc + 1u); asm volatile("" : "+v"(v0), "+v"(v1), "+v"(bad)); } }
      if constexpr (st == 14) {
        const unsigned tag = (d < 2 ? epc : epr) + 1u;
        if (d >= 2) combine(tag);
        bool ok = bad == 0u;
        if (__builtin_expect(!__all(ok), 0)) {
#pragma unroll 1
          for (int it = 0; it < 20000; ++it) {
            asm volatile("" ::: "memory");
            gather(0, 8);
            combine(tag);
            ok = bad == 0u;
            if (__all(ok)) break;
            __builtin_amdgcn_s_sleep(2);
          }
        }
        if (__builtin_expect(!__all(ok), 0)) {
          v0 = 0.f; v1 = 0.f;
          if (d < 2) {
            const u16* wc = w + ni * 128 + t;
#pragma unroll 1
            for (int k = 0; k < KD; ++k) {
              const float wv = bf_lo(wc[(size_t)k * ND]);
              v0 += bf_lo(cvt_pk(bf_lo(g[k]) * wv, 0.f)); v1 = fmaf(bf_lo(b[k]), wv, v1);
            }
          } else {
            const u16* xrow = x + (size_t)(mi * 128 + (t - 128)) * KD;
#pragma unroll 1
            for (int k = 0; k < KD; ++k) v0 += bf_lo(xrow[k]);
            v0 *= (1.f / 1024.f);
#pragma unroll 1
            for (int k = 0; k < KD; ++k) { const float dd = bf_lo(xrow[k]) - v0; v1 = fmaf(dd, dd, v1); }
            v1 = __builtin_amdgcn_rsqf(v1 * (1.f / 1024.f) + 1e-5f);
          }
        }
        if (d < 2) { *(LAS float*)(lds0 + LDS_TAB + 1024 + t * 4) = v0; *(LAS float*)(lds0 + LDS_TAB + 1536 + t * 4) = v1; }
        else *(LAS f32x2*)(lds0 + LDS_TAB + (t - 128) * 8) = (f32x2){v0, v1};
      }
      if constexpr (st == 14) { STAMP(23); BARRIER_L(); } else BARRIER_N();
      STAMP(3 + st);
    });
    if (t == 0) {
      __hip_atomic_fetch_add((GAS unsigned*)(ws + WS_EPR + mi * 128), 1u, __ATOMIC_RELAXED, __HIP_MEMORY_SCOPE_AGENT);
      __hip_atomic_fetch_add((GAS unsigned*)(ws + WS_EPC + ni * 128), 1u, __ATOMIC_RELAXED, __HIP_MEMORY_SCOPE_AGENT);
    }
  } else if (wave >= 4) {
    const int d = wave - 4;
    if (PRIO_L) __builtin_amdgcn_s_setprio(PRIO_L);
    unsigned xo[4], wo[4];
#pragma unroll
    for (int i = 0; i < 4; ++i) {
      const int row = i * 8 + (lane >> 3);
      xo[i] = (unsigned)row * 2048u + (unsigned)(((lane & 7) ^ ((row >> 1) & 7)) * 16) - (unsigned)i * 1024u;
      const int kr = i * 4 + (lane >> 4);
      wo[i] = (unsigned)kr * 2048u + (unsigned)(((lane & 15) ^ (((lane >> 4) << 2) | i)) * 16) - (unsigned)i * 1024u;
    }
    const unsigned dst = lds0 + (unsigned)d * 4096u;
    const char* xb = (const char*)x + (size_t)mi * 262144 + d * 65536;
    const char* wb = (const char*)w + d * 32768 + ni * 256;
    auto issue = [&](int st) {
      const unsigned ds_ = dst + (unsigned)(st & (RING - 1)) * SLOT;
      const int ks_ = (st + rot) & (NST - 1);
      glds16x4(wo[0], wo[1], wo[2], wo[3], wb + (size_t)ks_ * 131072, ds_ + 16384u);
      glds16x4(xo[0], xo[1], xo[2], xo[3], xb + ks_ * 128, ds_);
    };
    glds4((unsigned)lane * 4u, (const char*)g + ((d * 512 + rot * 128) & 2047), lds0 + LDS_G + (unsigned)d * 512u);
    glds4((unsigned)lane * 4u, (const char*)g + ((d * 512 + 256 + rot * 128) & 2047), lds0 + LDS_G + (unsigned)d * 512u + 256u);
    if (d == 0) glds4((unsigned)lane * 4u, (const char*)b + rot * 128, lds0 + LDS_B);
    issue(0); issue(1);
    STAMP(2);
    wait_vm<8>();
    STAMP(3);
    BARRIER_N();
#pragma unroll 1
    for (int j = 0; j < NST - 2; ++j) {
      issue(j + 2);
      wait_vm<8>();
      BARRIER_N();
      STAMP_DYN(j + 4);
    }
    wait_vm<0>();
    BARRIER_N();
    STAMP(18);
  } else {
    const int wn = wave;
    {
      const unsigned co = (unsigned)wn * 3072u + (unsigned)lane * 16u, lim = (code_len & ~15u) - 16u;
      const u32x4 t0 = *(const GAS u32x4*)(code_pc + min(co, lim)) ^ *(const GAS u32x4*)(code_pc + min(co + 1024u, lim)) ^ *(const GAS u32x4*)(code_pc + min(co + 2048u, lim));
      asm volatile("" :: "v"(t0));
    }
    STAMP(3);
    if (PRIO_M) __builtin_amdgcn_s_setprio(PRIO_M);
    const int r = lane & 31, h = lane >> 5, sw = (r >> 1) & 7;
    unsigned offx[4], offw[2];
#pragma unroll
    for (int ks = 0; ks < 4; ++ks) offx[ks] = lds0 + (unsigned)r * 128u + (unsigned)(((2 * ks + h) ^ sw) * 16);
    {
      const int blk = (lane >> 4) & 1, q = (lane & 15) >> 2, p = lane & 3;
#pragma unroll
      for (int tt = 0; tt < 2; ++tt) {
        const int row = 8 * h + 4 * tt + q, ch = 4 * wn + blk + 2 * (p & 1);
        offw[tt] = lds0 + 16384u + (unsigned)(256 * row + 16 * (ch ^ ((q << 2) | ((2 * h + tt) & 3))) + 8 * (p >> 1));
      }
    }
    unsigned offg = lds0 + LDS_G + (unsigned)h * 16u;
    const unsigned li = lane & 3;
    const unsigned mk0 = li == 0 ? 0xffffu : (li == 1 ? 0xffff0000u : 0u), mk1 = li == 2 ? 0xffffu : (li == 3 ? 0xffff0000u : 0u);
    f32x16 acc[4] = {};
    bf16x8 xf[4];
    s16x4 wra[2], wrb[2];
    u32x4 gq, dg;
    u32x4 wf, wfn;
    f32x4 d0, d1;
    const f32x4 z4 = {0.f, 0.f, 0.f, 0.f};
#define RX(mt, ks) xf[mt] = *(const LAS bf16x8*)(offx[ks] + (mt) * 4096)
#define RW(R, tt, ks) R[tt] = __builtin_amdgcn_ds_read_tr16_b64_v4i16((LAS s16x4*)(offw[tt] + (ks) * 4096))
#define RG(o) gq = *(const LAS u32x4*)(offg + (o) * 32)
#define DG0() do { dg[0] = gq[0] & mk[0]; dg[1] = gq[1] & mk[1]; } while (0)
#define DG1() do { dg[2] = gq[2] & mk[0]; dg[3] = gq[3] & mk[1]; } while (0)
#define SM0(R) d0 = __builtin_amdgcn_mfma_f32_4x4x4bf16_1k(__builtin_bit_cast(s16x4, (u32x2){dg[0], dg[1]}), R[0], z4, 0, 0, 0)
#define SM1(R) d1 = __builtin_amdgcn_mfma_f32_4x4x4bf16_1k(__builtin_bit_cast(s16x4, (u32x2){dg[2], dg[3]}), R[1], z4, 0, 0, 0)
#define CV0(W) do { W[0] = cvt_pk(d0[0], d0[1]); W[1] = cvt_pk(d0[2], d0[3]); } while (0)
#define CV1(W) do { W[2] = cvt_pk(d1[0], d1[1]); W[3] = cvt_pk(d1[2], d1[3]); } while (0)
#define MM(mt, W) acc[mt] = __builtin_amdgcn_mfma_f32_32x32x16_bf16(__builtin_bit_cast(bf16x8, W), xf[mt], acc[mt], 0, 0, 0)
#define STEP(WC, WN, RA, RB, kx, kw, kg, EXTRA2, EXTRA3) do { \
      SM0(RA); SM1(RA); SCHED(); \
      MM(0, WC); SCHED(); RX(0, kx); RW(RB, 0, kw); DG0(); SCHED(); \
      MM(1, WC); SCHED(); RX(1, kx); RW(RB, 1, kw); DG1(); SCHED(); \
      MM(2, WC); SCHED(); RX(2, kx); RG(kg); CV0(WN); EXTRA2; SCHED(); \
      MM(3, WC); SCHED(); RX(3, kx); CV1(WN); EXTRA3; SCHED(); } while (0)
    unsigned mk[2] = {mk0, mk1};
    asm volatile("" : "+v"(acc[0]), "+v"(acc[1]), "+v"(acc[2]), "+v"(acc[3]));
    asm volatile("" : "+v"(offx[0]), "+v"(offx[1]), "+v"(offx[2]), "+v"(offx[3]), "+v"(offw[0]), "+v"(offw[1]), "+v"(offg), "+v"(mk[0]), "+v"(mk[1]));
    __builtin_amdgcn_s_waitcnt(0xC07F);
    STAMP(4);
    BARRIER_N();
    STAMP(5);
    RG(0); RW(wrb, 0, 0); RW(wrb, 1, 0); RX(0, 0); RW(wra, 0, 1); RW(wra, 1, 1); RX(1, 0); RX(2, 0); RX(3, 0);
    DG0(); DG1(); SCHED(); RG(1); SM0(wrb); SM1(wrb); SCHED();
    CV0(wf); CV1(wf); DG0(); DG1(); SCHED(); RG(2); SCHED();
#pragma unroll 1
    for (int s = 0; s < NST - 1; ++s) {
      const int dlt = ((s & (RING - 1)) == RING - 1) ? -(RING - 1) * SLOT : SLOT;
      STEP(wf, wfn, wra, wrb, 1, 2, 3, (void)0, (void)0);
      STEP(wfn, wf, wrb, wra, 2, 3, 4, (offx[0] += dlt, offw[0] += dlt), (offw[1] += dlt, offx[1] += dlt));
      BARRIER_N();
      STEP(wf, wfn, wra, wrb, 3, 0, 5, (offx[2] += dlt), (void)0);
      STEP(wfn, wf, wrb, wra, 0, 1, 6, (offx[3] += dlt), (offg += 128u));
    }
    f32x4 c1v[4], c2v[4];
    f32x2 rs[4];
    const unsigned tc = lds0 + LDS_TAB + 1024 + (unsigned)(wn * 32 + h * 16) * 4u, tr = lds0 + LDS_TAB + (unsigned)r * 8u;
#define RC(q) do { c1v[q] = *(const LAS f32x4*)(tc + (q) * 16); c2v[q] = *(const LAS f32x4*)(tc + 512 + (q) * 16); rs[q] = *(const LAS f32x2*)(tr + (q) * 256); } while (0)
    STEP(wf, wfn, wra, wrb, 1, 2, 3, (void)0, (void)0);
    STEP(wfn, wf, wrb, wra, 2, 3, 4, (void)0, (void)0);
    SM0(wra); SM1(wra); SCHED();
    MM(0, wf); SCHED(); RX(0, 3); SCHED();
    MM(1, wf); SCHED(); RX(1, 3); SCHED();
    MM(2, wf); SCHED(); RX(2, 3); CV0(wfn); SCHED();
    MM(3, wf); SCHED(); RX(3, 3); CV1(wfn); SCHED();
    MM(0, wfn); SCHED(); RC(0); SCHED(); MM(1, wfn); SCHED(); RC(1); SCHED(); MM(2, wfn); SCHED(); RC(2); SCHED(); MM(3, wfn); SCHED(); RC(3); SCHED();
    STAMP(20);
    const unsigned so = lds0 + (unsigned)r * 256u, sc0 = (unsigned)(((wn * 4 + h * 2) ^ (r & 7)) * 16), sc1 = sc0 ^ 16u;
#pragma unroll
    for (int mt = 0; mt < 4; ++mt) {
      const float nm = -rs[mt][0], rstd = rs[mt][1];
      f32x4 tq[4];
#pragma unroll
      for (int q = 0; q < 4; ++q) {
        const f32x4 av = {acc[mt][4 * q], acc[mt][4 * q + 1], acc[mt][4 * q + 2], acc[mt][4 * q + 3]};
        tq[q] = av + c1v[q] * nm;
      }
#pragma unroll
      for (int q = 0; q < 4; ++q) tq[q] = tq[q] * rstd + c2v[q];
      const u32x4 o0 = {cvt_pk(tq[0][0], tq[0][1]), cvt_pk(tq[0][2], tq[0][3]), cvt_pk(tq[1][0], tq[1][1]), cvt_pk(tq[1][2], tq[1][3])};
      const u32x4 o1 = {cvt_pk(tq[2][0], tq[2][1]), cvt_pk(tq[2][2], tq[2][3]), cvt_pk(tq[3][0], tq[3][1]), cvt_pk(tq[3][2], tq[3][3])};
      *(LAS u32x4*)(so + mt * 8192 + sc0) = o0;
      *(LAS u32x4*)(so + mt * 8192 + sc1) = o1;
      SCHED();
      if (mt == 1) BARRIER_L();
    }
  }
  if (wave >= 4) {
    BARRIER_N();
    store_rows<2>(lds0, (wave - 4) * 8, lane, (char*)out + (size_t)mi * 262144 + ni * 256);
  }
  STAMP(24);
  BARRIER_L();
  STAMP(25);
  if (wave < 8) store_rows<2>(lds0, 64 + wave * 8, lane, (char*)out + (size_t)mi * 262144 + ni * 256);
  STAMP_FLUSH();
  asm volatile("lnlin_code_end:");
}

extern "C" void kernel_launch(void* const* d_in, const int* in_sizes, int n_in, void* d_out, int out_size, void* d_ws, size_t ws_size,
                              hipStream_t stream) {
  const u16* x = (const u16*)d_in[0]; const u16* g = (const u16*)d_in[1]; const u16* b = (const u16*)d_in[2]; const u16* w = (const u16*)d_in[3];
  (void)hipFuncSetAttribute((const void*)lnlin_kernel, hipFuncAttributeMaxDynamicSharedMemorySize, LDS_TOTAL);
  lnlin_kernel<<<256, 768, LDS_TOTAL, stream>>>(x, g, b, w, (u16*)d_out, (char*)d_ws);
}
```

```cpp
#include <hip/hip_runtime.h>
#include <hip/hip_bf16.h>
#include <utility>
#pragma clang diagnostic ignored "-Wint-to-pointer-cast"

typedef short bf16x8 __attribute__((ext_vector_type(8)));
typedef short s16x4 __attribute__((ext_vector_type(4)));
typedef float f32x16 __attribute__((ext_vector_type(16)));
typedef float f32x4 __attribute__((ext_vector_type(4)));
typedef float f32x2 __attribute__((ext_vector_type(2)));
typedef unsigned u32x4 __attribute__((ext_vector_type(4)));
typedef unsigned u32x2 __attribute__((ext_vector_type(2)));
typedef __bf16 bf16x2_t __attribute__((ext_vector_type(2)));
typedef unsigned short u16;
typedef unsigned long long u64;
#define GAS __attribute__((address_space(1)))
#define LAS __attribute__((address_space(3)))

#define ST_FLAVOR "nt"
#define SIDE_START 2
#define PRIO_M 2
#define PRIO_L 0

constexpr int KD = 1024, ND = 1024;
constexpr int NST = 16;
constexpr int RING = 4;
constexpr int SLOT = 32768;
constexpr int LDS_G = RING * SLOT;
constexpr int LDS_B = LDS_G + 2048 + 64;
constexpr int LDS_TAB = LDS_B + 256;
constexpr int LDS_TOTAL = LDS_TAB + 2048;

constexpr size_t WS_EPR = 0;
constexpr size_t WS_EPC = 32 * 128;
constexpr size_t WS_RS = 8192;
constexpr size_t WS_C = WS_RS + 4096 * 2 * 8;

#define STAMP_DECL
#define STAMP_RT(i)
#define STAMP(i)
#define STAMP_DYN(i)
#define STAMP_FLUSH()

template <int B, int E, class F> __device__ __forceinline__ void static_for(F&& f) {
  if constexpr (B < E) { f(std::integral_constant<int, B>{}); static_for<B + 1, E>(f); }
}
__device__ __forceinline__ float bf_lo(unsigned u) { return __uint_as_float(u << 16); }
__device__ __forceinline__ float bf_hi(unsigned u) { return __uint_as_float(u & 0xffff0000u); }
__device__ __forceinline__ unsigned cvt_pk(float lo, float hi) {
  f32x2 v = {lo, hi}; return __builtin_bit_cast(unsigned, __builtin_convertvector(v, bf16x2_t));
}
template <int CTRL> __device__ __forceinline__ float dpp_add(float x) {
  return x + __builtin_bit_cast(float, __builtin_amdgcn_update_dpp(0, __builtin_bit_cast(int, x), CTRL, 0xf, 0xf, true));
}
__device__ __forceinline__ float sum16(float x) {
  x = dpp_add<0x128>(x); x = dpp_add<0x124>(x); x = dpp_add<0x122>(x); x = dpp_add<0x121>(x);
  return x;
}
#define SCHED() __builtin_amdgcn_sched_barrier(0)
#define BARRIER_L() do { SCHED(); asm volatile("s_waitcnt lgkmcnt(0)\n\ts_barrier" ::: "memory"); SCHED(); } while (0)
#define BARRIER_N() do { SCHED(); asm volatile("s_barrier" ::: "memory"); SCHED(); } while (0)
template <int N> __device__ __forceinline__ void wait_vm() { asm volatile("s_waitcnt vmcnt(%0)" ::"n"(N) : "memory"); }

__device__ __forceinline__ void glds16x4(unsigned v0, unsigned v1, unsigned v2, unsigned v3, const void* sbase, unsigned lds_dst) {
  unsigned keep;
  asm volatile("s_mov_b32 %0, m0\n\ts_mov_b32 m0, %6\n\ts_nop 0\n\t"
               "global_load_lds_dwordx4 %1, %5\n\tglobal_load_lds_dwordx4 %2, %5 offset:1024\n\t"
               "global_load_lds_dwordx4 %3, %5 offset:2048\n\tglobal_load_lds_dwordx4 %4, %5 offset:3072\n\ts_mov_b32 m0, %0"
               : "=&s"(keep) : "v"(v0), "v"(v1), "v"(v2), "v"(v3), "s"(sbase), "s"(lds_dst) : "memory");
}
__device__ __forceinline__ void glds4(unsigned v0, const void* sbase, unsigned lds_dst) {
  unsigned keep;
  asm volatile("s_mov_b32 %0, m0\n\ts_mov_b32 m0, %3\n\ts_nop 0\n\tglobal_load_lds_dword %1, %2\n\ts_mov_b32 m0, %0"
               : "=&s"(keep) : "v"(v0), "s"(sbase), "s"(lds_dst) : "memory");
}
template <int OFF> __device__ __forceinline__ u32x4 gload16_asm(unsigned voff, const void* sbase) {
  u32x4 r; asm volatile("global_load_dwordx4 %0, %1, %2 offset:%3" : "=v"(r) : "v"(voff), "s"(sbase), "n"(OFF) : "memory"); return r;
}
__device__ __forceinline__ unsigned gload_u32_sc1_asm(unsigned voff, const void* sbase) {
  unsigned r; asm volatile("global_load_dword %0, %1, %2 sc1" : "=v"(r) : "v"(voff), "s"(sbase) : "memory"); return r;
}
template <int OFF> __device__ __forceinline__ u32x2 gload_u64_sc1_asm(unsigned voff, const void* sbase) {
  u32x2 r; asm volatile("global_load_dwordx2 %0, %1, %2 offset:%3 sc1" : "=v"(r) : "v"(voff), "s"(sbase), "n"(OFF) : "memory"); return r;
}
__device__ __forceinline__ void store_granule(char* ws, size_t off, unsigned tag, float v) {
  __hip_atomic_store((GAS u64*)(ws + off), ((u64)tag << 32) | __float_as_uint(v), __ATOMIC_RELAXED, __HIP_MEMORY_SCOPE_AGENT);
}
__device__ __forceinline__ u64 load_granule(const char* ws, size_t off) {
  return __hip_atomic_load((GAS u64*)(ws + off), __ATOMIC_RELAXED, __HIP_MEMORY_SCOPE_AGENT);
}
__device__ __forceinline__ unsigned load_word(const char* ws, size_t off) {
  return __hip_atomic_load((GAS unsigned*)(ws + off), __ATOMIC_RELAXED, __HIP_MEMORY_SCOPE_AGENT);
}

template <int N> __device__ __forceinline__ void store_rows(unsigned lds0, int rowb, int lane, char* ob) {
  const int row0 = rowb + (lane >> 4), ch = lane & 15;
  u32x4 v[N];
#pragma unroll
  for (int i = 0; i < N; ++i) { const int row = row0 + i * 4; v[i] = *(const LAS u32x4*)(lds0 + row * 256 + ((ch ^ (row & 7)) * 16)); }
  const unsigned oo = (unsigned)row0 * 2048u + (unsigned)ch * 16u;
#pragma unroll
  for (int i = 0; i < N; ++i)
    asm volatile("global_store_dwordx4 %0, %1, %2 " ST_FLAVOR "\n\ts_nop 1" :: "v"(oo + i * 8192u), "v"(v[i]), "s"(ob) : "memory");
}

__global__ __launch_bounds__(768, 3) void lnlin_kernel(const u16* __restrict__ x, const u16* __restrict__ g, const u16* __restrict__ b,
                                                       const u16* __restrict__ w, u16* __restrict__ out, char* ws) {
  u64 code_pc; unsigned code_len;
  asm volatile("s_getpc_b64 %0\nlnlin_code_begin:\n\ts_mov_b32 %1, lnlin_code_end-lnlin_code_begin" : "=s"(code_pc), "=s"(code_len));
  extern __shared__ __attribute__((aligned(16))) char smem_g[];
  const int bid = blockIdx.x, xcd = bid & 7, idx = bid >> 3;
  const int mi = (xcd >> 1) * 8 + (idx >> 2), ni = (xcd & 1) * 4 + (idx & 3);
  const int rot = (mi >> 2) * 2;
  const int tid = threadIdx.x, lane = tid & 63;
  const int wave = __builtin_amdgcn_readfirstlane(tid >> 6);
  const unsigned lds0 = (unsigned)(size_t)(LAS char*)smem_g;
  STAMP_DECL STAMP_RT(0); STAMP(1);

  if (wave >= 8) {
    const int d = wave - 8, t = tid - 512;
    const int scol = (mi & 3) * 32 + d * 8;
    const unsigned cw = lds0 + 16384u + (unsigned)(256 * lane + 16 * ((scol >> 3) ^ (((lane & 3) << 2) | ((lane >> 2) & 3))));
    const int srow = mi * 128 + ni * 16 + d * 4 + (lane >> 4), l4 = lane & 15;
    const char* xs = (const char*)x + (size_t)srow * 2048 + l4 * 16;
    u32x4 xr[8];
    unsigned ep;
    float p1[8], p2[8], part = 0.f, mean = 0.f, pub = 0.f, v0 = 0.f, v1 = 0.f;
    unsigned bad = 0u;
    u32x4 q[8];
    const char* gsrc = d < 2 ? ws + WS_C + (size_t)ni * 16384 + (size_t)t * 16 : ws + WS_RS + (size_t)mi * 2048 + (size_t)(t - 128) * 16;
    auto gather = [&](int j0, int j1) {
#pragma unroll
      for (int j = j0; j < j1; ++j)
        if (j == 0 || d < 2) q[j] = __builtin_amdgcn_raw_buffer_load_b128(__builtin_amdgcn_make_buffer_rsrc((void*)ws, 0, 0x40000, 0x00020000),
                                                                         (unsigned)(gsrc - ws) + j * 2048u, 0, 16  );
    };
    ep = load_word(ws, (lane & 1) ? WS_EPC + ni * 128 : WS_EPR + mi * 128);
    STAMP(2);
    BARRIER_N();
    unsigned epr = 0, epc = 0;
    static_for<0, NST - 1>([&](auto S) {
      constexpr int st = S;
      if constexpr (st <= 1) {
        const u32x4 wv = *(const LAS u32x4*)(cw + st * SLOT);
        const float ga = bf_lo(*(const LAS u16*)(lds0 + LDS_G + st * 128 + lane * 2)), ba = bf_lo(*(const LAS u16*)(lds0 + LDS_B + st * 128 + lane * 2));
#pragma unroll
        for (int i = 0; i < 4; ++i) {
          const float l0 = bf_lo(wv[i]), h0 = bf_hi(wv[i]);
          const unsigned pa = cvt_pk(ga * l0, ga * h0);
          if constexpr (st == 0) { p1[2 * i] = bf_lo(pa); p1[2 * i + 1] = bf_hi(pa); p2[2 * i] = ba * l0; p2[2 * i + 1] = ba * h0; }
          else { p1[2 * i] += bf_lo(pa); p1[2 * i + 1] += bf_hi(pa); p2[2 * i] = fmaf(ba, l0, p2[2 * i]); p2[2 * i + 1] = fmaf(ba, h0, p2[2 * i + 1]); }
        }
      }
      if constexpr (st == 2) {
        float a[8];
#pragma unroll
        for (int i = 0; i < 8; ++i) {
          auto rr = __builtin_amdgcn_permlane32_swap(__float_as_uint(p1[i]), __float_as_uint(p2[i]), false, false);
          a[i] = __uint_as_float(rr[0]) + __uint_as_float(rr[1]);
        }
        float cs[4];
#pragma unroll
        for (int i = 0; i < 4; ++i) {
          auto rr = __builtin_amdgcn_permlane16_swap(__float_as_uint(a[i]), __float_as_uint(a[4 + i]), false, false);
          cs[i] = sum16(__uint_as_float(rr[0]) + __uint_as_float(rr[1]));
        }
        pub = l4 == 0 ? cs[0] : (l4 == 1 ? cs[1] : (l4 == 2 ? cs[2] : cs[3]));
        epr = (unsigned)__builtin_amdgcn_readlane((int)ep, 0); epc = (unsigned)__builtin_amdgcn_readlane((int)ep, 1);
      }
      if constexpr (st >= 3 && st <= 5) {
        constexpr int j0 = st == 3 ? 0 : (st == 4 ? 2 : 5), j1 = st == 3 ? 2 : (st == 4 ? 5 : 8);
        float s0 = part, s1 = 0.f, s2 = 0.f, s3 = 0.f;
#pragma unroll
        for (int j = j0; j < j1; ++j) {
          s0 += bf_lo(xr[j][0]); s1 += bf_hi(xr[j][0]); s2 += bf_lo(xr[j][1]); s3 += bf_hi(xr[j][1]);
          s0 += bf_lo(xr[j][2]); s1 += bf_hi(xr[j][2]); s2 += bf_lo(xr[j][3]); s3 += bf_hi(xr[j][3]);
        }
        part = (s0 + s1) + (s2 + s3);
        if constexpr (st == 5) { mean = sum16(part) * (1.f / 1024.f); part = 0.f; }
      }
      if constexpr (st >= 6 && st <= 8) {
        constexpr int j0 = (st - 6) * 3, j1 = st == 8 ? 8 : j0 + 3;
        float q0 = part, q1 = 0.f, q2 = 0.f, q3 = 0.f;
#pragma unroll
        for (int j = j0; j < j1; ++j)
#pragma unroll
          for (int i = 0; i < 4; i += 2) {
            const float d0 = bf_lo(xr[j][i]) - mean, d1 = bf_hi(xr[j][i]) - mean, d2 = bf_lo(xr[j][i + 1]) - mean, d3 = bf_hi(xr[j][i + 1]) - mean;
            q0 = fmaf(d0, d0, q0); q1 = fmaf(d1, d1, q1); q2 = fmaf(d2, d2, q2); q3 = fmaf(d3, d3, q3);
          }
        part = (q0 + q1) + (q2 + q3);
        if constexpr (st == 8) {
          const float var = sum16(part) * (1.f / 1024.f);
          const float rstd = __builtin_amdgcn_rsqf(var + 1e-5f);
          if (l4 < 2) store_granule(ws, WS_RS + ((size_t)srow * 2 + (lane & 1)) * 8, epr + 1u, (lane & 1) ? rstd : mean);
        }
      }
      SCHED();
      if constexpr (st <= 3) {
#pragma unroll
        for (int j = 2 * st; j < 2 * st + 2; ++j) xr[j] = *(const GAS u32x4*)(xs + (((j + 2 + (rot >> 1)) & 7) * 256));
      }
      if constexpr (st == 3) {
        if (l4 < 4) store_granule(ws, WS_C + ((size_t)((ni * 8 + (mi >> 2)) * 128 + scol + ((lane >> 4) & 1) * 4 + l4) * 2 + (lane >> 5)) * 8, epc + 1u, pub);
      }
      if constexpr (st == 9) { if (d < 2) gather(4, 8); }
      if constexpr (st == 10) { if (d < 2) gather(0, 4); }
      if constexpr (st == 12) { if (d >= 2) gather(0, 1); }
      if constexpr (st == 12 || st == 13) {
        if (d < 2) {
          const unsigned tag = epc + 1u;
#pragma unroll
          for (int j = (st == 12 ? 4 : 0); j < (st == 12 ? 8 : 4); ++j) {
            bad |= (q[j][1] ^ tag) | (q[j][3] ^ tag); v0 += __uint_as_float(q[j][0]); v1 += __uint_as_float(q[j][2]);
          }
        }
      }
      if constexpr (st == 14) {
        const unsigned tag = (d < 2 ? epc : epr) + 1u;
        if (d >= 2) { bad = (q[0][1] ^ tag) | (q[0][3] ^ tag); v0 = __uint_as_float(q[0][0]); v1 = __uint_as_float(q[0][2]); }
        bool ok = bad == 0u;
        if (__builtin_expect(!__all(ok), 0)) {
#pragma unroll 1
          for (int it = 0; it < 20000; ++it) {
            asm volatile("" ::: "memory");
            gather(0, 8);
            bad = (q[0][1] ^ tag) | (q[0][3] ^ tag); v0 = __uint_as_float(q[0][0]); v1 = __uint_as_float(q[0][2]);
            if (d < 2) {
              v0 = 0.f; v1 = 0.f;
#pragma unroll
              for (int jj = 0; jj < 8; ++jj) { const int j = jj ^ 4; bad |= (q[j][1] ^ tag) | (q[j][3] ^ tag); v0 += __uint_as_float(q[j][0]); v1 += __uint_as_float(q[j][2]); }
            }
            ok = bad == 0u;
            if (__all(ok)) break;
            __builtin_amdgcn_s_sleep(2);
          }
        }
        if (__builtin_expect(!__all(ok), 0)) {
          v0 = 0.f; v1 = 0.f;
          if (d < 2) {
            const u16* wc = w + ni * 128 + t;
#pragma unroll 1
            for (int k = 0; k < KD; ++k) {
              const float wv = bf_lo(wc[(size_t)k * ND]);
              v0 += bf_lo(cvt_pk(bf_lo(g[k]) * wv, 0.f)); v1 = fmaf(bf_lo(b[k]), wv, v1);
            }
          } else {
            const u16* xrow = x + (size_t)(mi * 128 + (t - 128)) * KD;
#pragma unroll 1
            for (int k = 0; k < KD; ++k) v0 += bf_lo(xrow[k]);
            v0 *= (1.f / 1024.f);
#pragma unroll 1
            for (int k = 0; k < KD; ++k) { const float dd = bf_lo(xrow[k]) - v0; v1 = fmaf(dd, dd, v1); }
            v1 = __builtin_amdgcn_rsqf(v1 * (1.f / 1024.f) + 1e-5f);
          }
        }
        if (d < 2) { *(LAS float*)(lds0 + LDS_TAB + 1024 + t * 4) = v0; *(LAS float*)(lds0 + LDS_TAB + 1536 + t * 4) = v1; }
        else *(LAS f32x2*)(lds0 + LDS_TAB + (t - 128) * 8) = (f32x2){v0, v1};
      }
      if constexpr (st == 14) { STAMP(23); BARRIER_L(); } else BARRIER_N();
      STAMP(3 + st);
    });
    if (t == 0) {
      __hip_atomic_fetch_add((GAS unsigned*)(ws + WS_EPR + mi * 128), 1u, __ATOMIC_RELAXED, __HIP_MEMORY_SCOPE_AGENT);
      __hip_atomic_fetch_add((GAS unsigned*)(ws + WS_EPC + ni * 128), 1u, __ATOMIC_RELAXED, __HIP_MEMORY_SCOPE_AGENT);
    }
  } else if (wave >= 4) {
    const int d = wave - 4;
    if (PRIO_L) __builtin_amdgcn_s_setprio(PRIO_L);
    unsigned xo[4], wo[4];
#pragma unroll
    for (int i = 0; i < 4; ++i) {
      const int row = i * 8 + (lane >> 3);
      xo[i] = (unsigned)row * 2048u + (unsigned)(((lane & 7) ^ ((row >> 1) & 7)) * 16) - (unsigned)i * 1024u;
      const int kr = i * 4 + (lane >> 4);
      wo[i] = (unsigned)kr * 2048u + (unsigned)(((lane & 15) ^ (((lane >> 4) << 2) | i)) * 16) - (unsigned)i * 1024u;
    }
    const unsigned dst = lds0 + (unsigned)d * 4096u;
    const char* xb = (const char*)x + (size_t)mi * 262144 + d * 65536;
    const char* wb = (const char*)w + d * 32768 + ni * 256;
    auto issue = [&](int st) {
      const unsigned ds_ = dst + (unsigned)(st & (RING - 1)) * SLOT;
      const int ks_ = (st + rot) & (NST - 1);
      glds16x4(wo[0], wo[1], wo[2], wo[3], wb + (size_t)ks_ * 131072, ds_ + 16384u);
      glds16x4(xo[0], xo[1], xo[2], xo[3], xb + ks_ * 128, ds_);
    };
    glds4((unsigned)lane * 4u, (const char*)g + ((d * 512 + rot * 128) & 2047), lds0 + LDS_G + (unsigned)d * 512u);
    glds4((unsigned)lane * 4u, (const char*)g + ((d * 512 + 256 + rot * 128) & 2047), lds0 + LDS_G + (unsigned)d * 512u + 256u);
    if (d == 0) glds4((unsigned)lane * 4u, (const char*)b + rot * 128, lds0 + LDS_B);
    issue(0); issue(1);
    STAMP(2);
    wait_vm<8>();
    STAMP(3);
    BARRIER_N();
#pragma unroll 1
    for (int j = 0; j < NST - 2; ++j) {
      issue(j + 2);
      wait_vm<8>();
      BARRIER_N();
      STAMP_DYN(j + 4);
    }
    wait_vm<0>();
    BARRIER_N();
    STAMP(18);
  } else {
    const int wn = wave;
    {
      const unsigned co = (unsigned)wn * 3072u + (unsigned)lane * 16u, lim = (code_len & ~15u) - 16u;
      const u32x4 t0 = *(const GAS u32x4*)(code_pc + min(co, lim)) ^ *(const GAS u32x4*)(code_pc + min(co + 1024u, lim)) ^ *(const GAS u32x4*)(code_pc + min(co + 2048u, lim));
      asm volatile("" :: "v"(t0));
    }
    STAMP(3);
    if (PRIO_M) __builtin_amdgcn_s_setprio(PRIO_M);
    const int r = lane & 31, h = lane >> 5, sw = (r >> 1) & 7;
    unsigned offx[4], offw[2];
#pragma unroll
    for (int ks = 0; ks < 4; ++ks) offx[ks] = lds0 + (unsigned)r * 128u + (unsigned)(((2 * ks + h) ^ sw) * 16);
    {
      const int blk = (lane >> 4) & 1, q = (lane & 15) >> 2, p = lane & 3;
#pragma unroll
      for (int tt = 0; tt < 2; ++tt) {
        const int row = 8 * h + 4 * tt + q, ch = 4 * wn + blk + 2 * (p & 1);
        offw[tt] = lds0 + 16384u + (unsigned)(256 * row + 16 * (ch ^ ((q << 2) | ((2 * h + tt) & 3))) + 8 * (p >> 1));
      }
    }
    unsigned offg = lds0 + LDS_G + (unsigned)h * 16u;
    const unsigned li = lane & 3;
    const unsigned mk0 = li == 0 ? 0xffffu : (li == 1 ? 0xffff0000u : 0u), mk1 = li == 2 ? 0xffffu : (li == 3 ? 0xffff0000u : 0u);
    f32x16 acc[4] = {};
    bf16x8 xf[4];
    s16x4 wra[2], wrb[2];
    u32x4 gq, dg;
    u32x4 wf, wfn;
    f32x4 d0, d1;
    const f32x4 z4 = {0.f, 0.f, 0.f, 0.f};
#define RX(mt, ks) xf[mt] = *(const LAS bf16x8*)(offx[ks] + (mt) * 4096)
#define RW(R, tt, ks) R[tt] = __builtin_amdgcn_ds_read_tr16_b64_v4i16((LAS s16x4*)(offw[tt] + (ks) * 4096))
#define RG(o) gq = *(const LAS u32x4*)(offg + (o) * 32)
#define DG0() do { dg[0] = gq[0] & mk[0]; dg[1] = gq[1] & mk[1]; } while (0)
#define DG1() do { dg[2] = gq[2] & mk[0]; dg[3] = gq[3] & mk[1]; } while (0)
#define SM0(R) d0 = __builtin_amdgcn_mfma_f32_4x4x4bf16_1k(__builtin_bit_cast(s16x4, (u32x2){dg[0], dg[1]}), R[0], z4, 0, 0, 0)
#define SM1(R) d1 = __builtin_amdgcn_mfma_f32_4x4x4bf16_1k(__builtin_bit_cast(s16x4, (u32x2){dg[2], dg[3]}), R[1], z4, 0, 0, 0)
#define CV0(W) do { W[0] = cvt_pk(d0[0], d0[1]); W[1] = cvt_pk(d0[2], d0[3]); } while (0)
#define CV1(W) do { W[2] = cvt_pk(d1[0], d1[1]); W[3] = cvt_pk(d1[2], d1[3]); } while (0)
#define MM(mt, W) acc[mt] = __builtin_amdgcn_mfma_f32_32x32x16_bf16(__builtin_bit_cast(bf16x8, W), xf[mt], acc[mt], 0, 0, 0)
#define STEP(WC, WN, RA, RB, kx, kw, kg, EXTRA2, EXTRA3) do { \
      SM0(RA); SM1(RA); SCHED(); \
      MM(0, WC); SCHED(); RX(0, kx); RW(RB, 0, kw); DG0(); SCHED(); \
      MM(1, WC); SCHED(); RX(1, kx); RW(RB, 1, kw); DG1(); SCHED(); \
      MM(2, WC); SCHED(); RX(2, kx); RG(kg); CV0(WN); EXTRA2; SCHED(); \
      MM(3, WC); SCHED(); RX(3, kx); CV1(WN); EXTRA3; SCHED(); } while (0)
    unsigned mk[2] = {mk0, mk1};
    asm volatile("" : "+v"(acc[0]), "+v"(acc[1]), "+v"(acc[2]), "+v"(acc[3]));
    asm volatile("" : "+v"(offx[0]), "+v"(offx[1]), "+v"(offx[2]), "+v"(offx[3]), "+v"(offw[0]), "+v"(offw[1]), "+v"(offg), "+v"(mk[0]), "+v"(mk[1]));
    __builtin_amdgcn_s_waitcnt(0xC07F);
    STAMP(4);
    BARRIER_N();
    STAMP(5);
    RG(0); RW(wrb, 0, 0); RW(wrb, 1, 0); RX(0, 0); RW(wra, 0, 1); RW(wra, 1, 1); RX(1, 0); RX(2, 0); RX(3, 0);
    DG0(); DG1(); SCHED(); RG(1); SM0(wrb); SM1(wrb); SCHED();
    CV0(wf); CV1(wf); DG0(); DG1(); SCHED(); RG(2); SCHED();
#pragma unroll 1
    for (int s = 0; s < NST - 1; ++s) {
      const int dlt = ((s & (RING - 1)) == RING - 1) ? -(RING - 1) * SLOT : SLOT;
      STEP(wf, wfn, wra, wrb, 1, 2, 3, (void)0, (void)0);
      STEP(wfn, wf, wrb, wra, 2, 3, 4, (offx[0] += dlt, offw[0] += dlt), (offw[1] += dlt, offx[1] += dlt));
      BARRIER_N();
      STEP(wf, wfn, wra, wrb, 3, 0, 5, (offx[2] += dlt), (void)0);
      STEP(wfn, wf, wrb, wra, 0, 1, 6, (offx[3] += dlt), (offg += 128u));
    }
    f32x4 c1v[4], c2v[4];
    f32x2 rs[4];
    const unsigned tc = lds0 + LDS_TAB + 1024 + (unsigned)(wn * 32 + h * 16) * 4u, tr = lds0 + LDS_TAB + (unsigned)r * 8u;
#define RC(q) do { c1v[q] = *(const LAS f32x4*)(tc + (q) * 16); c2v[q] = *(const LAS f32x4*)(tc + 512 + (q) * 16); rs[q] = *(const LAS f32x2*)(tr + (q) * 256); } while (0)
    STEP(wf, wfn, wra, wrb, 1, 2, 3, (void)0, (void)0);
    STEP(wfn, wf, wrb, wra, 2, 3, 4, (void)0, (void)0);
    SM0(wra); SM1(wra); SCHED();
    MM(0, wf); SCHED(); RX(0, 3); SCHED();
    MM(1, wf); SCHED(); RX(1, 3); SCHED();
    MM(2, wf); SCHED(); RX(2, 3); CV0(wfn); SCHED();
    MM(3, wf); SCHED(); RX(3, 3); CV1(wfn); SCHED();
    MM(0, wfn); SCHED(); RC(0); SCHED(); MM(1, wfn); SCHED(); RC(1); SCHED(); MM(2, wfn); SCHED(); RC(2); SCHED(); MM(3, wfn); SCHED(); RC(3); SCHED();
    STAMP(20);
    const unsigned so = lds0 + (unsigned)r * 256u, sc0 = (unsigned)(((wn * 4 + h * 2) ^ (r & 7)) * 16), sc1 = sc0 ^ 16u;
#pragma unroll
    for (int mt = 0; mt < 4; ++mt) {
      const float nm = -rs[mt][0], rstd = rs[mt][1];
      f32x4 tq[4];
#pragma unroll
      for (int q = 0; q < 4; ++q) {
        const f32x4 av = {acc[mt][4 * q], acc[mt][4 * q + 1], acc[mt][4 * q + 2], acc[mt][4 * q + 3]};
        tq[q] = av + c1v[q] * nm;
      }
#pragma unroll
      for (int q = 0; q < 4; ++q) tq[q] = tq[q] * rstd + c2v[q];
      const u32x4 o0 = {cvt_pk(tq[0][0], tq[0][1]), cvt_pk(tq[0][2], tq[0][3]), cvt_pk(tq[1][0], tq[1][1]), cvt_pk(tq[1][2], tq[1][3])};
      const u32x4 o1 = {cvt_pk(tq[2][0], tq[2][1]), cvt_pk(tq[2][2], tq[2][3]), cvt_pk(tq[3][0], tq[3][1]), cvt_pk(tq[3][2], tq[3][3])};
      *(LAS u32x4*)(so + mt * 8192 + sc0) = o0;
      *(LAS u32x4*)(so + mt * 8192 + sc1) = o1;
      SCHED();
      if (mt == 1) BARRIER_L();
    }
  }
  if (wave >= 4) {
    BARRIER_N();
    store_rows<2>(lds0, (wave - 4) * 8, lane, (char*)out + (size_t)mi * 262144 + ni * 256);
  }
  STAMP(24);
  BARRIER_L();
  STAMP(25);
  if (wave < 8) store_rows<2>(lds0, 64 + wave * 8, lane, (char*)out + (size_t)mi * 262144 + ni * 256);
  STAMP_FLUSH();
  asm volatile("lnlin_code_end:");
}

extern "C" void kernel_launch(void* const* d_in, const int* in_sizes, int n_in, void* d_out, int out_size, void* d_ws, size_t ws_size,
                              hipStream_t stream) {
  const u16* x = (const u16*)d_in[0]; const u16* g = (const u16*)d_in[1]; const u16* b = (const u16*)d_in[2]; const u16* w = (const u16*)d_in[3];
  (void)hipFuncSetAttribute((const void*)lnlin_kernel, hipFuncAttributeMaxDynamicSharedMemorySize, LDS_TOTAL);
  lnlin_kernel<<<256, 768, LDS_TOTAL, stream>>>(x, g, b, w, (u16*)d_out, (char*)d_ws);
}
```

```cpp
#include <hip/hip_runtime.h>
#include <hip/hip_bf16.h>
#include <utility>
#pragma clang diagnostic ignored "-Wint-to-pointer-cast"

typedef short bf16x8 __attribute__((ext_vector_type(8)));
typedef short s16x4 __attribute__((ext_vector_type(4)));
typedef float f32x16 __attribute__((ext_vector_type(16)));
typedef float f32x4 __attribute__((ext_vector_type(4)));
typedef float f32x2 __attribute__((ext_vector_type(2)));
typedef unsigned u32x4 __attribute__((ext_vector_type(4)));
typedef unsigned u32x2 __attribute__((ext_vector_type(2)));
typedef __bf16 bf16x2_t __attribute__((ext_vector_type(2)));
typedef unsigned short u16;
typedef unsigned long long u64;
#define GAS __attribute__((address_space(1)))
#define LAS __attribute__((address_space(3)))

#define ST_FLAVOR "nt"
#define SIDE_START 2
#define PRIO_M 2
#define PRIO_L 0

constexpr int KD = 1024, ND = 1024;
constexpr int NST = 16;
constexpr int RING = 4;
constexpr int SLOT = 32768;
constexpr int LDS_G = RING * SLOT;
constexpr int LDS_B = LDS_G + 2048 + 64;
constexpr int LDS_TAB = LDS_B + 256;
constexpr int LDS_TOTAL = LDS_TAB + 2048;

constexpr size_t WS_EPR = 0;
constexpr size_t WS_EPC = 32 * 128;
constexpr size_t WS_RS = 8192;
constexpr size_t WS_C = WS_RS + 4096 * 2 * 8;

#define STAMP_DECL
#define STAMP_RT(i)
#define STAMP(i)
#define STAMP_DYN(i)
#define STAMP_FLUSH()

template <int B, int E, class F> __device__ __forceinline__ void static_for(F&& f) {
  if constexpr (B < E) { f(std::integral_constant<int, B>{}); static_for<B + 1, E>(f); }
}
__device__ __forceinline__ float bf_lo(unsigned u) { return __uint_as_float(u << 16); }
__device__ __forceinline__ float bf_hi(unsigned u) { return __uint_as_float(u & 0xffff0000u); }
__device__ __forceinline__ unsigned cvt_pk(float lo, float hi) {
  f32x2 v = {lo, hi}; return __builtin_bit_cast(unsigned, __builtin_convertvector(v, bf16x2_t));
}
template <int CTRL> __device__ __forceinline__ float dpp_add(float x) {
  return x + __builtin_bit_cast(float, __builtin_amdgcn_update_dpp(0, __builtin_bit_cast(int, x), CTRL, 0xf, 0xf, true));
}
__device__ __forceinline__ float sum16(float x) {
  x = dpp_add<0x128>(x); x = dpp_add<0x124>(x); x = dpp_add<0x122>(x); x = dpp_add<0x121>(x);
  return x;
}
#define SCHED() __builtin_amdgcn_sched_barrier(0)
#define BARRIER_L() do { SCHED(); asm volatile("s_waitcnt lgkmcnt(0)\n\ts_barrier" ::: "memory"); SCHED(); } while (0)
#define BARRIER_N() do { SCHED(); asm volatile("s_barrier" ::: "memory"); SCHED(); } while (0)
template <int N> __device__ __forceinline__ void wait_vm() { asm volatile("s_waitcnt vmcnt(%0)" ::"n"(N) : "memory"); }

__device__ __forceinline__ void glds16x4(unsigned v0, unsigned v1, unsigned v2, unsigned v3, const void* sbase, unsigned lds_dst) {
  unsigned keep;
  asm volatile("s_mov_b32 %0, m0\n\ts_mov_b32 m0, %6\n\ts_nop 0\n\t"
               "global_load_lds_dwordx4 %1, %5\n\tglobal_load_lds_dwordx4 %2, %5 offset:1024\n\t"
               "global_load_lds_dwordx4 %3, %5 offset:2048\n\tglobal_load_lds_dwordx4 %4, %5 offset:3072\n\ts_mov_b32 m0, %0"
               : "=&s"(keep) : "v"(v0), "v"(v1), "v"(v2), "v"(v3), "s"(sbase), "s"(lds_dst) : "memory");
}
__device__ __forceinline__ void glds4(unsigned v0, const void* sbase, unsigned lds_dst) {
  unsigned keep;
  asm volatile("s_mov_b32 %0, m0\n\ts_mov_b32 m0, %3\n\ts_nop 0\n\tglobal_load_lds_dword %1, %2\n\ts_mov_b32 m0, %0"
               : "=&s"(keep) : "v"(v0), "s"(sbase), "s"(lds_dst) : "memory");
}
template <int OFF> __device__ __forceinline__ u32x4 gload16_asm(unsigned voff, const void* sbase) {
  u32x4 r; asm volatile("global_load_dwordx4 %0, %1, %2 offset:%3" : "=v"(r) : "v"(voff), "s"(sbase), "n"(OFF) : "memory"); return r;
}
template <int OFF> __device__ __forceinline__ u32x4 gload16_sc1_asm(unsigned voff, const void* sbase) {
  u32x4 r; asm volatile("global_load_dwordx4 %0, %1, %2 offset:%3 sc1" : "=v"(r) : "v"(voff), "s"(sbase), "n"(OFF) : "memory"); return r;
}
__device__ __forceinline__ unsigned gload_u32_sc1_asm(unsigned voff, const void* sbase) {
  unsigned r; asm volatile("global_load_dword %0, %1, %2 sc1" : "=v"(r) : "v"(voff), "s"(sbase) : "memory"); return r;
}
template <int OFF> __device__ __forceinline__ u32x2 gload_u64_sc1_asm(unsigned voff, const void* sbase) {
  u32x2 r; asm volatile("global_load_dwordx2 %0, %1, %2 offset:%3 sc1" : "=v"(r) : "v"(voff), "s"(sbase), "n"(OFF) : "memory"); return r;
}
__device__ __forceinline__ void store_granule(char* ws, size_t off, unsigned tag, float v) {
  __hip_atomic_store((GAS u64*)(ws + off), ((u64)tag << 32) | __float_as_uint(v), __ATOMIC_RELAXED, __HIP_MEMORY_SCOPE_AGENT);
}
__device__ __forceinline__ u64 load_granule(const char* ws, size_t off) {
  return __hip_atomic_load((GAS u64*)(ws + off), __ATOMIC_RELAXED, __HIP_MEMORY_SCOPE_AGENT);
}
__device__ __forceinline__ unsigned load_word(const char* ws, size_t off) {
  return __hip_atomic_load((GAS unsigned*)(ws + off), __ATOMIC_RELAXED, __HIP_MEMORY_SCOPE_AGENT);
}

template <int N> __device__ __forceinline__ void store_rows(unsigned lds0, int rowb, int lane, char* ob) {
  const int row0 = rowb + (lane >> 4), ch = lane & 15;
  u32x4 v[N];
#pragma unroll
  for (int i = 0; i < N; ++i) { const int row = row0 + i * 4; v[i] = *(const LAS u32x4*)(lds0 + row * 256 + ((ch ^ (row & 7)) * 16)); }
  const unsigned oo = (unsigned)row0 * 2048u + (unsigned)ch * 16u;
#pragma unroll
  for (int i = 0; i < N; ++i)
    asm volatile("global_store_dwordx4 %0, %1, %2 " ST_FLAVOR "\n\ts_nop 1" :: "v"(oo + i * 8192u), "v"(v[i]), "s"(ob) : "memory");
}

__global__ __launch_bounds__(512, 3) void lnlin_kernel(const u16* __restrict__ x, const u16* __restrict__ g, const u16* __restrict__ b,
                                                       const u16* __restrict__ w, u16* __restrict__ out, char* ws) {
  u64 code_pc; unsigned code_len;
  asm volatile("s_getpc_b64 %0\nlnlin_code_begin:\n\ts_mov_b32 %1, lnlin_code_end-lnlin_code_begin" : "=s"(code_pc), "=s"(code_len));
  extern __shared__ __attribute__((aligned(16))) char smem_g[];
  const int bid = blockIdx.x, xcd = bid & 7, idx = bid >> 3;
  const int mi = xcd * 4 + (idx >> 3), ni = idx & 7;
  const int rot = (mi >> 2) * 2;
  const int tid = threadIdx.x, lane = tid & 63;
  const int wave = __builtin_amdgcn_readfirstlane(tid >> 6);
  const unsigned lds0 = (unsigned)(size_t)(LAS char*)smem_g;
  STAMP_DECL STAMP_RT(0); STAMP(1);
  const u64 tstart = __builtin_amdgcn_s_memrealtime();

  if (wave >= 4) {
    const int d = wave - 4;
    if (PRIO_L) __builtin_amdgcn_s_setprio(PRIO_L);
    unsigned xo[4], wo[4];
#pragma unroll
    for (int i = 0; i < 4; ++i) {
      const int row = i * 8 + (lane >> 3);
      xo[i] = (unsigned)row * 2048u + (unsigned)(((lane & 7) ^ ((row >> 1) & 7)) * 16) - (unsigned)i * 1024u;
      const int kr = i * 4 + (lane >> 4);
      wo[i] = (unsigned)kr * 2048u + (unsigned)(((lane & 15) ^ (((lane >> 4) << 2) | i)) * 16) - (unsigned)i * 1024u;
    }
    const unsigned dst = lds0 + (unsigned)d * 4096u;
    const char* xb = (const char*)x + (size_t)mi * 262144 + d * 65536;
    const char* wb = (const char*)w + d * 32768 + ni * 256;
    auto issue = [&](int st) {
      const unsigned ds_ = dst + (unsigned)(st & (RING - 1)) * SLOT;
      const int ks_ = (st + rot) & (NST - 1);
      glds16x4(wo[0], wo[1], wo[2], wo[3], wb + (size_t)ks_ * 131072, ds_ + 16384u);
      glds16x4(xo[0], xo[1], xo[2], xo[3], xb + ks_ * 128, ds_);
    };
    glds4((unsigned)lane * 4u, (const char*)g + ((d * 512 + rot * 128) & 2047), lds0 + LDS_G + (unsigned)d * 512u);
    glds4((unsigned)lane * 4u, (const char*)g + ((d * 512 + 256 + rot * 128) & 2047), lds0 + LDS_G + (unsigned)d * 512u + 256u);
    if (d == 0) glds4((unsigned)lane * 4u, (const char*)b + rot * 128, lds0 + LDS_B);
    issue(0);
    unsigned ep = gload_u32_sc1_asm((lane & 1) ? (unsigned)(WS_EPC + ni * 128) : (unsigned)(WS_EPR + mi * 128), ws);
    issue(1);
    STAMP(2);
    const int scol = (mi & 3) * 32 + d * 8;
    const unsigned cw = lds0 + 16384u + (unsigned)(256 * lane + 16 * ((scol >> 3) ^ (((lane & 3) << 2) | ((lane >> 2) & 3))));
    const int srow = mi * 128 + ni * 16 + d * 4 + (lane >> 4), l4 = lane & 15;
    const unsigned so = (unsigned)srow * 2048u + (unsigned)l4 * 16u;
    const unsigned goc = (unsigned)WS_C + (unsigned)ni * 16384u + (unsigned)(lane >> 5) * 8192u + (unsigned)(d * 32 + (lane & 31)) * 16u;
    const unsigned gor = (unsigned)WS_RS + (unsigned)mi * 2048u + (unsigned)(d * 32 + (lane & 31)) * 16u;
    u32x4 xr[8], q[5];
    float p1[8], p2[8], part = 0.f, mean = 0.f, pub = 0.f, v0 = 0.f, v1 = 0.f;
    unsigned bad = 0u, epr = 0, epc = 0;
    wait_vm<9>();
    STAMP(3);
    BARRIER_N();
    static_for<0, NST - 1>([&](auto S) {
      constexpr int st = S;
      constexpr int nside = st <= 2 ? (st == 2 ? 2 : 3) : (st == 3 || st == 9 || st == 12 ? 1 : (st == 8 ? 4 : 0));
      if constexpr (st <= 2) {
        static_for<3 * st, (st == 2 ? 8 : 3 * st + 3)>([&](auto J) { constexpr int j = J;
          xr[j] = gload16_asm<0>(so + (unsigned)(((j + 2 + (rot >> 1)) & 7) * 256), x); });
      }
      if constexpr (st == 3) {
        if (l4 < 4) store_granule(ws, WS_C + ((size_t)((ni * 8 + (mi >> 2)) * 128 + scol + ((lane >> 4) & 1) * 4 + l4) * 2 + (lane >> 5)) * 8, epc + 1u, pub);
      }
      if constexpr (st == 8) { static_for<0, 4>([&](auto J) { constexpr int j = J; q[j] = gload16_sc1_asm<(j & 1) * 2048>(goc + (j >> 1) * 4096u, ws); }); }
      if constexpr (st == 9) {
        if (l4 < 2) store_granule(ws, WS_RS + ((size_t)srow * 2 + (lane & 1)) * 8, epr + 1u, pub);
      }
      if constexpr (st == 12) q[4] = gload16_sc1_asm<0>(gor, ws);
      SCHED();
      if constexpr (st + 2 < NST) issue(st + 2);
      SCHED();
      if constexpr (st == 2) asm volatile("" : "+v"(xr[0]), "+v"(xr[1]), "+v"(xr[2]), "+v"(ep));
      if constexpr (st == 3) asm volatile("" : "+v"(xr[3]), "+v"(xr[4]), "+v"(xr[5]));
      if constexpr (st == 4) asm volatile("" : "+v"(xr[6]), "+v"(xr[7]));
      if constexpr (st == 10) asm volatile("" : "+v"(q[0]), "+v"(q[1]), "+v"(q[2]), "+v"(q[3]));
      if constexpr (st == 14) asm volatile("" : "+v"(q[4]));
      if constexpr (st <= 1) {
        const u32x4 wv = *(const LAS u32x4*)(cw + st * SLOT);
        const float ga = bf_lo(*(const LAS u16*)(lds0 + LDS_G + st * 128 + lane * 2)), ba = bf_lo(*(const LAS u16*)(lds0 + LDS_B + st * 128 + lane * 2));
#pragma unroll
        for (int i = 0; i < 4; ++i) {
          const float l0 = bf_lo(wv[i]), h0 = bf_hi(wv[i]);
          const unsigned pa = cvt_pk(ga * l0, ga * h0);
          if constexpr (st == 0) { p1[2 * i] = bf_lo(pa); p1[2 * i + 1] = bf_hi(pa); p2[2 * i] = ba * l0; p2[2 * i + 1] = ba * h0; }
          else { p1[2 * i] += bf_lo(pa); p1[2 * i + 1] += bf_hi(pa); p2[2 * i] = fmaf(ba, l0, p2[2 * i]); p2[2 * i + 1] = fmaf(ba, h0, p2[2 * i + 1]); }
        }
      }
      if constexpr (st == 2) {
        float a[8];
#pragma unroll
        for (int i = 0; i < 8; ++i) {
          auto rr = __builtin_amdgcn_permlane32_swap(__float_as_uint(p1[i]), __float_as_uint(p2[i]), false, false);
          a[i] = __uint_as_float(rr[0]) + __uint_as_float(rr[1]);
        }
        float cs[4];
#pragma unroll
        for (int i = 0; i < 4; ++i) {
          auto rr = __builtin_amdgcn_permlane16_swap(__float_as_uint(a[i]), __float_as_uint(a[4 + i]), false, false);
          cs[i] = sum16(__uint_as_float(rr[0]) + __uint_as_float(rr[1]));
        }
        pub = l4 == 0 ? cs[0] : (l4 == 1 ? cs[1] : (l4 == 2 ? cs[2] : cs[3]));
        epr = (unsigned)__builtin_amdgcn_readlane((int)ep, 0); epc = (unsigned)__builtin_amdgcn_readlane((int)ep, 1);
      }
      if constexpr (st >= 3 && st <= 5) {
        constexpr int j0 = (st - 3) * 3, j1 = st == 5 ? 8 : j0 + 3;
        float s0 = part, s1 = 0.f, s2 = 0.f, s3 = 0.f;
#pragma unroll
        for (int j = j0; j < j1; ++j) {
          s0 += bf_lo(xr[j][0]); s1 += bf_hi(xr[j][0]); s2 += bf_lo(xr[j][1]); s3 += bf_hi(xr[j][1]);
          s0 += bf_lo(xr[j][2]); s1 += bf_hi(xr[j][2]); s2 += bf_lo(xr[j][3]); s3 += bf_hi(xr[j][3]);
        }
        part = (s0 + s1) + (s2 + s3);
        if constexpr (st == 5) { mean = sum16(part) * (1.f / 1024.f); part = 0.f; }
      }
      if constexpr (st >= 6 && st <= 8) {
        constexpr int j0 = (st - 6) * 3, j1 = st == 8 ? 8 : j0 + 3;
        float q0 = part, q1 = 0.f, q2 = 0.f, q3 = 0.f;
#pragma unroll
        for (int j = j0; j < j1; ++j)
#pragma unroll
          for (int i = 0; i < 4; i += 2) {
            const float d0 = bf_lo(xr[j][i]) - mean, d1 = bf_hi(xr[j][i]) - mean, d2 = bf_lo(xr[j][i + 1]) - mean, d3 = bf_hi(xr[j][i + 1]) - mean;
            q0 = fmaf(d0, d0, q0); q1 = fmaf(d1, d1, q1); q2 = fmaf(d2, d2, q2); q3 = fmaf(d3, d3, q3);
          }
        part = (q0 + q1) + (q2 + q3);
        if constexpr (st == 8) {
          const float var = sum16(part) * (1.f / 1024.f);
          pub = (lane & 1) ? __builtin_amdgcn_rsqf(var + 1e-5f) : mean;
        }
      }
      auto sum_c = [&]() {
        const unsigned tag = epc + 1u;
        float s1 = 0.f, s2 = 0.f; bad = 0u;
#pragma unroll
        for (int j = 0; j < 4; ++j) { bad |= (q[j][1] ^ tag) | (q[j][3] ^ tag); s1 += __uint_as_float(q[j][0]); s2 += __uint_as_float(q[j][2]); }
        auto rr = __builtin_amdgcn_permlane32_swap(__float_as_uint(s1), __float_as_uint(s2), false, false);
        v0 = __uint_as_float(rr[0]) + __uint_as_float(rr[1]);
      };
      if constexpr (st == 10) sum_c();
      if constexpr (st == 14) {
        const unsigned tagr = epr + 1u;
        bad |= (q[4][1] ^ tagr) | (q[4][3] ^ tagr);
        bool ok = bad == 0u;
        if (__builtin_expect(!__all(ok), 0)) {
#pragma unroll 1
          for (int it = 0; it < 20000; ++it) {
            static_for<0, 4>([&](auto J) { constexpr int j = J; q[j] = gload16_sc1_asm<(j & 1) * 2048>(goc + (j >> 1) * 4096u, ws); });
            q[4] = gload16_sc1_asm<0>(gor, ws);
            asm volatile("s_waitcnt vmcnt(0)" : "+v"(q[0]), "+v"(q[1]), "+v"(q[2]), "+v"(q[3]), "+v"(q[4]) :: "memory");
            sum_c();
            bad |= (q[4][1] ^ tagr) | (q[4][3] ^ tagr);
            ok = bad == 0u;
            if (__all(ok)) break;
            __builtin_amdgcn_s_sleep(2);
          }
        }
        v1 = __uint_as_float((lane >> 5) ? q[4][2] : q[4][0]);
        if (__builtin_expect(!__all(ok), 0)) {
          const int e = d * 32 + (lane & 31);
          const u16* wc = w + ni * 128 + e;
          const u16* gb = (lane >> 5) ? b : g;
          v0 = 0.f;
#pragma unroll 1
          for (int k = 0; k < KD; ++k) {
            const float pr = bf_lo(gb[k]) * bf_lo(wc[(size_t)k * ND]);
            v0 += (lane >> 5) ? pr : bf_lo(cvt_pk(pr, 0.f));
          }
          const u16* xrow = x + (size_t)(mi * 128 + e) * KD;
          float sm = 0.f, sq = 0.f;
#pragma unroll 1
          for (int k = 0; k < KD; ++k) sm += bf_lo(xrow[k]);
          sm *= (1.f / 1024.f);
#pragma unroll 1
          for (int k = 0; k < KD; ++k) { const float dd = bf_lo(xrow[k]) - sm; sq = fmaf(dd, dd, sq); }
          v1 = (lane >> 5) ? __builtin_amdgcn_rsqf(sq * (1.f / 1024.f) + 1e-5f) : sm;
        }
        *(LAS float*)(lds0 + LDS_TAB + 1024 + (lane >> 5) * 512 + (d * 32 + (lane & 31)) * 4) = v0;
        *(LAS float*)(lds0 + LDS_TAB + (d * 32 + (lane & 31)) * 8 + (lane >> 5) * 4) = v1;
      }
      SCHED();
      wait_vm<(st + 2 < NST ? 8 + nside : 0)>();
      if constexpr (st == 14) BARRIER_L(); else BARRIER_N();
      STAMP(4 + st);
    });
    if (tid == 256) {
      __hip_atomic_fetch_add((GAS unsigned*)(ws + WS_EPR + mi * 128), 1u, __ATOMIC_RELAXED, __HIP_MEMORY_SCOPE_AGENT);
      __hip_atomic_fetch_add((GAS unsigned*)(ws + WS_EPC + ni * 128), 1u, __ATOMIC_RELAXED, __HIP_MEMORY_SCOPE_AGENT);
    }
  } else {
    const int wn = wave;
    {
      const unsigned co = (unsigned)wn * 3072u + (unsigned)lane * 16u, lim = (code_len & ~15u) - 16u;
      const u32x4 t0 = *(const GAS u32x4*)(code_pc + min(co, lim)) ^ *(const GAS u32x4*)(code_pc + min(co + 1024u, lim)) ^ *(const GAS u32x4*)(code_pc + min(co + 2048u, lim));
      asm volatile("" :: "v"(t0));
    }
    STAMP(3);
    if (PRIO_M) __builtin_amdgcn_s_setprio(PRIO_M);
    const int r = lane & 31, h = lane >> 5, sw = (r >> 1) & 7;
    unsigned offx[4], offw[2];
#pragma unroll
    for (int ks = 0; ks < 4; ++ks) offx[ks] = lds0 + (unsigned)r * 128u + (unsigned)(((2 * ks + h) ^ sw) * 16);
    {
      const int blk = (lane >> 4) & 1, q = (lane & 15) >> 2, p = lane & 3;
#pragma unroll
      for (int tt = 0; tt < 2; ++tt) {
        const int row = 8 * h + 4 * tt + q, ch = 4 * wn + blk + 2 * (p & 1);
        offw[tt] = lds0 + 16384u + (unsigned)(256 * row + 16 * (ch ^ ((q << 2) | ((2 * h + tt) & 3))) + 8 * (p >> 1));
      }
    }
    unsigned offg = lds0 + LDS_G + (unsigned)h * 16u;
    const unsigned li = lane & 3;
    const unsigned mk0 = li == 0 ? 0xffffu : (li == 1 ? 0xffff0000u : 0u), mk1 = li == 2 ? 0xffffu : (li == 3 ? 0xffff0000u : 0u);
    f32x16 acc[4] = {};
    bf16x8 xf[4];
    s16x4 wra[2], wrb[2];
    u32x4 gq, dg;
    u32x4 wf, wfn;
    f32x4 d0, d1;
    const f32x4 z4 = {0.f, 0.f, 0.f, 0.f};
#define RX(mt, ks) xf[mt] = *(const LAS bf16x8*)(offx[ks] + (mt) * 4096)
#define RW(R, tt, ks) R[tt] = __builtin_amdgcn_ds_read_tr16_b64_v4i16((LAS s16x4*)(offw[tt] + (ks) * 4096))
#define RG(o) gq = *(const LAS u32x4*)(offg + (o) * 32)
#define DG0() do { dg[0] = gq[0] & mk[0]; dg[1] = gq[1] & mk[1]; } while (0)
#define DG1() do { dg[2] = gq[2] & mk[0]; dg[3] = gq[3] & mk[1]; } while (0)
#define SM0(R) d0 = __builtin_amdgcn_mfma_f32_4x4x4bf16_1k(__builtin_bit_cast(s16x4, (u32x2){dg[0], dg[1]}), R[0], z4, 0, 0, 0)
#define SM1(R) d1 = __builtin_amdgcn_mfma_f32_4x4x4bf16_1k(__builtin_bit_cast(s16x4, (u32x2){dg[2], dg[3]}), R[1], z4, 0, 0, 0)
#define CV0(W) do { W[0] = cvt_pk(d0[0], d0[1]); W[1] = cvt_pk(d0[2], d0[3]); } while (0)
#define CV1(W) do { W[2] = cvt_pk(d1[0], d1[1]); W[3] = cvt_pk(d1[2], d1[3]); } while (0)
#define MM(mt, W) acc[mt] = __builtin_amdgcn_mfma_f32_32x32x16_bf16(__builtin_bit_cast(bf16x8, W), xf[mt], acc[mt], 0, 0, 0)
#define STEP(WC, WN, RA, RB, kx, kw, kg, EXTRA2, EXTRA3) do { \
      SM0(RA); SM1(RA); SCHED(); \
      MM(0, WC); SCHED(); RX(0, kx); RW(RB, 0, kw); DG0(); SCHED(); \
      MM(1, WC); SCHED(); RX(1, kx); RW(RB, 1, kw); DG1(); SCHED(); \
      MM(2, WC); SCHED(); RX(2, kx); RG(kg); CV0(WN); EXTRA2; SCHED(); \
      MM(3, WC); SCHED(); RX(3, kx); CV1(WN); EXTRA3; SCHED(); } while (0)
    unsigned mk[2] = {mk0, mk1};
    asm volatile("" : "+v"(acc[0]), "+v"(acc[1]), "+v"(acc[2]), "+v"(acc[3]));
    asm volatile("" : "+v"(offx[0]), "+v"(offx[1]), "+v"(offx[2]), "+v"(offx[3]), "+v"(offw[0]), "+v"(offw[1]), "+v"(offg), "+v"(mk[0]), "+v"(mk[1]));
    __builtin_amdgcn_s_waitcnt(0xC07F);
    STAMP(4);
    BARRIER_N();
    STAMP(5);
    RG(0); RW(wrb, 0, 0); RW(wrb, 1, 0); RX(0, 0); RW(wra, 0, 1); RW(wra, 1, 1); RX(1, 0); RX(2, 0); RX(3, 0);
    DG0(); DG1(); SCHED(); RG(1); SM0(wrb); SM1(wrb); SCHED();
    CV0(wf); CV1(wf); DG0(); DG1(); SCHED(); RG(2); SCHED();
#pragma unroll 1
    for (int s = 0; s < NST - 1; ++s) {
      const int dlt = ((s & (RING - 1)) == RING - 1) ? -(RING - 1) * SLOT : SLOT;
      STEP(wf, wfn, wra, wrb, 1, 2, 3, (void)0, (void)0);
      STEP(wfn, wf, wrb, wra, 2, 3, 4, (offx[0] += dlt, offw[0] += dlt), (offw[1] += dlt, offx[1] += dlt));
      BARRIER_N();
      STEP(wf, wfn, wra, wrb, 3, 0, 5, (offx[2] += dlt), (void)0);
      STEP(wfn, wf, wrb, wra, 0, 1, 6, (offx[3] += dlt), (offg += 128u));
    }
    f32x4 c1v[4], c2v[4];
    f32x2 rs[4];
    const unsigned tc = lds0 + LDS_TAB + 1024 + (unsigned)(wn * 32 + h * 16) * 4u, tr = lds0 + LDS_TAB + (unsigned)r * 8u;
#define RC(q) do { c1v[q] = *(const LAS f32x4*)(tc + (q) * 16); c2v[q] = *(const LAS f32x4*)(tc + 512 + (q) * 16); rs[q] = *(const LAS f32x2*)(tr + (q) * 256); } while (0)
    STEP(wf, wfn, wra, wrb, 1, 2, 3, (void)0, (void)0);
    STEP(wfn, wf, wrb, wra, 2, 3, 4, (void)0, (void)0);
    SM0(wra); SM1(wra); SCHED();
    MM(0, wf); SCHED(); RX(0, 3); SCHED();
    MM(1, wf); SCHED(); RX(1, 3); SCHED();
    MM(2, wf); SCHED(); RX(2, 3); CV0(wfn); SCHED();
    MM(3, wf); SCHED(); RX(3, 3); CV1(wfn); SCHED();
    MM(0, wfn); SCHED(); RC(0); SCHED(); MM(1, wfn); SCHED(); RC(1); SCHED(); MM(2, wfn); SCHED(); RC(2); SCHED(); MM(3, wfn); SCHED(); RC(3); SCHED();
    STAMP(20);
    const unsigned so = lds0 + (unsigned)r * 256u, sc0 = (unsigned)(((wn * 4 + h * 2) ^ (r & 7)) * 16), sc1 = sc0 ^ 16u;
#pragma unroll
    for (int mt = 0; mt < 4; ++mt) {
      const float nm = -rs[mt][0], rstd = rs[mt][1];
      f32x4 tq[4];
#pragma unroll
      for (int q = 0; q < 4; ++q) {
        const f32x4 av = {acc[mt][4 * q], acc[mt][4 * q + 1], acc[mt][4 * q + 2], acc[mt][4 * q + 3]};
        tq[q] = av + c1v[q] * nm;
      }
#pragma unroll
      for (int q = 0; q < 4; ++q) tq[q] = tq[q] * rstd + c2v[q];
      const u32x4 o0 = {cvt_pk(tq[0][0], tq[0][1]), cvt_pk(tq[0][2], tq[0][3]), cvt_pk(tq[1][0], tq[1][1]), cvt_pk(tq[1][2], tq[1][3])};
      const u32x4 o1 = {cvt_pk(tq[2][0], tq[2][1]), cvt_pk(tq[2][2], tq[2][3]), cvt_pk(tq[3][0], tq[3][1]), cvt_pk(tq[3][2], tq[3][3])};
      *(LAS u32x4*)(so + mt * 8192 + sc0) = o0;
      *(LAS u32x4*)(so + mt * 8192 + sc1) = o1;
      SCHED();
      if (mt == 1) BARRIER_L();
    }
  }
  if (wave >= 4) {
    BARRIER_N();
    store_rows<4>(lds0, (wave - 4) * 16, lane, (char*)out + (size_t)mi * 262144 + ni * 256);
  }
  STAMP(24);
  BARRIER_L();
  STAMP(25);
  store_rows<2>(lds0, 64 + wave * 8, lane, (char*)out + (size_t)mi * 262144 + ni * 256);
  asm volatile("s_waitcnt vmcnt(0)" ::: "memory");
  while (__builtin_amdgcn_s_memrealtime() - tstart < 2500) __builtin_amdgcn_s_sleep(1);
  STAMP_FLUSH();
  asm volatile("lnlin_code_end:");
}

extern "C" void kernel_launch(void* const* d_in, const int* in_sizes, int n_in, void* d_out, int out_size, void* d_ws, size_t ws_size,
                              hipStream_t stream) {
  const u16* x = (const u16*)d_in[0]; const u16* g = (const u16*)d_in[1]; const u16* b = (const u16*)d_in[2]; const u16* w = (const u16*)d_in[3];
  (void)hipFuncSetAttribute((const void*)lnlin_kernel, hipFuncAttributeMaxDynamicSharedMemorySize, LDS_TOTAL);
  lnlin_kernel<<<256, 512, LDS_TOTAL, stream>>>(x, g, b, w, (u16*)d_out, (char*)d_ws);
}
```

```cpp
#include <hip/hip_runtime.h>
#include <hip/hip_bf16.h>
#include <utility>
#pragma clang diagnostic ignored "-Wint-to-pointer-cast"

typedef short bf16x8 __attribute__((ext_vector_type(8)));
typedef short s16x4 __attribute__((ext_vector_type(4)));
typedef float f32x16 __attribute__((ext_vector_type(16)));
typedef float f32x4 __attribute__((ext_vector_type(4)));
typedef float f32x2 __attribute__((ext_vector_type(2)));
typedef unsigned u32x4 __attribute__((ext_vector_type(4)));
typedef unsigned u32x2 __attribute__((ext_vector_type(2)));
typedef __bf16 bf16x2_t __attribute__((ext_vector_type(2)));
typedef unsigned short u16;
typedef unsigned long long u64;
#define GAS __attribute__((address_space(1)))
#define LAS __attribute__((address_space(3)))

#define ST_FLAVOR "nt"
#define SIDE_START 2
#define PRIO_M 2
#define PRIO_L 0

constexpr int KD = 1024, ND = 1024;
constexpr int NST = 16;
constexpr int RING = 4;
constexpr int SLOT = 32768;
constexpr int GROW = 272;
constexpr int LDS_GT = RING * SLOT;
constexpr int LDS_G = LDS_GT + 8 * GROW;
constexpr int LDS_B = LDS_G + 256;
constexpr int LDS_TAB = LDS_B + 256;
constexpr int LDS_TOTAL = LDS_TAB + 2048;

constexpr size_t WS_EPR = 0;
constexpr size_t WS_EPC = 32 * 128;
constexpr size_t WS_C = 8192;
constexpr size_t WS_RS = WS_C + 8 * 16384;

#define STAMP_DECL
#define STAMP_RT(i)
#define STAMP(i)
#define STAMP_DYN(i)
#define STAMP_FLUSH()

template <int B, int E, class F> __device__ __forceinline__ void static_for(F&& f) {
  if constexpr (B < E) { f(std::integral_constant<int, B>{}); static_for<B + 1, E>(f); }
}
__device__ __forceinline__ float bf_lo(unsigned u) { return __uint_as_float(u << 16); }
__device__ __forceinline__ float bf_hi(unsigned u) { return __uint_as_float(u & 0xffff0000u); }
__device__ __forceinline__ unsigned cvt_pk(float lo, float hi) {
  f32x2 v = {lo, hi}; return __builtin_bit_cast(unsigned, __builtin_convertvector(v, bf16x2_t));
}
template <int CTRL> __device__ __forceinline__ float dpp_add(float x) {
  return x + __builtin_bit_cast(float, __builtin_amdgcn_update_dpp(0, __builtin_bit_cast(int, x), CTRL, 0xf, 0xf, true));
}
__device__ __forceinline__ float sum16(float x) {
  x = dpp_add<0x128>(x); x = dpp_add<0x124>(x); x = dpp_add<0x122>(x); x = dpp_add<0x121>(x);
  return x;
}
__device__ __forceinline__ float add1(float a, float b) { float r; asm("v_add_f32 %0, %1, %2" : "=v"(r) : "v"(a), "v"(b)); return r; }
__device__ __forceinline__ float mul1(float a, float b) { float r; asm("v_mul_f32 %0, %1, %2" : "=v"(r) : "v"(a), "v"(b)); return r; }
__device__ __forceinline__ float fma1(float a, float b, float c) { float r; asm("v_fma_f32 %0, %1, %2, %3" : "=v"(r) : "v"(a), "v"(b), "v"(c)); return r; }
#define PIN8(a) asm volatile("" : "+v"(a[0]), "+v"(a[1]), "+v"(a[2]), "+v"(a[3]), "+v"(a[4]), "+v"(a[5]), "+v"(a[6]), "+v"(a[7]))
#define SCHED() __builtin_amdgcn_sched_barrier(0)
#define BARRIER_L() do { SCHED(); asm volatile("s_waitcnt lgkmcnt(0)\n\ts_barrier" ::: "memory"); SCHED(); } while (0)
#define BARRIER_N() do { SCHED(); asm volatile("s_barrier" ::: "memory"); SCHED(); } while (0)
template <int N> __device__ __forceinline__ void wait_vm() { asm volatile("s_waitcnt vmcnt(%0)" ::"n"(N) : "memory"); }

__device__ __forceinline__ void glds16x4(unsigned v0, unsigned v1, unsigned v2, unsigned v3, const void* sbase, unsigned lds_dst) {
  unsigned keep;
  asm volatile("s_mov_b32 %0, m0\n\ts_mov_b32 m0, %6\n\ts_nop 0\n\t"
               "global_load_lds_dwordx4 %1, %5\n\tglobal_load_lds_dwordx4 %2, %5 offset:1024\n\t"
               "global_load_lds_dwordx4 %3, %5 offset:2048\n\tglobal_load_lds_dwordx4 %4, %5 offset:3072\n\ts_mov_b32 m0, %0"
               : "=&s"(keep) : "v"(v0), "v"(v1), "v"(v2), "v"(v3), "s"(sbase), "s"(lds_dst) : "memory");
}
__device__ __forceinline__ void glds4(unsigned v0, const void* sbase, unsigned lds_dst) {
  unsigned keep;
  asm volatile("s_mov_b32 %0, m0\n\ts_mov_b32 m0, %3\n\ts_nop 0\n\tglobal_load_lds_dword %1, %2\n\ts_mov_b32 m0, %0"
               : "=&s"(keep) : "v"(v0), "s"(sbase), "s"(lds_dst) : "memory");
}
template <int OFF> __device__ __forceinline__ u32x4 gload16_asm(unsigned voff, const void* sbase) {
  u32x4 r; asm volatile("global_load_dwordx4 %0, %1, %2 offset:%3" : "=v"(r) : "v"(voff), "s"(sbase), "n"(OFF) : "memory"); return r;
}
__device__ __forceinline__ unsigned gload_u32_sc1_asm(unsigned voff, const void* sbase) {
  unsigned r; asm volatile("global_load_dword %0, %1, %2 sc1" : "=v"(r) : "v"(voff), "s"(sbase) : "memory"); return r;
}
template <int OFF> __device__ __forceinline__ u32x2 gload_u64_sc1_asm(unsigned voff, const void* sbase) {
  u32x2 r; asm volatile("global_load_dwordx2 %0, %1, %2 offset:%3 sc1" : "=v"(r) : "v"(voff), "s"(sbase), "n"(OFF) : "memory"); return r;
}
__device__ __forceinline__ void store_granule(char* ws, size_t off, unsigned tag, float v) {
  __hip_atomic_store((GAS u64*)(ws + off), ((u64)tag << 32) | __float_as_uint(v), __ATOMIC_RELAXED, __HIP_MEMORY_SCOPE_AGENT);
}
__device__ __forceinline__ u64 load_granule(const char* ws, size_t off) {
  return __hip_atomic_load((GAS u64*)(ws + off), __ATOMIC_RELAXED, __HIP_MEMORY_SCOPE_AGENT);
}
__device__ __forceinline__ unsigned load_word(const char* ws, size_t off) {
  return __hip_atomic_load((GAS unsigned*)(ws + off), __ATOMIC_RELAXED, __HIP_MEMORY_SCOPE_AGENT);
}

template <int N> __device__ __forceinline__ void store_rows(unsigned lds0, int rowb, int lane, char* ob) {
  const int row0 = rowb + (lane >> 4), ch = lane & 15;
  u32x4 v[N];
#pragma unroll
  for (int i = 0; i < N; ++i) { const int row = row0 + i * 4; v[i] = *(const LAS u32x4*)(lds0 + row * 256 + ((ch ^ (row & 7)) * 16)); }
  const unsigned oo = (unsigned)row0 * 2048u + (unsigned)ch * 16u;
#pragma unroll
  for (int i = 0; i < N; ++i)
    asm volatile("global_store_dwordx4 %0, %1, %2 " ST_FLAVOR "\n\ts_nop 1" :: "v"(oo + i * 8192u), "v"(v[i]), "s"(ob) : "memory");
}

__global__ __launch_bounds__(768, 3) void lnlin_kernel(const u16* __restrict__ x, const u16* __restrict__ g, const u16* __restrict__ b,
                                                       const u16* __restrict__ w, u16* __restrict__ out, char* ws) {
  u64 code_pc; unsigned code_len;
  asm volatile("s_getpc_b64 %0\nlnlin_code_begin:\n\ts_mov_b32 %1, lnlin_code_end-lnlin_code_begin" : "=s"(code_pc), "=s"(code_len));
  extern __shared__ __attribute__((aligned(16))) char smem_g[];
  const int bid = blockIdx.x, xcd = bid & 7, idx = bid >> 3;
  const int mi = xcd * 4 + (idx >> 3), ni = idx & 7;
  const int slab = (ni + (mi >> 2)) & 7, rot = slab * 2;
  const int tid = threadIdx.x, lane = tid & 63;
  const int wave = __builtin_amdgcn_readfirstlane(tid >> 6);
  const unsigned lds0 = (unsigned)(size_t)(LAS char*)smem_g;
  STAMP_DECL STAMP_RT(0); STAMP(1);

  if (wave >= 8) {
    const int d = wave - 8, t = tid - 512;
    const int scol = (mi & 3) * 32 + d * 8;
    const unsigned cw = lds0 + 16384u + (unsigned)(256 * lane + 16 * ((scol >> 3) ^ (((lane & 3) << 2) | ((lane >> 2) & 3))));
    const int srow = t >> 1, l4 = lane & 15;
    const unsigned sx = lds0 + (unsigned)srow * 128u, ssw = (unsigned)(((t & 1) * 4) ^ ((srow >> 1) & 7));
    u32x4 xr[8];
    f32x4 f[16];
    unsigned ep;
    float p1[8], p2[8], part = 0.f, mean = 0.f, pub = 0.f, v0 = 0.f, v1 = 0.f;
    unsigned bad = 0u;
    u32x4 q[8];
    const unsigned gsrc = d < 2 ? (unsigned)WS_C + (unsigned)ni * 16384u + (unsigned)t * 16u : (unsigned)WS_RS + (unsigned)mi * 16384u + (unsigned)(t - 128) * 16u;
    auto gather = [&](int j0, int j1) __attribute__((always_inline)) {
#pragma unroll
      for (int j = j0; j < j1; ++j)
        q[j] = __builtin_amdgcn_raw_buffer_load_b128(__builtin_amdgcn_make_buffer_rsrc((void*)ws, 0, 0x100000, 0x00020000), gsrc + j * 2048u, 0, 16  );
    };
    auto combine = [&](unsigned tag) __attribute__((always_inline)) {
      bad = 0u; v0 = 0.f; v1 = 0.f;
#pragma unroll
      for (int j = 0; j < 8; ++j) { bad |= (q[j][1] ^ tag) | (q[j][3] ^ tag); v0 = add1(v0, __uint_as_float(q[j][0])); v1 = add1(v1, __uint_as_float(q[j][2])); }
      if (d >= 2) {
        v0 *= 0.125f;
        float dv = 0.f;
#pragma unroll
        for (int j = 0; j < 8; ++j) { const float e = __uint_as_float(q[j][0]) - v0; dv = fmaf(e, e, dv); }
        v1 = __builtin_amdgcn_rsqf(fmaf(dv, 128.f, v1) * (1.f / 1024.f) + 1e-5f);
      }
    };
    ep = load_word(ws, (lane & 1) ? WS_EPC + ni * 128 : WS_EPR + mi * 128);
    STAMP(2);
    BARRIER_N();
    unsigned epr = 0, epc = 0;
    static_for<0, NST - 1>([&](auto S) {
      constexpr int st = S;
      if constexpr (st <= 1) {
        const u32x4 wv = *(const LAS u32x4*)(cw + st * SLOT);
        const float ga = bf_lo(*(const LAS u16*)(lds0 + LDS_G + st * 128 + lane * 2)), ba = bf_lo(*(const LAS u16*)(lds0 + LDS_B + st * 128 + lane * 2));
#pragma unroll
        for (int i = 0; i < 4; ++i) xr[st * 4 + i] = *(const LAS u32x4*)(sx + st * SLOT + ((ssw ^ i) * 16u));
#pragma unroll
        for (int i = 0; i < 4; ++i) {
          const float l0 = bf_lo(wv[i]), h0 = bf_hi(wv[i]);
          const unsigned pa = cvt_pk(mul1(ga, l0), mul1(ga, h0));
          if constexpr (st == 0) { p1[2 * i] = bf_lo(pa); p1[2 * i + 1] = bf_hi(pa); p2[2 * i] = mul1(ba, l0); p2[2 * i + 1] = mul1(ba, h0); }
          else { p1[2 * i] = add1(p1[2 * i], bf_lo(pa)); p1[2 * i + 1] = add1(p1[2 * i + 1], bf_hi(pa)); p2[2 * i] = fma1(ba, l0, p2[2 * i]); p2[2 * i + 1] = fma1(ba, h0, p2[2 * i + 1]); }
        }
      }
      if constexpr (st == 2) {
        float a[8];
#pragma unroll
        for (int i = 0; i < 8; ++i) {
          auto rr = __builtin_amdgcn_permlane32_swap(__float_as_uint(p1[i]), __float_as_uint(p2[i]), false, false);
          a[i] = __uint_as_float(rr[0]) + __uint_as_float(rr[1]);
        }
        float cs[4];
#pragma unroll
        for (int i = 0; i < 4; ++i) {
          auto rr = __builtin_amdgcn_permlane16_swap(__float_as_uint(a[i]), __float_as_uint(a[4 + i]), false, false);
          cs[i] = sum16(__uint_as_float(rr[0]) + __uint_as_float(rr[1]));
        }
        pub = l4 == 0 ? cs[0] : (l4 == 1 ? cs[1] : (l4 == 2 ? cs[2] : cs[3]));
        epr = (unsigned)__builtin_amdgcn_readlane((int)ep, 0); epc = (unsigned)__builtin_amdgcn_readlane((int)ep, 1);
      }
      if constexpr (st <= 1) { PIN8(p1); PIN8(p2); }
      if constexpr (st == 2) asm volatile("" : "+v"(pub));
      if constexpr (st >= 2 && st <= 4) {
        constexpr int j0 = st == 2 ? 0 : (st == 3 ? 1 : 4), j1 = st == 2 ? 1 : (st == 3 ? 4 : 8);
        f32x4 s4 = {part, 0.f, 0.f, 0.f};
#pragma unroll
        for (int j = j0; j < j1; ++j) {
          f[2 * j] = (f32x4){bf_lo(xr[j][0]), bf_hi(xr[j][0]), bf_lo(xr[j][1]), bf_hi(xr[j][1])};
          f[2 * j + 1] = (f32x4){bf_lo(xr[j][2]), bf_hi(xr[j][2]), bf_lo(xr[j][3]), bf_hi(xr[j][3])};
#pragma unroll
          for (int e = 0; e < 4; ++e) { s4[e] = add1(s4[e], f[2 * j][e]); s4[e] = add1(s4[e], f[2 * j + 1][e]); }
        }
        part = add1(add1(s4[0], s4[1]), add1(s4[2], s4[3]));
        if constexpr (st == 4) { mean = dpp_add<0xB1>(part) * (1.f / 128.f); part = 0.f; }
        asm volatile("" : "+v"(part), "+v"(mean));
#pragma unroll
        for (int j = 2 * j0; j < 2 * j1; ++j) asm volatile("" : "+v"(f[j]));
      }
      if constexpr (st >= 5 && st <= 7) {
        constexpr int j0 = (st - 5) * 6, j1 = st == 7 ? 16 : j0 + 6;
        f32x4 s4 = {part, 0.f, 0.f, 0.f};
#pragma unroll
        for (int j = j0; j < j1; ++j)
#pragma unroll
          for (int e = 0; e < 4; ++e) { const float dd = add1(f[j][e], -mean); s4[e] = fma1(dd, dd, s4[e]); }
        part = add1(add1(s4[0], s4[1]), add1(s4[2], s4[3]));
        asm volatile("" : "+v"(part));
      }
      SCHED();
      if constexpr (st == 3) {
        if (l4 < 4) store_granule(ws, WS_C + ((size_t)((ni * 8 + slab) * 128 + scol + ((lane >> 4) & 1) * 4 + l4) * 2 + (lane >> 5)) * 8, epc + 1u, pub);
      }
      if constexpr (st == 7) {
        const float m2 = dpp_add<0xB1>(part);
        store_granule(ws, WS_RS + ((size_t)(mi * 8 + slab) * 256 + t) * 8, epr + 1u, (t & 1) ? m2 : mean);
      }
      if constexpr (st == 8) { if (d < 2) gather(0, 4); }
      if constexpr (st == 9) { if (d < 2) gather(4, 8); }
      if constexpr (st == 10) { if (d >= 2) gather(0, 4); }
      if constexpr (st == 11) { if (d >= 2) gather(4, 8); }
      if constexpr (st == 13) { if (d >= 2) { combine(epr + 1u); asm volatile("" : "+v"(v0), "+v"(v1), "+v"(bad)); } }
      if constexpr (st == 11) { if (d < 2) { combine(epc + 1u); asm volatile("" : "+v"(v0), "+v"(v1), "+v"(bad)); } }
      if constexpr (st == 14) {
        const unsigned tag = (d < 2 ? epc : epr) + 1u;
        bool ok = bad == 0u;
        if (__builtin_expect(!__all(ok), 0)) {
#pragma unroll 1
          for (int it = 0; it < 20000; ++it) {
            asm volatile("" ::: "memory");
            gather(0, 8);
            combine(tag);
            ok = bad == 0u;
            if (__all(ok)) break;
            __builtin_amdgcn_s_sleep(2);
          }
        }
        if (__builtin_expect(!__all(ok), 0)) {
          v0 = 0.f; v1 = 0.f;
          if (d < 2) {
            const u16* wc = w + ni * 128 + t;
#pragma unroll 1
            for (int k = 0; k < KD; ++k) {
              const float wv = bf_lo(wc[(size_t)k * ND]);
              v0 += bf_lo(cvt_pk(bf_lo(g[k]) * wv, 0.f)); v1 = fmaf(bf_lo(b[k]), wv, v1);
            }
          } else {
            const u16* xrow = x + (size_t)(mi * 128 + (t - 128)) * KD;
#pragma unroll 1
            for (int k = 0; k < KD; ++k) v0 += bf_lo(xrow[k]);
            v0 *= (1.f / 1024.f);
#pragma unroll 1
            for (int k = 0; k < KD; ++k) { const float dd = bf_lo(xrow[k]) - v0; v1 = fmaf(dd, dd, v1); }
            v1 = __builtin_amdgcn_rsqf(v1 * (1.f / 1024.f) + 1e-5f);
          }
        }
        if (d < 2) { *(LAS float*)(lds0 + LDS_TAB + 1024 + t * 4) = v0; *(LAS float*)(lds0 + LDS_TAB + 1536 + t * 4) = v1; }
        else *(LAS f32x2*)(lds0 + LDS_TAB + (t - 128) * 8) = (f32x2){v0, v1};
      }
      if constexpr (st == 14) { STAMP(23); BARRIER_L(); } else BARRIER_N();
      STAMP(3 + st);
    });
    if (t == 0) {
      __hip_atomic_fetch_add((GAS unsigned*)(ws + WS_EPR + mi * 128), 1u, __ATOMIC_RELAXED, __HIP_MEMORY_SCOPE_AGENT);
      __hip_atomic_fetch_add((GAS unsigned*)(ws + WS_EPC + ni * 128), 1u, __ATOMIC_RELAXED, __HIP_MEMORY_SCOPE_AGENT);
    }
  } else if (wave >= 4) {
    const int d = wave - 4;
    if (PRIO_L) __builtin_amdgcn_s_setprio(PRIO_L);
    unsigned xo[4], wo[4];
#pragma unroll
    for (int i = 0; i < 4; ++i) {
      const int row = i * 8 + (lane >> 3);
      xo[i] = (unsigned)row * 2048u + (unsigned)(((lane & 7) ^ ((row >> 1) & 7)) * 16) - (unsigned)i * 1024u;
      const int kr = i * 4 + (lane >> 4);
      wo[i] = (unsigned)kr * 2048u + (unsigned)(((lane & 15) ^ (((lane >> 4) << 2) | i)) * 16) - (unsigned)i * 1024u;
    }
    const unsigned dst = lds0 + (unsigned)d * 4096u;
    const char* xb = (const char*)x + (size_t)mi * 262144 + d * 65536;
    const char* wb = (const char*)w + d * 32768 + ni * 256;
    auto issue = [&](int st) {
      const unsigned ds_ = dst + (unsigned)(st & (RING - 1)) * SLOT;
      const int ks_ = (st + rot) & (NST - 1);
      glds16x4(wo[0], wo[1], wo[2], wo[3], wb + (size_t)ks_ * 131072, ds_ + 16384u);
      glds16x4(xo[0], xo[1], xo[2], xo[3], xb + ks_ * 128, ds_);
    };
    issue(0);
    if (d < 2) glds4((unsigned)lane * 4u, (const char*)(d ? g : b) + rot * 128, lds0 + (d ? LDS_G : LDS_B));
    issue(1);
    STAMP(2);
    wait_vm<8>();
    STAMP(3);
    BARRIER_N();
    issue(2); issue(3); wait_vm<16>(); BARRIER_N();
    wait_vm<8>(); BARRIER_N();
#pragma unroll 1
    for (int j = 2; j < NST - 2; ++j) {
      issue(j + 2);
      wait_vm<8>();
      BARRIER_N();
      STAMP_DYN(j + 4);
    }
    wait_vm<0>();
    BARRIER_N();
    STAMP(18);
  } else {
    const int wn = wave;
    {
      const u32x2 g4 = *(const GAS u32x2*)((const char*)g + ((tid * 8 + rot * 128) & 2047));
      const unsigned a = lds0 + LDS_GT + (unsigned)(((tid >> 1) & 1) * 4 * GROW + (tid >> 2) * 4 + (tid & 1) * 2);
      *(LAS u16*)(a) = (u16)g4[0]; *(LAS u16*)(a + GROW) = (u16)(g4[0] >> 16);
      *(LAS u16*)(a + 2 * GROW) = (u16)g4[1]; *(LAS u16*)(a + 3 * GROW) = (u16)(g4[1] >> 16);
    }
    {
      const unsigned co = (unsigned)wn * 3072u + (unsigned)lane * 16u, lim = (code_len & ~15u) - 16u;
      const u32x4 t0 = *(const GAS u32x4*)(code_pc + min(co, lim)) ^ *(const GAS u32x4*)(code_pc + min(co + 1024u, lim)) ^ *(const GAS u32x4*)(code_pc + min(co + 2048u, lim));
      asm volatile("" :: "v"(t0));
    }
    STAMP(3);
    if (PRIO_M) __builtin_amdgcn_s_setprio(PRIO_M);
    const int r = lane & 31, h = lane >> 5, sw = (r >> 1) & 7;
    unsigned offx[4], offw[2];
#pragma unroll
    for (int ks = 0; ks < 4; ++ks) offx[ks] = lds0 + (unsigned)r * 128u + (unsigned)(((2 * ks + h) ^ sw) * 16);
    {
      const int blk = (lane >> 4) & 1, q = (lane & 15) >> 2, p = lane & 3;
#pragma unroll
      for (int tt = 0; tt < 2; ++tt) {
        const int row = 8 * h + 4 * tt + q, ch = 4 * wn + blk + 2 * (p & 1);
        offw[tt] = lds0 + 16384u + (unsigned)(256 * row + 16 * (ch ^ ((q << 2) | ((2 * h + tt) & 3))) + 8 * (p >> 1));
      }
    }
    unsigned offg = lds0 + LDS_GT + (unsigned)((h * 4 + (lane & 3)) * GROW);
    const unsigned li = lane & 3;
    unsigned sel[4] = {li == 0 ? 0x0c0c0100u : (li == 1 ? 0x01000c0cu : 0x0c0c0c0cu), li == 2 ? 0x0c0c0100u : (li == 3 ? 0x01000c0cu : 0x0c0c0c0cu),
                       li == 0 ? 0x0c0c0302u : (li == 1 ? 0x03020c0cu : 0x0c0c0c0cu), li == 2 ? 0x0c0c0302u : (li == 3 ? 0x03020c0cu : 0x0c0c0c0cu)};
    f32x16 acc[4] = {};
    bf16x8 xf[4];
    s16x4 wra[2], wrb[2];
    u32x2 ga, gb;
    u32x4 dg;
    u32x4 wf, wfn;
    f32x4 d0, d1;
    const f32x4 z4 = {0.f, 0.f, 0.f, 0.f};
#define RX(mt, ks) xf[mt] = *(const LAS bf16x8*)(offx[ks] + (mt) * 4096)
#define RW(R, tt, ks) R[tt] = __builtin_amdgcn_ds_read_tr16_b64_v4i16((LAS s16x4*)(offw[tt] + (ks) * 4096))
#define RG(G, o) G = *(const LAS u32x2*)(offg + (o))
#define DG0(G) do { dg[0] = __builtin_amdgcn_perm(G, G, sel[0]); dg[1] = __builtin_amdgcn_perm(G, G, sel[1]); } while (0)
#define DG1(G) do { dg[2] = __builtin_amdgcn_perm(G, G, sel[2]); dg[3] = __builtin_amdgcn_perm(G, G, sel[3]); } while (0)
#define SM0(R) d0 = __builtin_amdgcn_mfma_f32_4x4x4bf16_1k(__builtin_bit_cast(s16x4, (u32x2){dg[0], dg[1]}), R[0], z4, 0, 0, 0)
#define SM1(R) d1 = __builtin_amdgcn_mfma_f32_4x4x4bf16_1k(__builtin_bit_cast(s16x4, (u32x2){dg[2], dg[3]}), R[1], z4, 0, 0, 0)
#define CV0(W) do { W[0] = cvt_pk(d0[0], d0[1]); W[1] = cvt_pk(d0[2], d0[3]); } while (0)
#define CV1(W) do { W[2] = cvt_pk(d1[0], d1[1]); W[3] = cvt_pk(d1[2], d1[3]); } while (0)
#define MM(mt, W) acc[mt] = __builtin_amdgcn_mfma_f32_32x32x16_bf16(__builtin_bit_cast(bf16x8, W), xf[mt], acc[mt], 0, 0, 0)
#define STEP(WC, WN, RA, RB, kx, kw, G, EXTRA2, EXTRA3) do { \
      SM0(RA); SM1(RA); SCHED(); \
      MM(0, WC); SCHED(); RX(0, kx); RW(RB, 0, kw); DG0(G); SCHED(); \
      MM(1, WC); SCHED(); RX(1, kx); RW(RB, 1, kw); DG1(G); SCHED(); \
      MM(2, WC); SCHED(); RX(2, kx); CV0(WN); EXTRA2; SCHED(); \
      MM(3, WC); SCHED(); RX(3, kx); CV1(WN); EXTRA3; SCHED(); } while (0)
    asm volatile("" : "+v"(acc[0]), "+v"(acc[1]), "+v"(acc[2]), "+v"(acc[3]));
    asm volatile("" : "+v"(offx[0]), "+v"(offx[1]), "+v"(offx[2]), "+v"(offx[3]), "+v"(offw[0]), "+v"(offw[1]), "+v"(offg),
                      "+v"(sel[0]), "+v"(sel[1]), "+v"(sel[2]), "+v"(sel[3]));
    __builtin_amdgcn_s_waitcnt(0xC07F);
    STAMP(4);
    BARRIER_N();
    STAMP(5);
    RG(gb, 0); RW(wrb, 0, 0); RW(wrb, 1, 0); RX(0, 0); RW(wra, 0, 1); RW(wra, 1, 1); RX(1, 0); RX(2, 0); RX(3, 0); RG(ga, 8);
    DG0(gb[0]); DG1(gb[0]); SCHED(); SM0(wrb); SM1(wrb); SCHED();
    DG0(gb[1]); DG1(gb[1]); CV0(wf); CV1(wf); SCHED();
#pragma unroll 1
    for (int s = 0; s < NST - 1; ++s) {
      const int dlt = ((s & (RING - 1)) == RING - 1) ? -(RING - 1) * SLOT : SLOT;
      STEP(wf, wfn, wra, wrb, 1, 2, ga[0], (RG(gb, 16)), (void)0);
      STEP(wfn, wf, wrb, wra, 2, 3, ga[1], (offx[0] += dlt, offw[0] += dlt), (offw[1] += dlt, offx[1] += dlt));
      BARRIER_N();
      STEP(wf, wfn, wra, wrb, 3, 0, gb[0], (RG(ga, 24), offx[2] += dlt), (void)0);
      STEP(wfn, wf, wrb, wra, 0, 1, gb[1], (offx[3] += dlt), (offg += 16u));
    }
    f32x4 c1v[4], c2v[4];
    f32x2 rs[4];
    const unsigned tc = lds0 + LDS_TAB + 1024 + (unsigned)(wn * 32 + h * 16) * 4u, tr = lds0 + LDS_TAB + (unsigned)r * 8u;
#define RC(q) do { c1v[q] = *(const LAS f32x4*)(tc + (q) * 16); c2v[q] = *(const LAS f32x4*)(tc + 512 + (q) * 16); rs[q] = *(const LAS f32x2*)(tr + (q) * 256); } while (0)
    STEP(wf, wfn, wra, wrb, 1, 2, ga[0], (void)0, (void)0);
    STEP(wfn, wf, wrb, wra, 2, 3, ga[1], (void)0, (void)0);
    SM0(wra); SM1(wra); SCHED();
    MM(0, wf); SCHED(); RX(0, 3); SCHED();
    MM(1, wf); SCHED(); RX(1, 3); SCHED();
    MM(2, wf); SCHED(); RX(2, 3); CV0(wfn); SCHED();
    MM(3, wf); SCHED(); RX(3, 3); CV1(wfn); SCHED();
    MM(0, wfn); SCHED(); RC(0); SCHED(); MM(1, wfn); SCHED(); RC(1); SCHED(); MM(2, wfn); SCHED(); RC(2); SCHED(); MM(3, wfn); SCHED(); RC(3); SCHED();
    STAMP(20);
    const unsigned so = lds0 + (unsigned)r * 256u, sc0 = (unsigned)(((wn * 4 + h * 2) ^ (r & 7)) * 16), sc1 = sc0 ^ 16u;
#pragma unroll
    for (int mt = 0; mt < 4; ++mt) {
      const float nm = -rs[mt][0], rstd = rs[mt][1];
      f32x4 tq[4];
#pragma unroll
      for (int q = 0; q < 4; ++q) {
        const f32x4 av = {acc[mt][4 * q], acc[mt][4 * q + 1], acc[mt][4 * q + 2], acc[mt][4 * q + 3]};
        tq[q] = av + c1v[q] * nm;
      }
#pragma unroll
      for (int q = 0; q < 4; ++q) tq[q] = tq[q] * rstd + c2v[q];
      const u32x4 o0 = {cvt_pk(tq[0][0], tq[0][1]), cvt_pk(tq[0][2], tq[0][3]), cvt_pk(tq[1][0], tq[1][1]), cvt_pk(tq[1][2], tq[1][3])};
      const u32x4 o1 = {cvt_pk(tq[2][0], tq[2][1]), cvt_pk(tq[2][2], tq[2][3]), cvt_pk(tq[3][0], tq[3][1]), cvt_pk(tq[3][2], tq[3][3])};
      *(LAS u32x4*)(so + mt * 8192 + sc0) = o0;
      *(LAS u32x4*)(so + mt * 8192 + sc1) = o1;
      SCHED();
      if (mt == 1) BARRIER_L();
    }
  }
  if (wave >= 4) {
    BARRIER_N();
    store_rows<2>(lds0, (wave - 4) * 8, lane, (char*)out + (size_t)mi * 262144 + ni * 256);
  }
  STAMP(24);
  BARRIER_L();
  STAMP(25);
  if (wave < 8) store_rows<2>(lds0, 64 + wave * 8, lane, (char*)out + (size_t)mi * 262144 + ni * 256);
  STAMP_FLUSH();
  asm volatile("lnlin_code_end:");
}

extern "C" void kernel_launch(void* const* d_in, const int* in_sizes, int n_in, void* d_out, int out_size, void* d_ws, size_t ws_size,
                              hipStream_t stream) {
  const u16* x = (const u16*)d_in[0]; const u16* g = (const u16*)d_in[1]; const u16* b = (const u16*)d_in[2]; const u16* w = (const u16*)d_in[3];
  (void)hipFuncSetAttribute((const void*)lnlin_kernel, hipFuncAttributeMaxDynamicSharedMemorySize, LDS_TOTAL);
  lnlin_kernel<<<256, 768, LDS_TOTAL, stream>>>(x, g, b, w, (u16*)d_out, (char*)d_ws);
}
```

```cpp
#include <hip/hip_runtime.h>
#include <hip/hip_bf16.h>
#include <utility>
#pragma clang diagnostic ignored "-Wint-to-pointer-cast"

typedef short bf16x8 __attribute__((ext_vector_type(8)));
typedef short s16x4 __attribute__((ext_vector_type(4)));
typedef float f32x16 __attribute__((ext_vector_type(16)));
typedef float f32x4 __attribute__((ext_vector_type(4)));
typedef float f32x2 __attribute__((ext_vector_type(2)));
typedef unsigned u32x4 __attribute__((ext_vector_type(4)));
typedef unsigned u32x2 __attribute__((ext_vector_type(2)));
typedef __bf16 bf16x2_t __attribute__((ext_vector_type(2)));
typedef unsigned short u16;
typedef unsigned long long u64;
#define GAS __attribute__((address_space(1)))
#define LAS __attribute__((address_space(3)))

#define ST_FLAVOR "nt"
#define PRIO_M 2
#define PRIO_L 0

constexpr int KD = 1024, ND = 1024;
constexpr int NST = 16;
constexpr int RING = 4;
constexpr int SLOT = 32768;
constexpr int GROW = 272;
constexpr int LDS_GT = RING * SLOT;
constexpr int LDS_G = LDS_GT + 8 * GROW;
constexpr int LDS_B = LDS_G + 256;
constexpr int LDS_TAB = LDS_B + 256;
constexpr int LDS_TOTAL = LDS_TAB + 2048;

constexpr size_t WS_EPR = 0;
constexpr size_t WS_EPC = 32 * 128;
constexpr size_t WS_C = 8192;
constexpr size_t WS_RS = WS_C + 8 * 16384;

template <int B, int E, class F> __device__ __forceinline__ void static_for(F&& f) {
  if constexpr (B < E) { f(std::integral_constant<int, B>{}); static_for<B + 1, E>(f); }
}
__device__ __forceinline__ float bf_lo(unsigned u) { return __uint_as_float(u << 16); }
__device__ __forceinline__ float bf_hi(unsigned u) { return __uint_as_float(u & 0xffff0000u); }
__device__ __forceinline__ unsigned cvt_pk(float lo, float hi) {
  f32x2 v = {lo, hi}; return __builtin_bit_cast(unsigned, __builtin_convertvector(v, bf16x2_t));
}
template <int CTRL> __device__ __forceinline__ float dpp_add(float x) {
  return x + __builtin_bit_cast(float, __builtin_amdgcn_update_dpp(0, __builtin_bit_cast(int, x), CTRL, 0xf, 0xf, true));
}
__device__ __forceinline__ float sum16(float x) {
  x = dpp_add<0x128>(x); x = dpp_add<0x124>(x); x = dpp_add<0x122>(x); x = dpp_add<0x121>(x);
  return x;
}
__device__ __forceinline__ float add1(float a, float b) { float r; asm("v_add_f32 %0, %1, %2" : "=v"(r) : "v"(a), "v"(b)); return r; }
__device__ __forceinline__ float mul1(float a, float b) { float r; asm("v_mul_f32 %0, %1, %2" : "=v"(r) : "v"(a), "v"(b)); return r; }
__device__ __forceinline__ float fma1(float a, float b, float c) { float r; asm("v_fma_f32 %0, %1, %2, %3" : "=v"(r) : "v"(a), "v"(b), "v"(c)); return r; }
#define PIN8(a) asm volatile("" : "+v"(a[0]), "+v"(a[1]), "+v"(a[2]), "+v"(a[3]), "+v"(a[4]), "+v"(a[5]), "+v"(a[6]), "+v"(a[7]))
#define SCHED() __builtin_amdgcn_sched_barrier(0)
#define BARRIER_L() do { SCHED(); asm volatile("s_waitcnt lgkmcnt(0)\n\ts_barrier" ::: "memory"); SCHED(); } while (0)
#define BARRIER_N() do { SCHED(); asm volatile("s_barrier" ::: "memory"); SCHED(); } while (0)
template <int N> __device__ __forceinline__ void wait_vm() { asm volatile("s_waitcnt vmcnt(%0)" ::"n"(N) : "memory"); }

__device__ __forceinline__ void glds16x4(unsigned v0, unsigned v1, unsigned v2, unsigned v3, const void* sbase, unsigned lds_dst) {
  unsigned keep;
  asm volatile("s_mov_b32 %0, m0\n\ts_mov_b32 m0, %6\n\ts_nop 0\n\t"
               "global_load_lds_dwordx4 %1, %5\n\tglobal_load_lds_dwordx4 %2, %5 offset:1024\n\t"
               "global_load_lds_dwordx4 %3, %5 offset:2048\n\tglobal_load_lds_dwordx4 %4, %5 offset:3072\n\ts_mov_b32 m0, %0"
               : "=&s"(keep) : "v"(v0), "v"(v1), "v"(v2), "v"(v3), "s"(sbase), "s"(lds_dst) : "memory");
}
__device__ __forceinline__ void glds4(unsigned v0, const void* sbase, unsigned lds_dst) {
  unsigned keep;
  asm volatile("s_mov_b32 %0, m0\n\ts_mov_b32 m0, %3\n\ts_nop 0\n\tglobal_load_lds_dword %1, %2\n\ts_mov_b32 m0, %0"
               : "=&s"(keep) : "v"(v0), "s"(sbase), "s"(lds_dst) : "memory");
}
__device__ __forceinline__ void store_granule(char* ws, size_t off, unsigned tag, float v) {
  __hip_atomic_store((GAS u64*)(ws + off), ((u64)tag << 32) | __float_as_uint(v), __ATOMIC_RELAXED, __HIP_MEMORY_SCOPE_AGENT);
}
__device__ __forceinline__ unsigned load_word(const char* ws, size_t off) {
  return __hip_atomic_load((GAS unsigned*)(ws + off), __ATOMIC_RELAXED, __HIP_MEMORY_SCOPE_AGENT);
}

template <int N> __device__ __forceinline__ void store_rows(unsigned lds0, int rowb, int lane, char* ob) {
  const int row0 = rowb + (lane >> 4), ch = lane & 15;
  u32x4 v[N];
#pragma unroll
  for (int i = 0; i < N; ++i) { const int row = row0 + i * 4; v[i] = *(const LAS u32x4*)(lds0 + row * 256 + ((ch ^ (row & 7)) * 16)); }
  const unsigned oo = (unsigned)row0 * 2048u + (unsigned)ch * 16u;
#pragma unroll
  for (int i = 0; i < N; ++i)
    asm volatile("global_store_dwordx4 %0, %1, %2 " ST_FLAVOR "\n\ts_nop 1" :: "v"(oo + i * 8192u), "v"(v[i]), "s"(ob) : "memory");
}

__global__ __launch_bounds__(768, 3) void lnlin_kernel(const u16* __restrict__ x, const u16* __restrict__ g, const u16* __restrict__ b,
                                                       const u16* __restrict__ w, u16* __restrict__ out, char* ws) {
  extern __shared__ __attribute__((aligned(16))) char smem_g[];
  const int bid = blockIdx.x, xcd = bid & 7, idx = bid >> 3;
  const int mi = xcd * 4 + (idx >> 3), ni = idx & 7;
  const int slab = ((mi >> 2) + 2 * (ni & 3)) & 7, rot = slab * 2, sp = ni >> 2, rslab = (slab + sp) & 7;
  const int tid = threadIdx.x, lane = tid & 63;
  const int wave = __builtin_amdgcn_readfirstlane(tid >> 6);
  const unsigned lds0 = (unsigned)(size_t)(LAS char*)smem_g;

  if (wave >= 8) {
    const int d = wave - 8, t = tid - 512;
    const int scol = (mi & 3) * 32 + d * 8;
    const unsigned cw = lds0 + 16384u + (unsigned)(256 * lane + 16 * ((scol >> 3) ^ (((lane & 3) << 2) | ((lane >> 2) & 3))));
    const int srow = t >> 1, l4 = lane & 15;
    const unsigned sx = lds0 + (unsigned)srow * 128u, ssw = (unsigned)(((t & 1) * 4) ^ ((srow >> 1) & 7));
    u32x4 xr[8];
    f32x4 f[16];
    unsigned ep;
    float p1[8], p2[8], part = 0.f, mean = 0.f, pub = 0.f, v0 = 0.f, v1 = 0.f;
    unsigned bad = 0u;
    u32x4 q[8];
    const unsigned gsrc = d < 2 ? (unsigned)WS_C + (unsigned)ni * 16384u + (unsigned)t * 16u : (unsigned)WS_RS + (unsigned)mi * 16384u + (unsigned)(t - 128) * 16u;
    auto gather = [&](int j0, int j1) __attribute__((always_inline)) {
#pragma unroll
      for (int j = j0; j < j1; ++j)
        q[j] = __builtin_amdgcn_raw_buffer_load_b128(__builtin_amdgcn_make_buffer_rsrc((void*)ws, 0, 0x100000, 0x00020000), gsrc + j * 2048u, 0, 16  );
    };
    auto combine = [&](unsigned tag) __attribute__((always_inline)) {
      bad = 0u; v0 = 0.f; v1 = 0.f;
#pragma unroll
      for (int j = 0; j < 8; ++j) { bad |= (q[j][1] ^ tag) | (q[j][3] ^ tag); v0 = add1(v0, __uint_as_float(q[j][0])); v1 = add1(v1, __uint_as_float(q[j][2])); }
      if (d >= 2) {
        v0 *= 0.125f;
        float dv = 0.f;
#pragma unroll
        for (int j = 0; j < 8; ++j) { const float e = __uint_as_float(q[j][0]) - v0; dv = fmaf(e, e, dv); }
        v1 = __builtin_amdgcn_rsqf(fmaf(dv, 128.f, v1) * (1.f / 1024.f) + 1e-5f);
      }
    };
    ep = load_word(ws, (lane & 1) ? WS_EPC + ni * 128 : WS_EPR + mi * 128);
    BARRIER_N();
    unsigned epr = 0, epc = 0;
    static_for<0, NST - 1>([&](auto S) {
      constexpr int st = S;
      if constexpr (st <= 3) {
        if ((st >> 1) == sp) {
#pragma unroll
          for (int i = 0; i < 4; ++i) xr[(st & 1) * 4 + i] = *(const LAS u32x4*)(sx + st * SLOT + ((ssw ^ i) * 16u));
        }
      }
      if constexpr (st <= 1) {
        const u32x4 wv = *(const LAS u32x4*)(cw + st * SLOT);
        const float ga = bf_lo(*(const LAS u16*)(lds0 + LDS_G + st * 128 + lane * 2)), ba = bf_lo(*(const LAS u16*)(lds0 + LDS_B + st * 128 + lane * 2));
#pragma unroll
        for (int i = 0; i < 4; ++i) {
          const float l0 = bf_lo(wv[i]), h0 = bf_hi(wv[i]);
          const unsigned pa = cvt_pk(mul1(ga, l0), mul1(ga, h0));
          if constexpr (st == 0) { p1[2 * i] = bf_lo(pa); p1[2 * i + 1] = bf_hi(pa); p2[2 * i] = mul1(ba, l0); p2[2 * i + 1] = mul1(ba, h0); }
          else { p1[2 * i] = add1(p1[2 * i], bf_lo(pa)); p1[2 * i + 1] = add1(p1[2 * i + 1], bf_hi(pa)); p2[2 * i] = fma1(ba, l0, p2[2 * i]); p2[2 * i + 1] = fma1(ba, h0, p2[2 * i + 1]); }
        }
      }
      if constexpr (st == 2) {
        float a[8];
#pragma unroll
        for (int i = 0; i < 8; ++i) {
          auto rr = __builtin_amdgcn_permlane32_swap(__float_as_uint(p1[i]), __float_as_uint(p2[i]), false, false);
          a[i] = __uint_as_float(rr[0]) + __uint_as_float(rr[1]);
        }
        float cs[4];
#pragma unroll
        for (int i = 0; i < 4; ++i) {
          auto rr = __builtin_amdgcn_permlane16_swap(__float_as_uint(a[i]), __float_as_uint(a[4 + i]), false, false);
          cs[i] = sum16(__uint_as_float(rr[0]) + __uint_as_float(rr[1]));
        }
        pub = l4 == 0 ? cs[0] : (l4 == 1 ? cs[1] : (l4 == 2 ? cs[2] : cs[3]));
        epr = (unsigned)__builtin_amdgcn_readlane((int)ep, 0); epc = (unsigned)__builtin_amdgcn_readlane((int)ep, 1);
      }
      if constexpr (st <= 1) { PIN8(p1); PIN8(p2); }
      if constexpr (st == 2) asm volatile("" : "+v"(pub));
      if constexpr (st >= 3 && st <= 5) {
        constexpr int j0 = (st - 3) * 3, j1 = st == 5 ? 8 : j0 + 3;
        f32x4 s4 = {part, 0.f, 0.f, 0.f};
#pragma unroll
        for (int j = j0; j < j1; ++j) {
          f[2 * j] = (f32x4){bf_lo(xr[j][0]), bf_hi(xr[j][0]), bf_lo(xr[j][1]), bf_hi(xr[j][1])};
          f[2 * j + 1] = (f32x4){bf_lo(xr[j][2]), bf_hi(xr[j][2]), bf_lo(xr[j][3]), bf_hi(xr[j][3])};
#pragma unroll
          for (int e = 0; e < 4; ++e) { s4[e] = add1(s4[e], f[2 * j][e]); s4[e] = add1(s4[e], f[2 * j + 1][e]); }
        }
        part = add1(add1(s4[0], s4[1]), add1(s4[2], s4[3]));
        if constexpr (st == 5) { mean = dpp_add<0xB1>(part) * (1.f / 128.f); part = 0.f; }
        asm volatile("" : "+v"(part), "+v"(mean));
#pragma unroll
        for (int j = 2 * j0; j < 2 * j1; ++j) asm volatile("" : "+v"(f[j]));
      }
      if constexpr (st >= 5 && st <= 7) {
        constexpr int j0 = st == 5 ? 0 : (st == 6 ? 2 : 9), j1 = st == 5 ? 2 : (st == 6 ? 9 : 16);
        f32x4 s4 = {part, 0.f, 0.f, 0.f};
#pragma unroll
        for (int j = j0; j < j1; ++j)
#pragma unroll
          for (int e = 0; e < 4; ++e) { const float dd = add1(f[j][e], -mean); s4[e] = fma1(dd, dd, s4[e]); }
        part = add1(add1(s4[0], s4[1]), add1(s4[2], s4[3]));
        asm volatile("" : "+v"(part));
      }
      SCHED();
      if constexpr (st == 3) {
        if (l4 < 4) store_granule(ws, WS_C + ((size_t)((ni * 8 + slab) * 128 + scol + ((lane >> 4) & 1) * 4 + l4) * 2 + (lane >> 5)) * 8, epc + 1u, pub);
      }
      if constexpr (st == 7) {
        const float m2 = dpp_add<0xB1>(part);
        store_granule(ws, WS_RS + ((size_t)(mi * 8 + rslab) * 256 + t) * 8, epr + 1u, (t & 1) ? m2 : mean);
      }
      if constexpr (st == 8) { if (d < 2) gather(0, 4); }
      if constexpr (st == 9) { if (d < 2) gather(4, 8); }
      if constexpr (st == 10) { if (d >= 2) gather(0, 4); }
      if constexpr (st == 11) { if (d >= 2) gather(4, 8); }
      if constexpr (st == 13) { if (d >= 2) { combine(epr + 1u); asm volatile("" : "+v"(v0), "+v"(v1), "+v"(bad)); } }
      if constexpr (st == 11) { if (d < 2) { combine(epc + 1u); asm volatile("" : "+v"(v0), "+v"(v1), "+v"(bad)); } }
      if constexpr (st == 14) {
        const unsigned tag = (d < 2 ? epc : epr) + 1u;
        bool ok = bad == 0u;
        if (__builtin_expect(!__all(ok), 0)) {
#pragma unroll 1
          for (int it = 0; it < 20000; ++it) {
            asm volatile("" ::: "memory");
            gather(0, 8);
            combine(tag);
            ok = bad == 0u;
            if (__all(ok)) break;
            __builtin_amdgcn_s_sleep(2);
          }
        }
        if (__builtin_expect(!__all(ok), 0)) {
          v0 = 0.f; v1 = 0.f;
          if (d < 2) {
            const u16* wc = w + ni * 128 + t;
#pragma unroll 1
            for (int k = 0; k < KD; ++k) {
              const float wv = bf_lo(wc[(size_t)k * ND]);
              v0 += bf_lo(cvt_pk(bf_lo(g[k]) * wv, 0.f)); v1 = fmaf(bf_lo(b[k]), wv, v1);
            }
          } else {
            const u16* xrow = x + (size_t)(mi * 128 + (t - 128)) * KD;
#pragma unroll 1
            for (int k = 0; k < KD; ++k) v0 += bf_lo(xrow[k]);
            v0 *= (1.f / 1024.f);
#pragma unroll 1
            for (int k = 0; k < KD; ++k) { const float dd = bf_lo(xrow[k]) - v0; v1 = fmaf(dd, dd, v1); }
            v1 = __builtin_amdgcn_rsqf(v1 * (1.f / 1024.f) + 1e-5f);
          }
        }
        if (d < 2) { *(LAS float*)(lds0 + LDS_TAB + 1024 + t * 4) = v0; *(LAS float*)(lds0 + LDS_TAB + 1536 + t * 4) = v1; }
        else *(LAS f32x2*)(lds0 + LDS_TAB + (t - 128) * 8) = (f32x2){v0, v1};
      }
      if constexpr (st <= 3 || st == 14) BARRIER_L(); else BARRIER_N();
    });
    if (t == 0) {
      __hip_atomic_fetch_add((GAS unsigned*)(ws + WS_EPR + mi * 128), 1u, __ATOMIC_RELAXED, __HIP_MEMORY_SCOPE_AGENT);
      __hip_atomic_fetch_add((GAS unsigned*)(ws + WS_EPC + ni * 128), 1u, __ATOMIC_RELAXED, __HIP_MEMORY_SCOPE_AGENT);
    }
  } else if (wave >= 4) {
    const int d = wave - 4;
    if (PRIO_L) __builtin_amdgcn_s_setprio(PRIO_L);
    unsigned xo[4], wo[4];
#pragma unroll
    for (int i = 0; i < 4; ++i) {
      const int row = i * 8 + (lane >> 3);
      xo[i] = (unsigned)row * 2048u + (unsigned)(((lane & 7) ^ ((row >> 1) & 7)) * 16) - (unsigned)i * 1024u;
      const int kr = i * 4 + (lane >> 4);
      wo[i] = (unsigned)kr * 2048u + (unsigned)(((lane & 15) ^ (((lane >> 4) << 2) | i)) * 16) - (unsigned)i * 1024u;
    }
    const unsigned dst = lds0 + (unsigned)d * 4096u;
    const char* xb = (const char*)x + (size_t)mi * 262144 + d * 65536;
    const char* wb = (const char*)w + d * 32768 + ni * 256;
    auto issue = [&](int st) {
      const unsigned ds_ = dst + (unsigned)(st & (RING - 1)) * SLOT;
      const int ks_ = (st + rot) & (NST - 1);
      glds16x4(wo[0], wo[1], wo[2], wo[3], wb + (size_t)ks_ * 131072, ds_ + 16384u);
      glds16x4(xo[0], xo[1], xo[2], xo[3], xb + ks_ * 128, ds_);
    };
    issue(0);
    if (d < 2) glds4((unsigned)lane * 4u, (const char*)(d ? g : b) + rot * 128, lds0 + (d ? LDS_G : LDS_B));
    issue(1);
    wait_vm<8>();
    BARRIER_N();
#pragma unroll 1
    for (int j = 0; j < NST - 2; ++j) {
      issue(j + 2);
      wait_vm<8>();
      BARRIER_N();
    }
    wait_vm<0>();
    BARRIER_N();
  } else {
    const int wn = wave;
    {
      const u32x2 g4 = *(const GAS u32x2*)((const char*)g + ((tid * 8 + rot * 128) & 2047));
      const unsigned a = lds0 + LDS_GT + (unsigned)(((tid >> 1) & 1) * 4 * GROW + (tid >> 2) * 4 + (tid & 1) * 2);
      *(LAS u16*)(a) = (u16)g4[0]; *(LAS u16*)(a + GROW) = (u16)(g4[0] >> 16);
      *(LAS u16*)(a + 2 * GROW) = (u16)g4[1]; *(LAS u16*)(a + 3 * GROW) = (u16)(g4[1] >> 16);
    }
    if (PRIO_M) __builtin_amdgcn_s_setprio(PRIO_M);
    const int r = lane & 31, h = lane >> 5, sw = (r >> 1) & 7;
    unsigned offx[4], offw[2];
#pragma unroll
    for (int ks = 0; ks < 4; ++ks) offx[ks] = lds0 + (unsigned)r * 128u + (unsigned)(((2 * ks + h) ^ sw) * 16);
    {
      const int blk = (lane >> 4) & 1, q = (lane & 15) >> 2, p = lane & 3;
#pragma unroll
      for (int tt = 0; tt < 2; ++tt) {
        const int row = 8 * h + 4 * tt + q, ch = 4 * wn + blk + 2 * (p & 1);
        offw[tt] = lds0 + 16384u + (unsigned)(256 * row + 16 * (ch ^ ((q << 2) | ((2 * h + tt) & 3))) + 8 * (p >> 1));
      }
    }
    unsigned offg = lds0 + LDS_GT + (unsigned)((h * 4 + (lane & 3)) * GROW);
    const unsigned li = lane & 3;
    unsigned sel[4] = {li == 0 ? 0x0c0c0100u : (li == 1 ? 0x01000c0cu : 0x0c0c0c0cu), li == 2 ? 0x0c0c0100u : (li == 3 ? 0x01000c0cu : 0x0c0c0c0cu),
                       li == 0 ? 0x0c0c0302u : (li == 1 ? 0x03020c0cu : 0x0c0c0c0cu), li == 2 ? 0x0c0c0302u : (li == 3 ? 0x03020c0cu : 0x0c0c0c0cu)};
    f32x16 acc[4] = {};
    bf16x8 xf[4];
    s16x4 wra[2], wrb[2];
    u32x2 ga, gb;
    u32x4 dg;
    u32x4 wf, wfn;
    f32x4 d0, d1;
    const f32x4 z4 = {0.f, 0.f, 0.f, 0.f};
#define RX(mt, ks) xf[mt] = *(const LAS bf16x8*)(offx[ks] + (mt) * 4096)
#define RW(R, tt, ks) R[tt] = __builtin_amdgcn_ds_read_tr16_b64_v4i16((LAS s16x4*)(offw[tt] + (ks) * 4096))
#define RG(G, o) G = *(const LAS u32x2*)(offg + (o))
#define DG0(G) do { dg[0] = __builtin_amdgcn_perm(G, G, sel[0]); dg[1] = __builtin_amdgcn_perm(G, G, sel[1]); } while (0)
#define DG1(G) do { dg[2] = __builtin_amdgcn_perm(G, G, sel[2]); dg[3] = __builtin_amdgcn_perm(G, G, sel[3]); } while (0)
#define SM0(R) d0 = __builtin_amdgcn_mfma_f32_4x4x4bf16_1k(__builtin_bit_cast(s16x4, (u32x2){dg[0], dg[1]}), R[0], z4, 0, 0, 0)
#define SM1(R) d1 = __builtin_amdgcn_mfma_f32_4x4x4bf16_1k(__builtin_bit_cast(s16x4, (u32x2){dg[2], dg[3]}), R[1], z4, 0, 0, 0)
#define CV0(W) do { W[0] = cvt_pk(d0[0], d0[1]); W[1] = cvt_pk(d0[2], d0[3]); } while (0)
#define CV1(W) do { W[2] = cvt_pk(d1[0], d1[1]); W[3] = cvt_pk(d1[2], d1[3]); } while (0)
#define MM(mt, W) acc[mt] = __builtin_amdgcn_mfma_f32_32x32x16_bf16(__builtin_bit_cast(bf16x8, W), xf[mt], acc[mt], 0, 0, 0)
#define STEP(WC, WN, RA, RB, kx, kw, G, EXTRA2, EXTRA3) do { \
      SM0(RA); SM1(RA); SCHED(); \
      MM(0, WC); SCHED(); RX(0, kx); RW(RB, 0, kw); DG0(G); SCHED(); \
      MM(1, WC); SCHED(); RX(1, kx); RW(RB, 1, kw); DG1(G); SCHED(); \
      MM(2, WC); SCHED(); RX(2, kx); CV0(WN); EXTRA2; SCHED(); \
      MM(3, WC); SCHED(); RX(3, kx); CV1(WN); EXTRA3; SCHED(); } while (0)
    asm volatile("" : "+v"(acc[0]), "+v"(acc[1]), "+v"(acc[2]), "+v"(acc[3]));
    asm volatile("" : "+v"(offx[0]), "+v"(offx[1]), "+v"(offx[2]), "+v"(offx[3]), "+v"(offw[0]), "+v"(offw[1]), "+v"(offg),
                      "+v"(sel[0]), "+v"(sel[1]), "+v"(sel[2]), "+v"(sel[3]));
    __builtin_amdgcn_s_waitcnt(0xC07F);
    BARRIER_N();
    RG(gb, 0); RW(wrb, 0, 0); RW(wrb, 1, 0); RX(0, 0); RW(wra, 0, 1); RW(wra, 1, 1); RX(1, 0); RX(2, 0); RX(3, 0); RG(ga, 8);
    DG0(gb[0]); DG1(gb[0]); SCHED(); SM0(wrb); SM1(wrb); SCHED();
    DG0(gb[1]); DG1(gb[1]); CV0(wf); CV1(wf); SCHED();
#pragma unroll 1
    for (int s = 0; s < NST - 1; ++s) {
      const int dlt = ((s & (RING - 1)) == RING - 1) ? -(RING - 1) * SLOT : SLOT;
      STEP(wf, wfn, wra, wrb, 1, 2, ga[0], (RG(gb, 16)), (void)0);
      STEP(wfn, wf, wrb, wra, 2, 3, ga[1], (offx[0] += dlt, offw[0] += dlt), (offw[1] += dlt, offx[1] += dlt));
      BARRIER_N();
      STEP(wf, wfn, wra, wrb, 3, 0, gb[0], (RG(ga, 24), offx[2] += dlt), (void)0);
      STEP(wfn, wf, wrb, wra, 0, 1, gb[1], (offx[3] += dlt), (offg += 16u));
    }
    f32x4 c1v[4], c2v[4];
    f32x2 rs[4];
    const unsigned tc = lds0 + LDS_TAB + 1024 + (unsigned)(wn * 32 + h * 16) * 4u, tr = lds0 + LDS_TAB + (unsigned)r * 8u;
#define RC(q) do { c1v[q] = *(const LAS f32x4*)(tc + (q) * 16); c2v[q] = *(const LAS f32x4*)(tc + 512 + (q) * 16); rs[q] = *(const LAS f32x2*)(tr + (q) * 256); } while (0)
    STEP(wf, wfn, wra, wrb, 1, 2, ga[0], (void)0, (void)0);
    STEP(wfn, wf, wrb, wra, 2, 3, ga[1], (void)0, (void)0);
    SM0(wra); SM1(wra); SCHED();
    MM(0, wf); SCHED(); RX(0, 3); SCHED();
    MM(1, wf); SCHED(); RX(1, 3); SCHED();
    MM(2, wf); SCHED(); RX(2, 3); CV0(wfn); SCHED();
    MM(3, wf); SCHED(); RX(3, 3); CV1(wfn); SCHED();
    MM(0, wfn); SCHED(); RC(0); SCHED(); MM(1, wfn); SCHED(); RC(1); SCHED(); MM(2, wfn); SCHED(); RC(2); SCHED(); MM(3, wfn); SCHED(); RC(3); SCHED();
    const unsigned so = lds0 + (unsigned)r * 256u, sc0 = (unsigned)(((wn * 4 + h * 2) ^ (r & 7)) * 16), sc1 = sc0 ^ 16u;
#pragma unroll
    for (int mt = 0; mt < 4; ++mt) {
      const float nm = -rs[mt][0], rstd = rs[mt][1];
      f32x4 tq[4];
#pragma unroll
      for (int q = 0; q < 4; ++q) {
        const f32x4 av = {acc[mt][4 * q], acc[mt][4 * q + 1], acc[mt][4 * q + 2], acc[mt][4 * q + 3]};
        tq[q] = av + c1v[q] * nm;
      }
#pragma unroll
      for (int q = 0; q < 4; ++q) tq[q] = tq[q] * rstd + c2v[q];
      const u32x4 o0 = {cvt_pk(tq[0][0], tq[0][1]), cvt_pk(tq[0][2], tq[0][3]), cvt_pk(tq[1][0], tq[1][1]), cvt_pk(tq[1][2], tq[1][3])};
      const u32x4 o1 = {cvt_pk(tq[2][0], tq[2][1]), cvt_pk(tq[2][2], tq[2][3]), cvt_pk(tq[3][0], tq[3][1]), cvt_pk(tq[3][2], tq[3][3])};
      *(LAS u32x4*)(so + mt * 8192 + sc0) = o0;
      *(LAS u32x4*)(so + mt * 8192 + sc1) = o1;
      SCHED();
      if (mt == 1) BARRIER_L();
    }
  }
  if (wave >= 4) {
    BARRIER_N();
    store_rows<2>(lds0, (wave - 4) * 8, lane, (char*)out + (size_t)mi * 262144 + ni * 256);
  }
  BARRIER_L();
  if (wave < 8) store_rows<2>(lds0, 64 + wave * 8, lane, (char*)out + (size_t)mi * 262144 + ni * 256);
}

extern "C" void kernel_launch(void* const* d_in, const int* in_sizes, int n_in, void* d_out, int out_size, void* d_ws, size_t ws_size,
                              hipStream_t stream) {
  const u16* x = (const u16*)d_in[0]; const u16* g = (const u16*)d_in[1]; const u16* b = (const u16*)d_in[2]; const u16* w = (const u16*)d_in[3];
  (void)hipFuncSetAttribute((const void*)lnlin_kernel, hipFuncAttributeMaxDynamicSharedMemorySize, LDS_TOTAL);
  lnlin_kernel<<<256, 768, LDS_TOTAL, stream>>>(x, g, b, w, (u16*)d_out, (char*)d_ws);
}
```

```cpp
#include <hip/hip_runtime.h>
#include <hip/hip_bf16.h>
#include <utility>
#pragma clang diagnostic ignored "-Wint-to-pointer-cast"

typedef short bf16x8 __attribute__((ext_vector_type(8)));
typedef short s16x4 __attribute__((ext_vector_type(4)));
typedef float f32x16 __attribute__((ext_vector_type(16)));
typedef float f32x4 __attribute__((ext_vector_type(4)));
typedef float f32x2 __attribute__((ext_vector_type(2)));
typedef unsigned u32x4 __attribute__((ext_vector_type(4)));
typedef unsigned u32x2 __attribute__((ext_vector_type(2)));
typedef __bf16 bf16x2_t __attribute__((ext_vector_type(2)));
typedef unsigned short u16;
typedef unsigned long long u64;
#define GAS __attribute__((address_space(1)))
#define LAS __attribute__((address_space(3)))

#define ST_FLAVOR "nt"
#define SIDE_START 2
#define PRIO_M 2
#define PRIO_L 0

constexpr int KD = 1024, ND = 1024;
constexpr int NST = 16;
constexpr int RING = 4;
constexpr int SLOT = 32768;
constexpr int LDS_G = RING * SLOT;
constexpr int LDS_B = LDS_G + 2048 + 64;
constexpr int LDS_TAB = LDS_B + 256;
constexpr int LDS_TOTAL = LDS_TAB + 2048;

constexpr size_t WS_EPR = 0;
constexpr size_t WS_EPC = 32 * 128;
constexpr size_t WS_C = 8192;
constexpr size_t WS_RS = WS_C + 8 * 16384;

#define STAMP_DECL
#define STAMP_RT(i)
#define STAMP(i)
#define STAMP_DYN(i)
#define STAMP_FLUSH()

template <int B, int E, class F> __device__ __forceinline__ void static_for(F&& f) {
  if constexpr (B < E) { f(std::integral_constant<int, B>{}); static_for<B + 1, E>(f); }
}
__device__ __forceinline__ float bf_lo(unsigned u) { return __uint_as_float(u << 16); }
__device__ __forceinline__ float bf_hi(unsigned u) { return __uint_as_float(u & 0xffff0000u); }
__device__ __forceinline__ unsigned cvt_pk(float lo, float hi) {
  f32x2 v = {lo, hi}; return __builtin_bit_cast(unsigned, __builtin_convertvector(v, bf16x2_t));
}
template <int CTRL> __device__ __forceinline__ float dpp_add(float x) {
  return x + __builtin_bit_cast(float, __builtin_amdgcn_update_dpp(0, __builtin_bit_cast(int, x), CTRL, 0xf, 0xf, true));
}
__device__ __forceinline__ float sum16(float x) {
  x = dpp_add<0x128>(x); x = dpp_add<0x124>(x); x = dpp_add<0x122>(x); x = dpp_add<0x121>(x);
  return x;
}
__device__ __forceinline__ float add1(float a, float b) { float r; asm("v_add_f32 %0, %1, %2" : "=v"(r) : "v"(a), "v"(b)); return r; }
__device__ __forceinline__ float mul1(float a, float b) { float r; asm("v_mul_f32 %0, %1, %2" : "=v"(r) : "v"(a), "v"(b)); return r; }
__device__ __forceinline__ float fma1(float a, float b, float c) { float r; asm("v_fma_f32 %0, %1, %2, %3" : "=v"(r) : "v"(a), "v"(b), "v"(c)); return r; }
#define PIN8(a) asm volatile("" : "+v"(a[0]), "+v"(a[1]), "+v"(a[2]), "+v"(a[3]), "+v"(a[4]), "+v"(a[5]), "+v"(a[6]), "+v"(a[7]))
#define SCHED() __builtin_amdgcn_sched_barrier(0)
#define BARRIER_L() do { SCHED(); asm volatile("s_waitcnt lgkmcnt(0)\n\ts_barrier" ::: "memory"); SCHED(); } while (0)
#define BARRIER_N() do { SCHED(); asm volatile("s_barrier" ::: "memory"); SCHED(); } while (0)
template <int N> __device__ __forceinline__ void wait_vm() { asm volatile("s_waitcnt vmcnt(%0)" ::"n"(N) : "memory"); }

__device__ __forceinline__ void glds16x4(unsigned v0, unsigned v1, unsigned v2, unsigned v3, const void* sbase, unsigned lds_dst) {
  unsigned keep;
  asm volatile("s_mov_b32 %0, m0\n\ts_mov_b32 m0, %6\n\ts_nop 0\n\t"
               "global_load_lds_dwordx4 %1, %5\n\tglobal_load_lds_dwordx4 %2, %5 offset:1024\n\t"
               "global_load_lds_dwordx4 %3, %5 offset:2048\n\tglobal_load_lds_dwordx4 %4, %5 offset:3072\n\ts_mov_b32 m0, %0"
               : "=&s"(keep) : "v"(v0), "v"(v1), "v"(v2), "v"(v3), "s"(sbase), "s"(lds_dst) : "memory");
}
__device__ __forceinline__ void glds4(unsigned v0, const void* sbase, unsigned lds_dst) {
  unsigned keep;
  asm volatile("s_mov_b32 %0, m0\n\ts_mov_b32 m0, %3\n\ts_nop 0\n\tglobal_load_lds_dword %1, %2\n\ts_mov_b32 m0, %0"
               : "=&s"(keep) : "v"(v0), "s"(sbase), "s"(lds_dst) : "memory");
}
template <int OFF> __device__ __forceinline__ u32x4 gload16_asm(unsigned voff, const void* sbase) {
  u32x4 r; asm volatile("global_load_dwordx4 %0, %1, %2 offset:%3" : "=v"(r) : "v"(voff), "s"(sbase), "n"(OFF) : "memory"); return r;
}
__device__ __forceinline__ unsigned gload_u32_sc1_asm(unsigned voff, const void* sbase) {
  unsigned r; asm volatile("global_load_dword %0, %1, %2 sc1" : "=v"(r) : "v"(voff), "s"(sbase) : "memory"); return r;
}
template <int OFF> __device__ __forceinline__ u32x2 gload_u64_sc1_asm(unsigned voff, const void* sbase) {
  u32x2 r; asm volatile("global_load_dwordx2 %0, %1, %2 offset:%3 sc1" : "=v"(r) : "v"(voff), "s"(sbase), "n"(OFF) : "memory"); return r;
}
__device__ __forceinline__ void store_granule(char* ws, size_t off, unsigned tag, float v) {
  __hip_atomic_store((GAS u64*)(ws + off), ((u64)tag << 32) | __float_as_uint(v), __ATOMIC_RELAXED, __HIP_MEMORY_SCOPE_AGENT);
}
__device__ __forceinline__ u64 load_granule(const char* ws, size_t off) {
  return __hip_atomic_load((GAS u64*)(ws + off), __ATOMIC_RELAXED, __HIP_MEMORY_SCOPE_AGENT);
}
__device__ __forceinline__ unsigned load_word(const char* ws, size_t off) {
  return __hip_atomic_load((GAS unsigned*)(ws + off), __ATOMIC_RELAXED, __HIP_MEMORY_SCOPE_AGENT);
}

template <int N> __device__ __forceinline__ void store_rows(unsigned lds0, int rowb, int lane, char* ob) {
  const int row0 = rowb + (lane >> 4), ch = lane & 15;
  u32x4 v[N];
#pragma unroll
  for (int i = 0; i < N; ++i) { const int row = row0 + i * 4; v[i] = *(const LAS u32x4*)(lds0 + row * 256 + ((ch ^ (row & 7)) * 16)); }
  const unsigned oo = (unsigned)row0 * 2048u + (unsigned)ch * 16u;
#pragma unroll
  for (int i = 0; i < N; ++i)
    asm volatile("global_store_dwordx4 %0, %1, %2 " ST_FLAVOR "\n\ts_nop 1" :: "v"(oo + i * 8192u), "v"(v[i]), "s"(ob) : "memory");
}

__global__ __launch_bounds__(768, 3) void lnlin_kernel(const u16* __restrict__ x, const u16* __restrict__ g, const u16* __restrict__ b,
                                                       const u16* __restrict__ w, u16* __restrict__ out, char* ws) {
  u64 code_pc; unsigned code_len;
  asm volatile("s_getpc_b64 %0\nlnlin_code_begin:\n\ts_mov_b32 %1, lnlin_code_end-lnlin_code_begin" : "=s"(code_pc), "=s"(code_len));
  extern __shared__ __attribute__((aligned(16))) char smem_g[];
  const int bid = blockIdx.x, xcd = bid & 7, idx = bid >> 3;
  const int mi = xcd * 4 + (idx >> 3), ni = idx & 7;
  const int slab = (ni + (mi >> 2)) & 7, rot = slab * 2;
  const int tid = threadIdx.x, lane = tid & 63;
  const int wave = __builtin_amdgcn_readfirstlane(tid >> 6);
  const unsigned lds0 = (unsigned)(size_t)(LAS char*)smem_g;
  STAMP_DECL STAMP_RT(0); STAMP(1);
  const u64 tstart = __builtin_amdgcn_s_memrealtime();

  if (wave >= 8) {
    const int d = wave - 8, t = tid - 512;
    const int scol = (mi & 3) * 32 + d * 8;
    const unsigned cw = lds0 + 16384u + (unsigned)(256 * lane + 16 * ((scol >> 3) ^ (((lane & 3) << 2) | ((lane >> 2) & 3))));
    const int srow = t >> 1, l4 = lane & 15;
    const unsigned sx = lds0 + (unsigned)srow * 128u, ssw = (unsigned)(((t & 1) * 4) ^ ((srow >> 1) & 7));
    u32x4 xr[8];
    f32x4 f[16];
    unsigned ep;
    float p1[8], p2[8], part = 0.f, mean = 0.f, pub = 0.f, v0 = 0.f, v1 = 0.f;
    unsigned bad = 0u;
    u32x4 q[8];
    const unsigned gsrc = d < 2 ? (unsigned)WS_C + (unsigned)ni * 16384u + (unsigned)t * 16u : (unsigned)WS_RS + (unsigned)mi * 16384u + (unsigned)(t - 128) * 16u;
    auto gather = [&](int j0, int j1) __attribute__((always_inline)) {
#pragma unroll
      for (int j = j0; j < j1; ++j)
        q[j] = __builtin_amdgcn_raw_buffer_load_b128(__builtin_amdgcn_make_buffer_rsrc((void*)ws, 0, 0x100000, 0x00020000), gsrc + j * 2048u, 0, 16  );
    };
    auto combine = [&](unsigned tag) __attribute__((always_inline)) {
      bad = 0u; v0 = 0.f; v1 = 0.f;
#pragma unroll
      for (int j = 0; j < 8; ++j) { bad |= (q[j][1] ^ tag) | (q[j][3] ^ tag); v0 = add1(v0, __uint_as_float(q[j][0])); v1 = add1(v1, __uint_as_float(q[j][2])); }
      if (d >= 2) {
        v0 *= 0.125f;
        float dv = 0.f;
#pragma unroll
        for (int j = 0; j < 8; ++j) { const float e = __uint_as_float(q[j][0]) - v0; dv = fmaf(e, e, dv); }
        v1 = __builtin_amdgcn_rsqf(fmaf(dv, 128.f, v1) * (1.f / 1024.f) + 1e-5f);
      }
    };
    ep = load_word(ws, (lane & 1) ? WS_EPC + ni * 128 : WS_EPR + mi * 128);
    STAMP(2);
    BARRIER_N();
    unsigned epr = 0, epc = 0;
    static_for<0, NST - 1>([&](auto S) {
      constexpr int st = S;
      if constexpr (st <= 1) {
        const u32x4 wv = *(const LAS u32x4*)(cw + st * SLOT);
        const float ga = bf_lo(*(const LAS u16*)(lds0 + LDS_G + st * 128 + lane * 2)), ba = bf_lo(*(const LAS u16*)(lds0 + LDS_B + st * 128 + lane * 2));
#pragma unroll
        for (int i = 0; i < 4; ++i) xr[st * 4 + i] = *(const LAS u32x4*)(sx + st * SLOT + ((ssw ^ i) * 16u));
#pragma unroll
        for (int i = 0; i < 4; ++i) {
          const float l0 = bf_lo(wv[i]), h0 = bf_hi(wv[i]);
          const unsigned pa = cvt_pk(mul1(ga, l0), mul1(ga, h0));
          if constexpr (st == 0) { p1[2 * i] = bf_lo(pa); p1[2 * i + 1] = bf_hi(pa); p2[2 * i] = mul1(ba, l0); p2[2 * i + 1] = mul1(ba, h0); }
          else { p1[2 * i] = add1(p1[2 * i], bf_lo(pa)); p1[2 * i + 1] = add1(p1[2 * i + 1], bf_hi(pa)); p2[2 * i] = fma1(ba, l0, p2[2 * i]); p2[2 * i + 1] = fma1(ba, h0, p2[2 * i + 1]); }
        }
      }
      if constexpr (st == 2) {
        float a[8];
#pragma unroll
        for (int i = 0; i < 8; ++i) {
          auto rr = __builtin_amdgcn_permlane32_swap(__float_as_uint(p1[i]), __float_as_uint(p2[i]), false, false);
          a[i] = __uint_as_float(rr[0]) + __uint_as_float(rr[1]);
        }
        float cs[4];
#pragma unroll
        for (int i = 0; i < 4; ++i) {
          auto rr = __builtin_amdgcn_permlane16_swap(__float_as_uint(a[i]), __float_as_uint(a[4 + i]), false, false);
          cs[i] = sum16(__uint_as_float(rr[0]) + __uint_as_float(rr[1]));
        }
        pub = l4 == 0 ? cs[0] : (l4 == 1 ? cs[1] : (l4 == 2 ? cs[2] : cs[3]));
        epr = (unsigned)__builtin_amdgcn_readlane((int)ep, 0); epc = (unsigned)__builtin_amdgcn_readlane((int)ep, 1);
      }
      if constexpr (st <= 1) { PIN8(p1); PIN8(p2); }
      if constexpr (st == 2) asm volatile("" : "+v"(pub));
      if constexpr (st == 3 || st == 4) {
        constexpr int j0 = (st - 3) * 4;
        f32x4 s4 = {part, 0.f, 0.f, 0.f};
#pragma unroll
        for (int j = j0; j < j0 + 4; ++j) {
          f[2 * j] = (f32x4){bf_lo(xr[j][0]), bf_hi(xr[j][0]), bf_lo(xr[j][1]), bf_hi(xr[j][1])};
          f[2 * j + 1] = (f32x4){bf_lo(xr[j][2]), bf_hi(xr[j][2]), bf_lo(xr[j][3]), bf_hi(xr[j][3])};
#pragma unroll
          for (int e = 0; e < 4; ++e) { s4[e] = add1(s4[e], f[2 * j][e]); s4[e] = add1(s4[e], f[2 * j + 1][e]); }
        }
        part = add1(add1(s4[0], s4[1]), add1(s4[2], s4[3]));
        if constexpr (st == 4) { mean = dpp_add<0xB1>(part) * (1.f / 128.f); part = 0.f; }
        asm volatile("" : "+v"(part), "+v"(mean), "+v"(f[2 * j0]), "+v"(f[2 * j0 + 1]), "+v"(f[2 * j0 + 2]), "+v"(f[2 * j0 + 3]),
                          "+v"(f[2 * j0 + 4]), "+v"(f[2 * j0 + 5]), "+v"(f[2 * j0 + 6]), "+v"(f[2 * j0 + 7]));
      }
      if constexpr (st == 5 || st == 6) {
        constexpr int j0 = (st - 5) * 8;
        f32x4 s4 = {part, 0.f, 0.f, 0.f};
#pragma unroll
        for (int j = j0; j < j0 + 8; ++j)
#pragma unroll
          for (int e = 0; e < 4; ++e) { const float dd = add1(f[j][e], -mean); s4[e] = fma1(dd, dd, s4[e]); }
        part = add1(add1(s4[0], s4[1]), add1(s4[2], s4[3]));
        asm volatile("" : "+v"(part));
      }
      SCHED();
      if constexpr (st == 3) {
        if (l4 < 4) store_granule(ws, WS_C + ((size_t)((ni * 8 + slab) * 128 + scol + ((lane >> 4) & 1) * 4 + l4) * 2 + (lane >> 5)) * 8, epc + 1u, pub);
      }
      if constexpr (st == 7) {
        const float m2 = dpp_add<0xB1>(part);
        store_granule(ws, WS_RS + ((size_t)(mi * 8 + slab) * 256 + t) * 8, epr + 1u, (t & 1) ? m2 : mean);
      }
      if constexpr (st == 8) { if (d < 2) gather(0, 4); }
      if constexpr (st == 9) { if (d < 2) gather(4, 8); }
      if constexpr (st == 11) { if (d >= 2) gather(0, 4); }
      if constexpr (st == 12) { if (d >= 2) gather(4, 8); }
      if constexpr (st == 11) { if (d < 2) { combine(epc + 1u); asm volatile("" : "+v"(v0), "+v"(v1), "+v"(bad)); } }
      if constexpr (st == 14) {
        const unsigned tag = (d < 2 ? epc : epr) + 1u;
        if (d >= 2) combine(tag);
        bool ok = bad == 0u;
        if (__builtin_expect(!__all(ok), 0)) {
#pragma unroll 1
          for (int it = 0; it < 20000; ++it) {
            asm volatile("" ::: "memory");
            gather(0, 8);
            combine(tag);
            ok = bad == 0u;
            if (__all(ok)) break;
            __builtin_amdgcn_s_sleep(2);
          }
        }
        if (__builtin_expect(!__all(ok), 0)) {
          v0 = 0.f; v1 = 0.f;
          if (d < 2) {
            const u16* wc = w + ni * 128 + t;
#pragma unroll 1
            for (int k = 0; k < KD; ++k) {
              const float wv = bf_lo(wc[(size_t)k * ND]);
              v0 += bf_lo(cvt_pk(bf_lo(g[k]) * wv, 0.f)); v1 = fmaf(bf_lo(b[k]), wv, v1);
            }
          } else {
            const u16* xrow = x + (size_t)(mi * 128 + (t - 128)) * KD;
#pragma unroll 1
            for (int k = 0; k < KD; ++k) v0 += bf_lo(xrow[k]);
            v0 *= (1.f / 1024.f);
#pragma unroll 1
            for (int k = 0; k < KD; ++k) { const float dd = bf_lo(xrow[k]) - v0; v1 = fmaf(dd, dd, v1); }
            v1 = __builtin_amdgcn_rsqf(v1 * (1.f / 1024.f) + 1e-5f);
          }
        }
        if (d < 2) { *(LAS float*)(lds0 + LDS_TAB + 1024 + t * 4) = v0; *(LAS float*)(lds0 + LDS_TAB + 1536 + t * 4) = v1; }
        else *(LAS f32x2*)(lds0 + LDS_TAB + (t - 128) * 8) = (f32x2){v0, v1};
      }
      if constexpr (st == 14) { STAMP(23); BARRIER_L(); } else BARRIER_N();
      STAMP(3 + st);
    });
    if (t == 0) {
      __hip_atomic_fetch_add((GAS unsigned*)(ws + WS_EPR + mi * 128), 1u, __ATOMIC_RELAXED, __HIP_MEMORY_SCOPE_AGENT);
      __hip_atomic_fetch_add((GAS unsigned*)(ws + WS_EPC + ni * 128), 1u, __ATOMIC_RELAXED, __HIP_MEMORY_SCOPE_AGENT);
    }
  } else if (wave >= 4) {
    const int d = wave - 4;
    if (PRIO_L) __builtin_amdgcn_s_setprio(PRIO_L);
    unsigned xo[4], wo[4];
#pragma unroll
    for (int i = 0; i < 4; ++i) {
      const int row = i * 8 + (lane >> 3);
      xo[i] = (unsigned)row * 2048u + (unsigned)(((lane & 7) ^ ((row >> 1) & 7)) * 16) - (unsigned)i * 1024u;
      const int kr = i * 4 + (lane >> 4);
      wo[i] = (unsigned)kr * 2048u + (unsigned)(((lane & 15) ^ (((lane >> 4) << 2) | i)) * 16) - (unsigned)i * 1024u;
    }
    const unsigned dst = lds0 + (unsigned)d * 4096u;
    const char* xb = (const char*)x + (size_t)mi * 262144 + d * 65536;
    const char* wb = (const char*)w + d * 32768 + ni * 256;
    auto issue = [&](int st) {
      const unsigned ds_ = dst + (unsigned)(st & (RING - 1)) * SLOT;
      const int ks_ = (st + rot) & (NST - 1);
      glds16x4(wo[0], wo[1], wo[2], wo[3], wb + (size_t)ks_ * 131072, ds_ + 16384u);
      glds16x4(xo[0], xo[1], xo[2], xo[3], xb + ks_ * 128, ds_);
    };
    glds4((unsigned)lane * 4u, (const char*)g + ((d * 512 + rot * 128) & 2047), lds0 + LDS_G + (unsigned)d * 512u);
    glds4((unsigned)lane * 4u, (const char*)g + ((d * 512 + 256 + rot * 128) & 2047), lds0 + LDS_G + (unsigned)d * 512u + 256u);
    if (d == 0) glds4((unsigned)lane * 4u, (const char*)b + rot * 128, lds0 + LDS_B);
    issue(0); issue(1);
    STAMP(2);
    wait_vm<8>();
    STAMP(3);
    BARRIER_N();
#pragma unroll 1
    for (int j = 0; j < NST - 2; ++j) {
      issue(j + 2);
      wait_vm<8>();
      BARRIER_N();
      STAMP_DYN(j + 4);
    }
    wait_vm<0>();
    BARRIER_N();
    STAMP(18);
  } else {
    const int wn = wave;
    {
      const unsigned co = (unsigned)wn * 3072u + (unsigned)lane * 16u, lim = (code_len & ~15u) - 16u;
      const u32x4 t0 = *(const GAS u32x4*)(code_pc + min(co, lim)) ^ *(const GAS u32x4*)(code_pc + min(co + 1024u, lim)) ^ *(const GAS u32x4*)(code_pc + min(co + 2048u, lim));
      asm volatile("" :: "v"(t0));
    }
    STAMP(3);
    if (PRIO_M) __builtin_amdgcn_s_setprio(PRIO_M);
    const int r = lane & 31, h = lane >> 5, sw = (r >> 1) & 7;
    unsigned offx[4], offw[2];
#pragma unroll
    for (int ks = 0; ks < 4; ++ks) offx[ks] = lds0 + (unsigned)r * 128u + (unsigned)(((2 * ks + h) ^ sw) * 16);
    {
      const int blk = (lane >> 4) & 1, q = (lane & 15) >> 2, p = lane & 3;
#pragma unroll
      for (int tt = 0; tt < 2; ++tt) {
        const int row = 8 * h + 4 * tt + q, ch = 4 * wn + blk + 2 * (p & 1);
        offw[tt] = lds0 + 16384u + (unsigned)(256 * row + 16 * (ch ^ ((q << 2) | ((2 * h + tt) & 3))) + 8 * (p >> 1));
      }
    }
    unsigned offg = lds0 + LDS_G + (unsigned)h * 16u;
    const unsigned li = lane & 3;
    const unsigned mk0 = li == 0 ? 0xffffu : (li == 1 ? 0xffff0000u : 0u), mk1 = li == 2 ? 0xffffu : (li == 3 ? 0xffff0000u : 0u);
    f32x16 acc[4] = {};
    bf16x8 xf[4];
    s16x4 wra[2], wrb[2];
    u32x4 gq, dg;
    u32x4 wf, wfn;
    f32x4 d0, d1;
    const f32x4 z4 = {0.f, 0.f, 0.f, 0.f};
#define RX(mt, ks) xf[mt] = *(const LAS bf16x8*)(offx[ks] + (mt) * 4096)
#define RW(R, tt, ks) R[tt] = __builtin_amdgcn_ds_read_tr16_b64_v4i16((LAS s16x4*)(offw[tt] + (ks) * 4096))
#define RG(o) gq = *(const LAS u32x4*)(offg + (o) * 32)
#define DG0() do { dg[0] = gq[0] & mk[0]; dg[1] = gq[1] & mk[1]; } while (0)
#define DG1() do { dg[2] = gq[2] & mk[0]; dg[3] = gq[3] & mk[1]; } while (0)
#define SM0(R) d0 = __builtin_amdgcn_mfma_f32_4x4x4bf16_1k(__builtin_bit_cast(s16x4, (u32x2){dg[0], dg[1]}), R[0], z4, 0, 0, 0)
#define SM1(R) d1 = __builtin_amdgcn_mfma_f32_4x4x4bf16_1k(__builtin_bit_cast(s16x4, (u32x2){dg[2], dg[3]}), R[1], z4, 0, 0, 0)
#define CV0(W) do { W[0] = cvt_pk(d0[0], d0[1]); W[1] = cvt_pk(d0[2], d0[3]); } while (0)
#define CV1(W) do { W[2] = cvt_pk(d1[0], d1[1]); W[3] = cvt_pk(d1[2], d1[3]); } while (0)
#define MM(mt, W) acc[mt] = __builtin_amdgcn_mfma_f32_32x32x16_bf16(__builtin_bit_cast(bf16x8, W), xf[mt], acc[mt], 0, 0, 0)
#define STEP(WC, WN, RA, RB, kx, kw, kg, EXTRA2, EXTRA3) do { \
      SM0(RA); SM1(RA); SCHED(); \
      MM(0, WC); SCHED(); RX(0, kx); RW(RB, 0, kw); DG0(); SCHED(); \
      MM(1, WC); SCHED(); RX(1, kx); RW(RB, 1, kw); DG1(); SCHED(); \
      MM(2, WC); SCHED(); RX(2, kx); RG(kg); CV0(WN); EXTRA2; SCHED(); \
      MM(3, WC); SCHED(); RX(3, kx); CV1(WN); EXTRA3; SCHED(); } while (0)
    unsigned mk[2] = {mk0, mk1};
    asm volatile("" : "+v"(acc[0]), "+v"(acc[1]), "+v"(acc[2]), "+v"(acc[3]));
    asm volatile("" : "+v"(offx[0]), "+v"(offx[1]), "+v"(offx[2]), "+v"(offx[3]), "+v"(offw[0]), "+v"(offw[1]), "+v"(offg), "+v"(mk[0]), "+v"(mk[1]));
    __builtin_amdgcn_s_waitcnt(0xC07F);
    STAMP(4);
    BARRIER_N();
    STAMP(5);
    RG(0); RW(wrb, 0, 0); RW(wrb, 1, 0); RX(0, 0); RW(wra, 0, 1); RW(wra, 1, 1); RX(1, 0); RX(2, 0); RX(3, 0);
    DG0(); DG1(); SCHED(); RG(1); SM0(wrb); SM1(wrb); SCHED();
    CV0(wf); CV1(wf); DG0(); DG1(); SCHED(); RG(2); SCHED();
#pragma unroll 1
    for (int s = 0; s < NST - 1; ++s) {
      const int dlt = ((s & (RING - 1)) == RING - 1) ? -(RING - 1) * SLOT : SLOT;
      STEP(wf, wfn, wra, wrb, 1, 2, 3, (void)0, (void)0);
      STEP(wfn, wf, wrb, wra, 2, 3, 4, (offx[0] += dlt, offw[0] += dlt), (offw[1] += dlt, offx[1] += dlt));
      BARRIER_N();
      STEP(wf, wfn, wra, wrb, 3, 0, 5, (offx[2] += dlt), (void)0);
      STEP(wfn, wf, wrb, wra, 0, 1, 6, (offx[3] += dlt), (offg += 128u));
    }
    f32x4 c1v[4], c2v[4];
    f32x2 rs[4];
    const unsigned tc = lds0 + LDS_TAB + 1024 + (unsigned)(wn * 32 + h * 16) * 4u, tr = lds0 + LDS_TAB + (unsigned)r * 8u;
#define RC(q) do { c1v[q] = *(const LAS f32x4*)(tc + (q) * 16); c2v[q] = *(const LAS f32x4*)(tc + 512 + (q) * 16); rs[q] = *(const LAS f32x2*)(tr + (q) * 256); } while (0)
    STEP(wf, wfn, wra, wrb, 1, 2, 3, (void)0, (void)0);
    STEP(wfn, wf, wrb, wra, 2, 3, 4, (void)0, (void)0);
    SM0(wra); SM1(wra); SCHED();
    MM(0, wf); SCHED(); RX(0, 3); SCHED();
    MM(1, wf); SCHED(); RX(1, 3); SCHED();
    MM(2, wf); SCHED(); RX(2, 3); CV0(wfn); SCHED();
    MM(3, wf); SCHED(); RX(3, 3); CV1(wfn); SCHED();
    MM(0, wfn); SCHED(); RC(0); SCHED(); MM(1, wfn); SCHED(); RC(1); SCHED(); MM(2, wfn); SCHED(); RC(2); SCHED(); MM(3, wfn); SCHED(); RC(3); SCHED();
    STAMP(20);
    const unsigned so = lds0 + (unsigned)r * 256u, sc0 = (unsigned)(((wn * 4 + h * 2) ^ (r & 7)) * 16), sc1 = sc0 ^ 16u;
#pragma unroll
    for (int mt = 0; mt < 4; ++mt) {
      const float nm = -rs[mt][0], rstd = rs[mt][1];
      f32x4 tq[4];
#pragma unroll
      for (int q = 0; q < 4; ++q) {
        const f32x4 av = {acc[mt][4 * q], acc[mt][4 * q + 1], acc[mt][4 * q + 2], acc[mt][4 * q + 3]};
        tq[q] = av + c1v[q] * nm;
      }
#pragma unroll
      for (int q = 0; q < 4; ++q) tq[q] = tq[q] * rstd + c2v[q];
      const u32x4 o0 = {cvt_pk(tq[0][0], tq[0][1]), cvt_pk(tq[0][2], tq[0][3]), cvt_pk(tq[1][0], tq[1][1]), cvt_pk(tq[1][2], tq[1][3])};
      const u32x4 o1 = {cvt_pk(tq[2][0], tq[2][1]), cvt_pk(tq[2][2], tq[2][3]), cvt_pk(tq[3][0], tq[3][1]), cvt_pk(tq[3][2], tq[3][3])};
      *(LAS u32x4*)(so + mt * 8192 + sc0) = o0;
      *(LAS u32x4*)(so + mt * 8192 + sc1) = o1;
      SCHED();
      if (mt == 1) BARRIER_L();
    }
  }
  if (wave >= 4) {
    BARRIER_N();
    store_rows<2>(lds0, (wave - 4) * 8, lane, (char*)out + (size_t)mi * 262144 + ni * 256);
  }
  STAMP(24);
  BARRIER_L();
  STAMP(25);
  if (wave < 8) store_rows<2>(lds0, 64 + wave * 8, lane, (char*)out + (size_t)mi * 262144 + ni * 256);
  asm volatile("s_waitcnt vmcnt(0)" ::: "memory");
  while (__builtin_amdgcn_s_memrealtime() - tstart < 2500) __builtin_amdgcn_s_sleep(1);
  STAMP_FLUSH();
  asm volatile("lnlin_code_end:");
}

extern "C" void kernel_launch(void* const* d_in, const int* in_sizes, int n_in, void* d_out, int out_size, void* d_ws, size_t ws_size,
                              hipStream_t stream) {
  const u16* x = (const u16*)d_in[0]; const u16* g = (const u16*)d_in[1]; const u16* b = (const u16*)d_in[2]; const u16* w = (const u16*)d_in[3];
  (void)hipFuncSetAttribute((const void*)lnlin_kernel, hipFuncAttributeMaxDynamicSharedMemorySize, LDS_TOTAL);
  lnlin_kernel<<<256, 768, LDS_TOTAL, stream>>>(x, g, b, w, (u16*)d_out, (char*)d_ws);
}
```

```cpp
#include <hip/hip_runtime.h>
#include <hip/hip_bf16.h>
#include <utility>
#pragma clang diagnostic ignored "-Wint-to-pointer-cast"

typedef short bf16x8 __attribute__((ext_vector_type(8)));
typedef short s16x4 __attribute__((ext_vector_type(4)));
typedef float f32x16 __attribute__((ext_vector_type(16)));
typedef float f32x4 __attribute__((ext_vector_type(4)));
typedef float f32x2 __attribute__((ext_vector_type(2)));
typedef unsigned u32x4 __attribute__((ext_vector_type(4)));
typedef unsigned u32x2 __attribute__((ext_vector_type(2)));
typedef __bf16 bf16x2_t __attribute__((ext_vector_type(2)));
typedef unsigned short u16;
typedef unsigned long long u64;
#define GAS __attribute__((address_space(1)))
#define LAS __attribute__((address_space(3)))

#define ST_FLAVOR "nt"
#define SIDE_START 2
#define PRIO_M 2
#define PRIO_L 0

constexpr int KD = 1024, ND = 1024;
constexpr int NST = 16;
constexpr int RING = 4;
constexpr int SLOT = 32768;
constexpr int GROW = 136;
constexpr int LDS_GT = RING * SLOT;
constexpr int LDS_G = LDS_GT + 16 * GROW;
constexpr int LDS_B = LDS_G + 256;
constexpr int LDS_TAB = LDS_B + 256;
constexpr int LDS_TOTAL = LDS_TAB + 2048;

constexpr size_t WS_EPR = 0;
constexpr size_t WS_EPC = 32 * 128;
constexpr size_t WS_C = 8192;
constexpr size_t WS_RS = WS_C + 8 * 16384;

#define STAMP_DECL
#define STAMP_RT(i)
#define STAMP(i)
#define STAMP_DYN(i)
#define STAMP_FLUSH()

template <int B, int E, class F> __device__ __forceinline__ void static_for(F&& f) {
  if constexpr (B < E) { f(std::integral_constant<int, B>{}); static_for<B + 1, E>(f); }
}
__device__ __forceinline__ float bf_lo(unsigned u) { return __uint_as_float(u << 16); }
__device__ __forceinline__ float bf_hi(unsigned u) { return __uint_as_float(u & 0xffff0000u); }
__device__ __forceinline__ unsigned cvt_pk(float lo, float hi) {
  f32x2 v = {lo, hi}; return __builtin_bit_cast(unsigned, __builtin_convertvector(v, bf16x2_t));
}
template <int CTRL> __device__ __forceinline__ float dpp_add(float x) {
  return x + __builtin_bit_cast(float, __builtin_amdgcn_update_dpp(0, __builtin_bit_cast(int, x), CTRL, 0xf, 0xf, true));
}
__device__ __forceinline__ float sum16(float x) {
  x = dpp_add<0x128>(x); x = dpp_add<0x124>(x); x = dpp_add<0x122>(x); x = dpp_add<0x121>(x);
  return x;
}
__device__ __forceinline__ float add1(float a, float b) { float r; asm("v_add_f32 %0, %1, %2" : "=v"(r) : "v"(a), "v"(b)); return r; }
__device__ __forceinline__ float mul1(float a, float b) { float r; asm("v_mul_f32 %0, %1, %2" : "=v"(r) : "v"(a), "v"(b)); return r; }
__device__ __forceinline__ float fma1(float a, float b, float c) { float r; asm("v_fma_f32 %0, %1, %2, %3" : "=v"(r) : "v"(a), "v"(b), "v"(c)); return r; }
#define PIN8(a) asm volatile("" : "+v"(a[0]), "+v"(a[1]), "+v"(a[2]), "+v"(a[3]), "+v"(a[4]), "+v"(a[5]), "+v"(a[6]), "+v"(a[7]))
#define SCHED() __builtin_amdgcn_sched_barrier(0)
#define BARRIER_L() do { SCHED(); asm volatile("s_waitcnt lgkmcnt(0)\n\ts_barrier" ::: "memory"); SCHED(); } while (0)
#define BARRIER_N() do { SCHED(); asm volatile("s_barrier" ::: "memory"); SCHED(); } while (0)
template <int N> __device__ __forceinline__ void wait_vm() { asm volatile("s_waitcnt vmcnt(%0)" ::"n"(N) : "memory"); }

__device__ __forceinline__ void glds16x4(unsigned v0, unsigned v1, unsigned v2, unsigned v3, const void* sbase, unsigned lds_dst) {
  unsigned keep;
  asm volatile("s_mov_b32 %0, m0\n\ts_mov_b32 m0, %6\n\ts_nop 0\n\t"
               "global_load_lds_dwordx4 %1, %5\n\tglobal_load_lds_dwordx4 %2, %5 offset:1024\n\t"
               "global_load_lds_dwordx4 %3, %5 offset:2048\n\tglobal_load_lds_dwordx4 %4, %5 offset:3072\n\ts_mov_b32 m0, %0"
               : "=&s"(keep) : "v"(v0), "v"(v1), "v"(v2), "v"(v3), "s"(sbase), "s"(lds_dst) : "memory");
}
__device__ __forceinline__ void glds4(unsigned v0, const void* sbase, unsigned lds_dst) {
  unsigned keep;
  asm volatile("s_mov_b32 %0, m0\n\ts_mov_b32 m0, %3\n\ts_nop 0\n\tglobal_load_lds_dword %1, %2\n\ts_mov_b32 m0, %0"
               : "=&s"(keep) : "v"(v0), "s"(sbase), "s"(lds_dst) : "memory");
}
template <int OFF> __device__ __forceinline__ u32x4 gload16_asm(unsigned voff, const void* sbase) {
  u32x4 r; asm volatile("global_load_dwordx4 %0, %1, %2 offset:%3" : "=v"(r) : "v"(voff), "s"(sbase), "n"(OFF) : "memory"); return r;
}
__device__ __forceinline__ unsigned gload_u32_sc1_asm(unsigned voff, const void* sbase) {
  unsigned r; asm volatile("global_load_dword %0, %1, %2 sc1" : "=v"(r) : "v"(voff), "s"(sbase) : "memory"); return r;
}
template <int OFF> __device__ __forceinline__ u32x2 gload_u64_sc1_asm(unsigned voff, const void* sbase) {
  u32x2 r; asm volatile("global_load_dwordx2 %0, %1, %2 offset:%3 sc1" : "=v"(r) : "v"(voff), "s"(sbase), "n"(OFF) : "memory"); return r;
}
__device__ __forceinline__ void store_granule(char* ws, size_t off, unsigned tag, float v) {
  __hip_atomic_store((GAS u64*)(ws + off), ((u64)tag << 32) | __float_as_uint(v), __ATOMIC_RELAXED, __HIP_MEMORY_SCOPE_AGENT);
}
__device__ __forceinline__ u64 load_granule(const char* ws, size_t off) {
  return __hip_atomic_load((GAS u64*)(ws + off), __ATOMIC_RELAXED, __HIP_MEMORY_SCOPE_AGENT);
}
__device__ __forceinline__ unsigned load_word(const char* ws, size_t off) {
  return __hip_atomic_load((GAS unsigned*)(ws + off), __ATOMIC_RELAXED, __HIP_MEMORY_SCOPE_AGENT);
}

template <int N> __device__ __forceinline__ void store_rows(unsigned lds0, int rowb, int lane, char* ob) {
  const int row0 = rowb + (lane >> 4), ch = lane & 15;
  u32x4 v[N];
#pragma unroll
  for (int i = 0; i < N; ++i) { const int row = row0 + i * 4; v[i] = *(const LAS u32x4*)(lds0 + row * 256 + ((ch ^ (row & 7)) * 16)); }
  const unsigned oo = (unsigned)row0 * 2048u + (unsigned)ch * 16u;
#pragma unroll
  for (int i = 0; i < N; ++i)
    asm volatile("global_store_dwordx4 %0, %1, %2 " ST_FLAVOR "\n\ts_nop 1" :: "v"(oo + i * 8192u), "v"(v[i]), "s"(ob) : "memory");
}

__global__ __launch_bounds__(768, 3) void lnlin_kernel(const u16* __restrict__ x, const u16* __restrict__ g, const u16* __restrict__ b,
                                                       const u16* __restrict__ w, u16* __restrict__ out, char* ws) {
  extern __shared__ __attribute__((aligned(16))) char smem_g[];
  const int bid = blockIdx.x, xcd = bid & 7, idx = bid >> 3;
  const int mi = xcd * 4 + (idx >> 3), ni = idx & 7;
  const int slab = ((mi >> 2) + 2 * (ni & 3)) & 7, rot = slab * 2, sp = ni >> 2, rslab = (slab + sp) & 7;
  const int tid = threadIdx.x, lane = tid & 63;
  const int wave = __builtin_amdgcn_readfirstlane(tid >> 6);
  const unsigned lds0 = (unsigned)(size_t)(LAS char*)smem_g;
  STAMP_DECL STAMP_RT(0); STAMP(1);

  if (wave >= 8) {
    const int d = wave - 8, t = tid - 512;
    const int scol = (mi & 3) * 32 + d * 8;
    const unsigned cw = lds0 + 16384u + (unsigned)(256 * lane + 16 * ((scol >> 3) ^ (((lane & 3) << 2) | ((lane >> 2) & 3))));
    const int srow = t >> 1, l4 = lane & 15;
    const unsigned sx = lds0 + (unsigned)srow * 128u, ssw = (unsigned)(((t & 1) * 4) ^ ((srow >> 1) & 7));
    u32x4 xr[8];
    unsigned ep;
    float p1[8], p2[8], cs[4], part = 0.f, mean = 0.f, pub = 0.f, v0 = 0.f, v1 = 0.f;
    unsigned bad = 0u;
    u32x4 q[8];
    const unsigned gsrc = d < 2 ? (unsigned)WS_C + (unsigned)ni * 16384u + (unsigned)t * 16u : (unsigned)WS_RS + (unsigned)mi * 16384u + (unsigned)(t - 128) * 16u;
    auto gather = [&](int j0, int j1) __attribute__((always_inline)) {
#pragma unroll
      for (int j = j0; j < j1; ++j)
        q[j] = __builtin_amdgcn_raw_buffer_load_b128(__builtin_amdgcn_make_buffer_rsrc((void*)ws, 0, 0x100000, 0x00020000), gsrc + j * 2048u, 0, 16  );
    };
    auto combine = [&](unsigned tag) __attribute__((always_inline)) {
      bad = 0u; v0 = 0.f; v1 = 0.f;
#pragma unroll
      for (int j = 0; j < 8; ++j) { bad |= (q[j][1] ^ tag) | (q[j][3] ^ tag); v0 = add1(v0, __uint_as_float(q[j][0])); v1 = add1(v1, __uint_as_float(q[j][2])); }
      if (d >= 2) {
        v0 *= 0.125f;
        float dv = 0.f;
#pragma unroll
        for (int j = 0; j < 8; ++j) { const float e = __uint_as_float(q[j][0]) - v0; dv = fmaf(e, e, dv); }
        v1 = __builtin_amdgcn_rsqf(fmaf(dv, 128.f, v1) * (1.f / 1024.f) + 1e-5f);
      }
    };
    ep = load_word(ws, (lane & 1) ? WS_EPC + ni * 128 : WS_EPR + mi * 128);
    STAMP(2);
    BARRIER_N();
    unsigned epr = 0, epc = 0;
    static_for<0, NST - 1>([&](auto S) {
      constexpr int st = S;
      if constexpr (st <= 3) {
        if ((st >> 1) == sp) {
#pragma unroll
          for (int i = 0; i < 4; ++i) xr[(st & 1) * 4 + i] = *(const LAS u32x4*)(sx + st * SLOT + ((ssw ^ i) * 16u));
        }
      }
      if constexpr (st <= 1) {
        const u32x4 wv = *(const LAS u32x4*)(cw + st * SLOT);
        const float ga = bf_lo(*(const LAS u16*)(lds0 + LDS_G + st * 128 + lane * 2)), ba = bf_lo(*(const LAS u16*)(lds0 + LDS_B + st * 128 + lane * 2));
#pragma unroll
        for (int i = 0; i < 4; ++i) {
          const float l0 = bf_lo(wv[i]), h0 = bf_hi(wv[i]);
          const unsigned pa = cvt_pk(mul1(ga, l0), mul1(ga, h0));
          if constexpr (st == 0) { p1[2 * i] = bf_lo(pa); p1[2 * i + 1] = bf_hi(pa); p2[2 * i] = mul1(ba, l0); p2[2 * i + 1] = mul1(ba, h0); }
          else { p1[2 * i] = add1(p1[2 * i], bf_lo(pa)); p1[2 * i + 1] = add1(p1[2 * i + 1], bf_hi(pa)); p2[2 * i] = fma1(ba, l0, p2[2 * i]); p2[2 * i + 1] = fma1(ba, h0, p2[2 * i + 1]); }
        }
      }
      if constexpr (st == 2) {
        float a[8];
#pragma unroll
        for (int i = 0; i < 8; ++i) {
          auto rr = __builtin_amdgcn_permlane32_swap(__float_as_uint(p1[i]), __float_as_uint(p2[i]), false, false);
          a[i] = add1(__uint_as_float(rr[0]), __uint_as_float(rr[1]));
        }
        asm volatile("s_nop 1" : "+v"(a[0]), "+v"(a[1]), "+v"(a[2]), "+v"(a[3]), "+v"(a[4]), "+v"(a[5]), "+v"(a[6]), "+v"(a[7]));
#pragma unroll
        for (int i = 0; i < 4; ++i) {
          auto rr = __builtin_amdgcn_permlane16_swap(__float_as_uint(a[i]), __float_as_uint(a[4 + i]), false, false);
          cs[i] = add1(__uint_as_float(rr[0]), __uint_as_float(rr[1]));
        }
        asm volatile("" : "+v"(cs[0]), "+v"(cs[1]), "+v"(cs[2]), "+v"(cs[3]));
      }
      if constexpr (st == 3) {
#pragma unroll
        for (int i = 0; i < 4; ++i) cs[i] = sum16(cs[i]);
        pub = l4 == 0 ? cs[0] : (l4 == 1 ? cs[1] : (l4 == 2 ? cs[2] : cs[3]));
        epr = (unsigned)__builtin_amdgcn_readlane((int)ep, 0); epc = (unsigned)__builtin_amdgcn_readlane((int)ep, 1);
      }
      if constexpr (st <= 1) { PIN8(p1); PIN8(p2); }
      if constexpr (st == 3) asm volatile("" : "+v"(pub));
      if constexpr (st >= 4 && st <= 6) __builtin_amdgcn_s_setprio(3);
      if constexpr (st == 4) {
        f32x4 sa = {0.f, 0.f, 0.f, 0.f}, sb = sa;
        const s16x4 ones = {0x3f80, 0x3f80, 0x3f80, 0x3f80};
#pragma unroll
        for (int j = 0; j < 8; ++j) {
          sa = __builtin_amdgcn_mfma_f32_4x4x4bf16_1k(ones, __builtin_bit_cast(s16x4, (u32x2){xr[j][0], xr[j][1]}), sa, 0, 0, 0);
          sb = __builtin_amdgcn_mfma_f32_4x4x4bf16_1k(ones, __builtin_bit_cast(s16x4, (u32x2){xr[j][2], xr[j][3]}), sb, 0, 0, 0);
        }
        SCHED(); __builtin_amdgcn_s_setprio(0);
        mean = dpp_add<0xB1>(sa[0] + sb[0]) * (1.f / 128.f);
        asm volatile("" : "+v"(mean));
      }
      if constexpr (st == 5 || st == 6) {
        constexpr int j0 = (st - 5) * 4;
        const unsigned one = 0x3f80u << ((lane & 1) * 16);
        const s16x4 ident = __builtin_bit_cast(s16x4, (u32x2){(lane & 2) ? 0u : one, (lane & 2) ? one : 0u});
        const f32x4 nm = {-mean, -mean, -mean, -mean};
        f32x4 dv[8];
#pragma unroll
        for (int j = 0; j < 4; ++j) {
          dv[2 * j] = __builtin_amdgcn_mfma_f32_4x4x4bf16_1k(ident, __builtin_bit_cast(s16x4, (u32x2){xr[j0 + j][0], xr[j0 + j][1]}), nm, 0, 0, 0);
          dv[2 * j + 1] = __builtin_amdgcn_mfma_f32_4x4x4bf16_1k(ident, __builtin_bit_cast(s16x4, (u32x2){xr[j0 + j][2], xr[j0 + j][3]}), nm, 0, 0, 0);
        }
        SCHED(); __builtin_amdgcn_s_setprio(0);
        asm volatile("s_nop 7" : "+v"(dv[0]), "+v"(dv[1]), "+v"(dv[2]), "+v"(dv[3]), "+v"(dv[4]), "+v"(dv[5]), "+v"(dv[6]), "+v"(dv[7]));
        f32x4 s4 = {part, 0.f, 0.f, 0.f};
#pragma unroll
        for (int j = 0; j < 8; ++j)
#pragma unroll
          for (int e = 0; e < 4; ++e) s4[e] = fma1(dv[j][e], dv[j][e], s4[e]);
        part = add1(add1(s4[0], s4[1]), add1(s4[2], s4[3]));
        asm volatile("" : "+v"(part));
      }
      SCHED();
      if constexpr (st == 4) {
        if (l4 < 4) store_granule(ws, WS_C + ((size_t)((ni * 8 + slab) * 128 + scol + ((lane >> 4) & 1) * 4 + l4) * 2 + (lane >> 5)) * 8, epc + 1u, pub);
      }
      if constexpr (st == 7) {
        const float m2 = dpp_add<0xB1>(part);
        store_granule(ws, WS_RS + ((size_t)(mi * 8 + rslab) * 256 + t) * 8, epr + 1u, (t & 1) ? m2 : mean);
      }
      if constexpr (st == 8) { if (d < 2) gather(0, 4); }
      if constexpr (st == 9) { if (d < 2) gather(4, 8); }
      if constexpr (st == 10) { if (d >= 2) gather(0, 4); }
      if constexpr (st == 11) { if (d >= 2) gather(4, 8); }
      if constexpr (st == 13) { if (d >= 2) { combine(epr + 1u); asm volatile("" : "+v"(v0), "+v"(v1), "+v"(bad)); } }
      if constexpr (st == 11) { if (d < 2) { combine(epc + 1u); asm volatile("" : "+v"(v0), "+v"(v1), "+v"(bad)); } }
      if constexpr (st == 14) {
        const unsigned tag = (d < 2 ? epc : epr) + 1u;
        bool ok = bad == 0u;
        if (__builtin_expect(!__all(ok), 0)) {
#pragma unroll 1
          for (int it = 0; it < 20000; ++it) {
            asm volatile("" ::: "memory");
            gather(0, 8);
            combine(tag);
            ok = bad == 0u;
            if (__all(ok)) break;
            __builtin_amdgcn_s_sleep(2);
          }
        }
        if (__builtin_expect(!__all(ok), 0)) {
          v0 = 0.f; v1 = 0.f;
          if (d < 2) {
            const u16* wc = w + ni * 128 + t;
#pragma unroll 1
            for (int k = 0; k < KD; ++k) {
              const float wv = bf_lo(wc[(size_t)k * ND]);
              v0 += bf_lo(cvt_pk(bf_lo(g[k]) * wv, 0.f)); v1 = fmaf(bf_lo(b[k]), wv, v1);
            }
          } else {
            const u16* xrow = x + (size_t)(mi * 128 + (t - 128)) * KD;
#pragma unroll 1
            for (int k = 0; k < KD; ++k) v0 += bf_lo(xrow[k]);
            v0 *= (1.f / 1024.f);
#pragma unroll 1
            for (int k = 0; k < KD; ++k) { const float dd = bf_lo(xrow[k]) - v0; v1 = fmaf(dd, dd, v1); }
            v1 = __builtin_amdgcn_rsqf(v1 * (1.f / 1024.f) + 1e-5f);
          }
        }
        if (d < 2) { *(LAS float*)(lds0 + LDS_TAB + 1024 + t * 4) = v0; *(LAS float*)(lds0 + LDS_TAB + 1536 + t * 4) = v1; }
        else *(LAS f32x2*)(lds0 + LDS_TAB + (t - 128) * 8) = (f32x2){v0, v1};
      }
      if constexpr (st <= 3 || st == 14) BARRIER_L(); else BARRIER_N();
      STAMP(3 + st);
    });
    if (t == 0) {
      __hip_atomic_fetch_add((GAS unsigned*)(ws + WS_EPR + mi * 128), 1u, __ATOMIC_RELAXED, __HIP_MEMORY_SCOPE_AGENT);
      __hip_atomic_fetch_add((GAS unsigned*)(ws + WS_EPC + ni * 128), 1u, __ATOMIC_RELAXED, __HIP_MEMORY_SCOPE_AGENT);
    }
  } else if (wave >= 4) {
    const int d = wave - 4;
    if (PRIO_L) __builtin_amdgcn_s_setprio(PRIO_L);
    unsigned xo[4], wo[4];
#pragma unroll
    for (int i = 0; i < 4; ++i) {
      const int row = i * 8 + (lane >> 3);
      xo[i] = (unsigned)row * 2048u + (unsigned)(((lane & 7) ^ ((row >> 1) & 7)) * 16) - (unsigned)i * 1024u;
      const int kr = i * 4 + (lane >> 4);
      wo[i] = (unsigned)kr * 2048u + (unsigned)(((lane & 15) ^ (((lane >> 4) << 2) | i)) * 16) - (unsigned)i * 1024u;
    }
    const unsigned dst = lds0 + (unsigned)d * 4096u;
    const char* xb = (const char*)x + (size_t)mi * 262144 + d * 65536;
    const char* wb = (const char*)w + d * 32768 + ni * 256;
    auto issue = [&](int st) {
      const unsigned ds_ = dst + (unsigned)(st & (RING - 1)) * SLOT;
      const int ks_ = (st + rot) & (NST - 1);
      glds16x4(wo[0], wo[1], wo[2], wo[3], wb + (size_t)ks_ * 131072, ds_ + 16384u);
      glds16x4(xo[0], xo[1], xo[2], xo[3], xb + ks_ * 128, ds_);
    };
    issue(0);
    if (d < 2) glds4((unsigned)lane * 4u, (const char*)(d ? g : b) + rot * 128, lds0 + (d ? LDS_G : LDS_B));
    issue(1);
    STAMP(2);
    wait_vm<8>();
    STAMP(3);
    BARRIER_N();
#pragma unroll 1
    for (int j = 0; j < NST - 2; ++j) {
      issue(j + 2);
      wait_vm<8>();
      BARRIER_N();
      STAMP_DYN(j + 4);
    }
    wait_vm<0>();
    BARRIER_N();
    STAMP(18);
  } else {
    const int wn = wave;
    {
      const u32x2 g4 = *(const GAS u32x2*)((const char*)g + ((tid * 8 + rot * 128) & 2047));
      const unsigned a = lds0 + LDS_GT + (unsigned)(((tid >> 1) & 3) * 4 * GROW + (tid >> 3) * 4 + (tid & 1) * 2);
      *(LAS u16*)(a) = (u16)g4[0]; *(LAS u16*)(a + GROW) = (u16)(g4[0] >> 16);
      *(LAS u16*)(a + 2 * GROW) = (u16)g4[1]; *(LAS u16*)(a + 3 * GROW) = (u16)(g4[1] >> 16);
    }
    STAMP(3);
    if (PRIO_M) __builtin_amdgcn_s_setprio(PRIO_M);
    const int r = lane & 15, q4 = lane >> 4, sw = (r >> 1) & 7;
    unsigned offx[2], offw[4];
#pragma unroll
    for (int ks = 0; ks < 2; ++ks) offx[ks] = lds0 + (unsigned)r * 128u + (unsigned)(((4 * ks + q4) ^ sw) * 16);
    {
      const int q = (lane & 15) >> 2, p = lane & 3;
#pragma unroll
      for (int i = 0; i < 4; ++i) {
        const int tt = i & 1, c = i >> 1, row = 8 * q4 + 4 * tt + q, ch = 4 * wn + 2 * c + (p >> 1);
        offw[i] = lds0 + 16384u + (unsigned)(256 * row + 16 * (ch ^ ((q << 2) | ((2 * q4 + tt) & 3))) + 8 * (p & 1));
      }
    }
    unsigned offg = lds0 + LDS_GT + (unsigned)((q4 * 4 + (lane & 3)) * GROW);
    const unsigned li = lane & 3;
    unsigned sel[4] = {li == 0 ? 0x0c0c0100u : (li == 1 ? 0x01000c0cu : 0x0c0c0c0cu), li == 2 ? 0x0c0c0100u : (li == 3 ? 0x01000c0cu : 0x0c0c0c0cu),
                       li == 0 ? 0x0c0c0302u : (li == 1 ? 0x03020c0cu : 0x0c0c0c0cu), li == 2 ? 0x0c0c0302u : (li == 3 ? 0x03020c0cu : 0x0c0c0c0cu)};
    f32x4 acc[8][2] = {};
    bf16x8 xf[8];
    s16x4 wr[4];
    u32x2 gq;
    u32x4 dg;
    u32x4 wa[2], wb[2];
    f32x4 dd[4];
    const f32x4 z4 = {0.f, 0.f, 0.f, 0.f};
#define RX(mt, ks) xf[mt] = *(const LAS bf16x8*)(offx[ks] + (mt) * 2048)
#define RW(i, ks) wr[i] = __builtin_amdgcn_ds_read_tr16_b64_v4i16((LAS s16x4*)(offw[i] + (ks) * 8192))
#define RG(o) gq = *(const LAS u32x2*)(offg + (o))
#define DG(i, G) dg[i] = __builtin_amdgcn_perm(G, G, sel[i])
#define SM(i) dd[i] = __builtin_amdgcn_mfma_f32_4x4x4bf16_1k(__builtin_bit_cast(s16x4, (u32x2){dg[((i) & 1) * 2], dg[((i) & 1) * 2 + 1]}), wr[i], z4, 0, 0, 0)
#define CV(W, e) W[(e) >> 2][(e) & 3] = cvt_pk(dd[(e) >> 1][((e) & 1) * 2], dd[(e) >> 1][((e) & 1) * 2 + 1])
#define MM(mt, c, W) acc[mt][c] = __builtin_amdgcn_mfma_f32_16x16x32_bf16(__builtin_bit_cast(bf16x8, W[c]), xf[mt], acc[mt][c], 0, 0, 0)
#define G2(mt, W, A, B) do { MM(mt, 0, W); SCHED(); A; SCHED(); MM(mt, 1, W); SCHED(); B; SCHED(); } while (0)
#define STEP(WC, WN, kx, kw, G, MID, E0, E1, E2) do { \
      G2(0, WC, (void)0, (RX(0, kx))); \
      SM(0); SM(1); SM(2); SM(3); SCHED(); \
      G2(1, WC, (DG(0, G), DG(1, G)), (RX(1, kx), DG(2, G))); \
      G2(2, WC, (CV(WN, 0), DG(3, G)), (RX(2, kx), CV(WN, 1))); \
      G2(3, WC, (CV(WN, 2), E0), (RX(3, kx), CV(WN, 3))); \
      G2(4, WC, (CV(WN, 4), E1), (RX(4, kx), CV(WN, 5))); \
      G2(5, WC, (CV(WN, 6), E2), (RX(5, kx), CV(WN, 7))); \
      MID; \
      G2(6, WC, (RW(0, kw), RW(1, kw)), (RX(6, kx))); \
      G2(7, WC, (RW(2, kw), RW(3, kw)), (RX(7, kx))); } while (0)
#pragma unroll
    for (int i = 0; i < 8; ++i) asm volatile("" : "+v"(acc[i][0]), "+v"(acc[i][1]));
    asm volatile("" : "+v"(offx[0]), "+v"(offx[1]), "+v"(offw[0]), "+v"(offw[1]), "+v"(offw[2]), "+v"(offw[3]), "+v"(offg),
                      "+v"(sel[0]), "+v"(sel[1]), "+v"(sel[2]), "+v"(sel[3]));
    __builtin_amdgcn_s_waitcnt(0xC07F);
    STAMP(4);
    BARRIER_N();
    STAMP(5);
    RG(0); RW(0, 0); RW(1, 0); RW(2, 0); RW(3, 0);
    RX(0, 0); RX(1, 0); RX(2, 0); RX(3, 0); RX(4, 0); RX(5, 0); RX(6, 0); RX(7, 0);
    DG(0, gq[0]); DG(1, gq[0]); DG(2, gq[0]); DG(3, gq[0]); SCHED();
    SM(0); SM(1); SM(2); SM(3); SCHED();
    RW(0, 1); RW(1, 1); RW(2, 1); RW(3, 1);
    DG(0, gq[1]); DG(1, gq[1]); DG(2, gq[1]); DG(3, gq[1]); SCHED();
    RG(8);
    CV(wa, 0); CV(wa, 1); CV(wa, 2); CV(wa, 3); CV(wa, 4); CV(wa, 5); CV(wa, 6); CV(wa, 7); SCHED();
#pragma unroll 1
    for (int s = 0; s < NST - 1; ++s) {
      const int dlt = ((s & (RING - 1)) == RING - 1) ? -(RING - 1) * SLOT : SLOT;
      STEP(wa, wb, 1, 0, gq[0], BARRIER_N()  , (offw[0] += dlt, offw[1] += dlt), (offw[2] += dlt, offw[3] += dlt), (offx[0] += dlt));
      STEP(wb, wa, 0, 1, gq[1], (void)0, (RG(16), offx[1] += dlt), (offg += 8u), (void)0);
    }
    f32x4 c1v[2], c2v[2];
    f32x2 rs[8];
    const unsigned tc = lds0 + LDS_TAB + 1024 + (unsigned)(wn * 32 + q4 * 4) * 4u, tr = lds0 + LDS_TAB + (unsigned)r * 8u;
#define RS(i) rs[i] = *(const LAS f32x2*)(tr + (i) * 128)
    G2(0, wa, (void)0, (RX(0, 1)));
    SM(0); SM(1); SM(2); SM(3); SCHED();
    G2(1, wa, (void)0, (RX(1, 1)));
    G2(2, wa, (CV(wb, 0)), (RX(2, 1), CV(wb, 1)));
    G2(3, wa, (CV(wb, 2)), (RX(3, 1), CV(wb, 3)));
    G2(4, wa, (CV(wb, 4)), (RX(4, 1), CV(wb, 5)));
    G2(5, wa, (CV(wb, 6)), (RX(5, 1), CV(wb, 7)));
    G2(6, wa, (void)0, (RX(6, 1)));
    G2(7, wa, (void)0, (RX(7, 1)));
    G2(0, wb, (c1v[0] = *(const LAS f32x4*)(tc)), (c1v[1] = *(const LAS f32x4*)(tc + 64)));
    G2(1, wb, (c2v[0] = *(const LAS f32x4*)(tc + 512)), (c2v[1] = *(const LAS f32x4*)(tc + 512 + 64)));
    G2(2, wb, (RS(0)), (RS(1))); G2(3, wb, (RS(2)), (RS(3))); G2(4, wb, (RS(4)), (RS(5))); G2(5, wb, (RS(6)), (RS(7)));
    G2(6, wb, (void)0, (void)0); G2(7, wb, (void)0, (void)0);
    STAMP(20);
    const unsigned so = lds0 + (unsigned)r * 256u + (unsigned)(q4 & 1) * 8u;
    const unsigned sc0 = (unsigned)(((wn * 4 + (q4 >> 1)) ^ (r & 7)) * 16), sc1 = sc0 ^ 32u;
#pragma unroll
    for (int m2 = 0; m2 < 4; ++m2) {
      f32x4 tq[4];
#pragma unroll
      for (int i = 0; i < 4; ++i) tq[i] = acc[2 * m2 + (i >> 1)][i & 1] - c1v[i & 1] * rs[2 * m2 + (i >> 1)][0];
#pragma unroll
      for (int i = 0; i < 4; ++i) tq[i] = tq[i] * rs[2 * m2 + (i >> 1)][1] + c2v[i & 1];
#pragma unroll
      for (int i = 0; i < 4; ++i)
        *(LAS u32x2*)(so + (2 * m2 + (i >> 1)) * 4096 + ((i & 1) ? sc1 : sc0)) = (u32x2){cvt_pk(tq[i][0], tq[i][1]), cvt_pk(tq[i][2], tq[i][3])};
      SCHED();
      if (m2 == 1) BARRIER_L();
    }
  }
  if (wave >= 4) {
    BARRIER_N();
    store_rows<2>(lds0, (wave - 4) * 8, lane, (char*)out + (size_t)mi * 262144 + ni * 256);
  }
  STAMP(24);
  BARRIER_L();
  STAMP(25);
  if (wave < 8) store_rows<2>(lds0, 64 + wave * 8, lane, (char*)out + (size_t)mi * 262144 + ni * 256);
  STAMP_FLUSH();
}

extern "C" void kernel_launch(void* const* d_in, const int* in_sizes, int n_in, void* d_out, int out_size, void* d_ws, size_t ws_size,
                              hipStream_t stream) {
  const u16* x = (const u16*)d_in[0]; const u16* g = (const u16*)d_in[1]; const u16* b = (const u16*)d_in[2]; const u16* w = (const u16*)d_in[3];
  (void)hipFuncSetAttribute((const void*)lnlin_kernel, hipFuncAttributeMaxDynamicSharedMemorySize, LDS_TOTAL);
  lnlin_kernel<<<256, 768, LDS_TOTAL, stream>>>(x, g, b, w, (u16*)d_out, (char*)d_ws);
}
```

```cpp
#include <hip/hip_runtime.h>
#include <hip/hip_bf16.h>
#include <utility>
#pragma clang diagnostic ignored "-Wint-to-pointer-cast"

typedef short bf16x8 __attribute__((ext_vector_type(8)));
typedef short s16x4 __attribute__((ext_vector_type(4)));
typedef float f32x4 __attribute__((ext_vector_type(4)));
typedef float f32x2 __attribute__((ext_vector_type(2)));
typedef unsigned u32x4 __attribute__((ext_vector_type(4)));
typedef unsigned u32x2 __attribute__((ext_vector_type(2)));
typedef __bf16 bf16x2_t __attribute__((ext_vector_type(2)));
typedef unsigned short u16;
typedef unsigned long long u64;
#define GAS __attribute__((address_space(1)))
#define LAS __attribute__((address_space(3)))

#define ST_FLAVOR "nt"

constexpr int KD = 1024, ND = 1024;
constexpr int NST = 16;
constexpr int RING = 4;
constexpr int SLOT = 32768;
constexpr int GROW = 136;
constexpr int LDS_GT = RING * SLOT;
constexpr int LDS_G = LDS_GT + 16 * GROW;
constexpr int LDS_B = LDS_G + 256;
constexpr int LDS_TAB = LDS_B + 256;
constexpr int LDS_TOTAL = LDS_TAB + 2048;

constexpr size_t WS_EPR = 0;
constexpr size_t WS_EPC = 32 * 128;
constexpr size_t WS_C = 8192;
constexpr size_t WS_RS = WS_C + 8 * 16384;


template <int B, int E, class F> __device__ __forceinline__ void static_for(F&& f) {
  if constexpr (B < E) { f(std::integral_constant<int, B>{}); static_for<B + 1, E>(f); }
}
__device__ __forceinline__ float bf_lo(unsigned u) { return __uint_as_float(u << 16); }
__device__ __forceinline__ float bf_hi(unsigned u) { return __uint_as_float(u & 0xffff0000u); }
__device__ __forceinline__ unsigned cvt_pk(float lo, float hi) {
  f32x2 v = {lo, hi}; return __builtin_bit_cast(unsigned, __builtin_convertvector(v, bf16x2_t));
}
template <int CTRL> __device__ __forceinline__ float dpp_add(float x) {
  return x + __builtin_bit_cast(float, __builtin_amdgcn_update_dpp(0, __builtin_bit_cast(int, x), CTRL, 0xf, 0xf, true));
}
__device__ __forceinline__ float sum16(float x) {
  x = dpp_add<0x128>(x); x = dpp_add<0x124>(x); x = dpp_add<0x122>(x); x = dpp_add<0x121>(x);
  return x;
}
__device__ __forceinline__ float add1(float a, float b) { float r; asm("v_add_f32 %0, %1, %2" : "=v"(r) : "v"(a), "v"(b)); return r; }
__device__ __forceinline__ float mul1(float a, float b) { float r; asm("v_mul_f32 %0, %1, %2" : "=v"(r) : "v"(a), "v"(b)); return r; }
__device__ __forceinline__ float fma1(float a, float b, float c) { float r; asm("v_fma_f32 %0, %1, %2, %3" : "=v"(r) : "v"(a), "v"(b), "v"(c)); return r; }
#define PIN8(a) asm volatile("" : "+v"(a[0]), "+v"(a[1]), "+v"(a[2]), "+v"(a[3]), "+v"(a[4]), "+v"(a[5]), "+v"(a[6]), "+v"(a[7]))
#define SCHED() __builtin_amdgcn_sched_barrier(0)
#define BARRIER_L() do { SCHED(); asm volatile("s_waitcnt lgkmcnt(0)\n\ts_barrier" ::: "memory"); SCHED(); } while (0)
#define BARRIER_N() do { SCHED(); asm volatile("s_barrier" ::: "memory"); SCHED(); } while (0)
template <int N> __device__ __forceinline__ void wait_vm() { asm volatile("s_waitcnt vmcnt(%0)" ::"n"(N) : "memory"); }

__device__ __forceinline__ void glds16x4(unsigned v0, unsigned v1, unsigned v2, unsigned v3, const void* sbase, unsigned lds_dst) {
  unsigned keep;
  asm volatile("s_mov_b32 %0, m0\n\ts_mov_b32 m0, %6\n\ts_nop 0\n\t"
               "global_load_lds_dwordx4 %1, %5\n\tglobal_load_lds_dwordx4 %2, %5 offset:1024\n\t"
               "global_load_lds_dwordx4 %3, %5 offset:2048\n\tglobal_load_lds_dwordx4 %4, %5 offset:3072\n\ts_mov_b32 m0, %0"
               : "=&s"(keep) : "v"(v0), "v"(v1), "v"(v2), "v"(v3), "s"(sbase), "s"(lds_dst) : "memory");
}
__device__ __forceinline__ void glds4(unsigned v0, const void* sbase, unsigned lds_dst) {
  unsigned keep;
  asm volatile("s_mov_b32 %0, m0\n\ts_mov_b32 m0, %3\n\ts_nop 0\n\tglobal_load_lds_dword %1, %2\n\ts_mov_b32 m0, %0"
               : "=&s"(keep) : "v"(v0), "s"(sbase), "s"(lds_dst) : "memory");
}
__device__ __forceinline__ void store_granule(char* ws, size_t off, unsigned tag, float v) {
  __hip_atomic_store((GAS u64*)(ws + off), ((u64)tag << 32) | __float_as_uint(v), __ATOMIC_RELAXED, __HIP_MEMORY_SCOPE_AGENT);
}
__device__ __forceinline__ unsigned load_word(const char* ws, size_t off) {
  return __hip_atomic_load((GAS unsigned*)(ws + off), __ATOMIC_RELAXED, __HIP_MEMORY_SCOPE_AGENT);
}

template <int N> __device__ __forceinline__ void store_rows(unsigned lds0, int rowb, int lane, char* ob) {
  const int row0 = rowb + (lane >> 4), ch = lane & 15;
  u32x4 v[N];
#pragma unroll
  for (int i = 0; i < N; ++i) { const int row = row0 + i * 4; v[i] = *(const LAS u32x4*)(lds0 + row * 256 + ((ch ^ (row & 7)) * 16)); }
  const unsigned oo = (unsigned)row0 * 2048u + (unsigned)ch * 16u;
#pragma unroll
  for (int i = 0; i < N; ++i)
    asm volatile("global_store_dwordx4 %0, %1, %2 " ST_FLAVOR "\n\ts_nop 1" :: "v"(oo + i * 8192u), "v"(v[i]), "s"(ob) : "memory");
}

__global__ __launch_bounds__(768, 3) void lnlin_kernel(const u16* __restrict__ x, const u16* __restrict__ g, const u16* __restrict__ b,
                                                       const u16* __restrict__ w, u16* __restrict__ out, char* ws) {
  extern __shared__ __attribute__((aligned(16))) char smem_g[];
  const int bid = blockIdx.x, xcd = bid & 7, idx = bid >> 3;
  const int mi = xcd * 4 + (idx >> 3), ni = idx & 7;
  const int slab = ((mi >> 2) + 2 * (ni & 3)) & 7, rot = slab * 2, sp = ni >> 2, rslab = (slab + sp) & 7;
  const int tid = threadIdx.x, lane = tid & 63;
  const int wave = __builtin_amdgcn_readfirstlane(tid >> 6);
  const unsigned lds0 = (unsigned)(size_t)(LAS char*)smem_g;

  if (wave >= 8) {
    const int d = wave - 8, t = tid - 512;
    const int scol = (mi & 3) * 32 + d * 8;
    const unsigned cw = lds0 + 16384u + (unsigned)(256 * lane + 16 * ((scol >> 3) ^ (((lane & 3) << 2) | ((lane >> 2) & 3))));
    const int srow = t >> 1, l4 = lane & 15;
    const unsigned sx = lds0 + (unsigned)srow * 128u, ssw = (unsigned)(((t & 1) * 4) ^ ((srow >> 1) & 7));
    u32x4 xr[8];
    unsigned ep;
    float p1[8], p2[8], cs[4], part = 0.f, mean = 0.f, pub = 0.f, v0 = 0.f, v1 = 0.f;
    unsigned bad = 0u;
    u32x4 q[8];
    const unsigned gsrc = d < 2 ? (unsigned)WS_C + (unsigned)ni * 16384u + (unsigned)t * 16u : (unsigned)WS_RS + (unsigned)mi * 16384u + (unsigned)(t - 128) * 16u;
    auto gather = [&](int j0, int j1) __attribute__((always_inline)) {
#pragma unroll
      for (int j = j0; j < j1; ++j)
        q[j] = __builtin_amdgcn_raw_buffer_load_b128(__builtin_amdgcn_make_buffer_rsrc((void*)ws, 0, 0x100000, 0x00020000), gsrc + j * 2048u, 0, 16  );
    };
    auto combine = [&](unsigned tag) __attribute__((always_inline)) {
      bad = 0u; v0 = 0.f; v1 = 0.f;
#pragma unroll
      for (int j = 0; j < 8; ++j) { bad |= (q[j][1] ^ tag) | (q[j][3] ^ tag); v0 = add1(v0, __uint_as_float(q[j][0])); v1 = add1(v1, __uint_as_float(q[j][2])); }
      if (d >= 2) {
        v0 *= 0.125f;
        float dv = 0.f;
#pragma unroll
        for (int j = 0; j < 8; ++j) { const float e = __uint_as_float(q[j][0]) - v0; dv = fmaf(e, e, dv); }
        v1 = __builtin_amdgcn_rsqf(fmaf(dv, 128.f, v1) * (1.f / 1024.f) + 1e-5f);
      }
    };
    ep = load_word(ws, (lane & 1) ? WS_EPC + ni * 128 : WS_EPR + mi * 128);
    BARRIER_N();
    unsigned epr = 0, epc = 0;
    static_for<0, NST - 1>([&](auto S) {
      constexpr int st = S;
      if constexpr (st <= 3) {
        if ((st >> 1) == sp) {
#pragma unroll
          for (int i = 0; i < 4; ++i) xr[(st & 1) * 4 + i] = *(const LAS u32x4*)(sx + st * SLOT + ((ssw ^ i) * 16u));
        }
      }
      if constexpr (st <= 1) {
        const u32x4 wv = *(const LAS u32x4*)(cw + st * SLOT);
        const float ga = bf_lo(*(const LAS u16*)(lds0 + LDS_G + st * 128 + lane * 2)), ba = bf_lo(*(const LAS u16*)(lds0 + LDS_B + st * 128 + lane * 2));
#pragma unroll
        for (int i = 0; i < 4; ++i) {
          const float l0 = bf_lo(wv[i]), h0 = bf_hi(wv[i]);
          const unsigned pa = cvt_pk(mul1(ga, l0), mul1(ga, h0));
          if constexpr (st == 0) { p1[2 * i] = bf_lo(pa); p1[2 * i + 1] = bf_hi(pa); p2[2 * i] = mul1(ba, l0); p2[2 * i + 1] = mul1(ba, h0); }
          else { p1[2 * i] = add1(p1[2 * i], bf_lo(pa)); p1[2 * i + 1] = add1(p1[2 * i + 1], bf_hi(pa)); p2[2 * i] = fma1(ba, l0, p2[2 * i]); p2[2 * i + 1] = fma1(ba, h0, p2[2 * i + 1]); }
        }
      }
      if constexpr (st == 2) {
        float a[8];
#pragma unroll
        for (int i = 0; i < 8; ++i) {
          auto rr = __builtin_amdgcn_permlane32_swap(__float_as_uint(p1[i]), __float_as_uint(p2[i]), false, false);
          a[i] = add1(__uint_as_float(rr[0]), __uint_as_float(rr[1]));
        }
        asm volatile("s_nop 1" : "+v"(a[0]), "+v"(a[1]), "+v"(a[2]), "+v"(a[3]), "+v"(a[4]), "+v"(a[5]), "+v"(a[6]), "+v"(a[7]));
#pragma unroll
        for (int i = 0; i < 4; ++i) {
          auto rr = __builtin_amdgcn_permlane16_swap(__float_as_uint(a[i]), __float_as_uint(a[4 + i]), false, false);
          cs[i] = add1(__uint_as_float(rr[0]), __uint_as_float(rr[1]));
        }
        asm volatile("" : "+v"(cs[0]), "+v"(cs[1]), "+v"(cs[2]), "+v"(cs[3]));
      }
      if constexpr (st == 3) {
#pragma unroll
        for (int i = 0; i < 4; ++i) cs[i] = sum16(cs[i]);
        pub = l4 == 0 ? cs[0] : (l4 == 1 ? cs[1] : (l4 == 2 ? cs[2] : cs[3]));
        epr = (unsigned)__builtin_amdgcn_readlane((int)ep, 0); epc = (unsigned)__builtin_amdgcn_readlane((int)ep, 1);
      }
      if constexpr (st <= 1) { PIN8(p1); PIN8(p2); }
      if constexpr (st == 3) asm volatile("" : "+v"(pub));
      if constexpr (st >= 4 && st <= 6) __builtin_amdgcn_s_setprio(3);
      if constexpr (st == 4) {
        f32x4 sa = {0.f, 0.f, 0.f, 0.f}, sb = sa;
        const s16x4 ones = {0x3f80, 0x3f80, 0x3f80, 0x3f80};
#pragma unroll
        for (int j = 0; j < 8; ++j) {
          sa = __builtin_amdgcn_mfma_f32_4x4x4bf16_1k(ones, __builtin_bit_cast(s16x4, (u32x2){xr[j][0], xr[j][1]}), sa, 0, 0, 0);
          sb = __builtin_amdgcn_mfma_f32_4x4x4bf16_1k(ones, __builtin_bit_cast(s16x4, (u32x2){xr[j][2], xr[j][3]}), sb, 0, 0, 0);
        }
        SCHED(); __builtin_amdgcn_s_setprio(0);
        mean = dpp_add<0xB1>(sa[0] + sb[0]) * (1.f / 128.f);
        asm volatile("" : "+v"(mean));
      }
      if constexpr (st == 5 || st == 6) {
        constexpr int j0 = (st - 5) * 4;
        const unsigned one = 0x3f80u << ((lane & 1) * 16);
        const s16x4 ident = __builtin_bit_cast(s16x4, (u32x2){(lane & 2) ? 0u : one, (lane & 2) ? one : 0u});
        const f32x4 nm = {-mean, -mean, -mean, -mean};
        f32x4 dv[8];
#pragma unroll
        for (int j = 0; j < 4; ++j) {
          dv[2 * j] = __builtin_amdgcn_mfma_f32_4x4x4bf16_1k(ident, __builtin_bit_cast(s16x4, (u32x2){xr[j0 + j][0], xr[j0 + j][1]}), nm, 0, 0, 0);
          dv[2 * j + 1] = __builtin_amdgcn_mfma_f32_4x4x4bf16_1k(ident, __builtin_bit_cast(s16x4, (u32x2){xr[j0 + j][2], xr[j0 + j][3]}), nm, 0, 0, 0);
        }
        SCHED(); __builtin_amdgcn_s_setprio(0);
        asm volatile("s_nop 7" : "+v"(dv[0]), "+v"(dv[1]), "+v"(dv[2]), "+v"(dv[3]), "+v"(dv[4]), "+v"(dv[5]), "+v"(dv[6]), "+v"(dv[7]));
        f32x4 s4 = {part, 0.f, 0.f, 0.f};
#pragma unroll
        for (int j = 0; j < 8; ++j)
#pragma unroll
          for (int e = 0; e < 4; ++e) s4[e] = fma1(dv[j][e], dv[j][e], s4[e]);
        part = add1(add1(s4[0], s4[1]), add1(s4[2], s4[3]));
        asm volatile("" : "+v"(part));
      }
      SCHED();
      if constexpr (st == 4) {
        if (l4 < 4) store_granule(ws, WS_C + ((size_t)((ni * 8 + slab) * 128 + scol + ((lane >> 4) & 1) * 4 + l4) * 2 + (lane >> 5)) * 8, epc + 1u, pub);
      }
      if constexpr (st == 7) {
        const float m2 = dpp_add<0xB1>(part);
        store_granule(ws, WS_RS + ((size_t)(mi * 8 + rslab) * 256 + t) * 8, epr + 1u, (t & 1) ? m2 : mean);
      }
      if constexpr (st == 8) { if (d < 2) gather(0, 4); }
      if constexpr (st == 9) { if (d < 2) gather(4, 8); }
      if constexpr (st == 10) { if (d >= 2) gather(0, 4); }
      if constexpr (st == 11) { if (d >= 2) gather(4, 8); }
      if constexpr (st == 13) { if (d >= 2) { combine(epr + 1u); asm volatile("" : "+v"(v0), "+v"(v1), "+v"(bad)); } }
      if constexpr (st == 11) { if (d < 2) { combine(epc + 1u); asm volatile("" : "+v"(v0), "+v"(v1), "+v"(bad)); } }
      if constexpr (st == 14) {
        const unsigned tag = (d < 2 ? epc : epr) + 1u;
        bool ok = bad == 0u;
        if (__builtin_expect(!__all(ok), 0)) {
#pragma unroll 1
          for (int it = 0; it < 20000; ++it) {
            asm volatile("" ::: "memory");
            gather(0, 8);
            combine(tag);
            ok = bad == 0u;
            if (__all(ok)) break;
            __builtin_amdgcn_s_sleep(2);
          }
        }
        if (__builtin_expect(!__all(ok), 0)) {
          v0 = 0.f; v1 = 0.f;
          if (d < 2) {
            const u16* wc = w + ni * 128 + t;
#pragma unroll 1
            for (int k = 0; k < KD; ++k) {
              const float wv = bf_lo(wc[(size_t)k * ND]);
              v0 += bf_lo(cvt_pk(bf_lo(g[k]) * wv, 0.f)); v1 = fmaf(bf_lo(b[k]), wv, v1);
            }
          } else {
            const u16* xrow = x + (size_t)(mi * 128 + (t - 128)) * KD;
#pragma unroll 1
            for (int k = 0; k < KD; ++k) v0 += bf_lo(xrow[k]);
            v0 *= (1.f / 1024.f);
#pragma unroll 1
            for (int k = 0; k < KD; ++k) { const float dd = bf_lo(xrow[k]) - v0; v1 = fmaf(dd, dd, v1); }
            v1 = __builtin_amdgcn_rsqf(v1 * (1.f / 1024.f) + 1e-5f);
          }
        }
        if (d < 2) { *(LAS float*)(lds0 + LDS_TAB + 1024 + t * 4) = v0; *(LAS float*)(lds0 + LDS_TAB + 1536 + t * 4) = v1; }
        else *(LAS f32x2*)(lds0 + LDS_TAB + (t - 128) * 8) = (f32x2){v0, v1};
      }
      if constexpr (st <= 3 || st == 14) BARRIER_L(); else BARRIER_N();
    });
    if (t == 0) {
      __hip_atomic_fetch_add((GAS unsigned*)(ws + WS_EPR + mi * 128), 1u, __ATOMIC_RELAXED, __HIP_MEMORY_SCOPE_AGENT);
      __hip_atomic_fetch_add((GAS unsigned*)(ws + WS_EPC + ni * 128), 1u, __ATOMIC_RELAXED, __HIP_MEMORY_SCOPE_AGENT);
    }
  } else if (wave >= 4) {
    const int d = wave - 4;
    unsigned xo[4], wo[4];
#pragma unroll
    for (int i = 0; i < 4; ++i) {
      const int row = i * 8 + (lane >> 3);
      xo[i] = (unsigned)row * 2048u + (unsigned)(((lane & 7) ^ ((row >> 1) & 7)) * 16) - (unsigned)i * 1024u;
      const int kr = i * 4 + (lane >> 4);
      wo[i] = (unsigned)kr * 2048u + (unsigned)(((lane & 15) ^ (((lane >> 4) << 2) | i)) * 16) - (unsigned)i * 1024u;
    }
    const unsigned dst = lds0 + (unsigned)d * 4096u;
    const char* xb = (const char*)x + (size_t)mi * 262144 + d * 65536;
    const char* wb = (const char*)w + d * 32768 + ni * 256;
    auto issue = [&](int st) {
      const unsigned ds_ = dst + (unsigned)(st & (RING - 1)) * SLOT;
      const int ks_ = (st + rot) & (NST - 1);
      glds16x4(wo[0], wo[1], wo[2], wo[3], wb + (size_t)ks_ * 131072, ds_ + 16384u);
      glds16x4(xo[0], xo[1], xo[2], xo[3], xb + ks_ * 128, ds_);
    };
    issue(0);
    if (d < 2) glds4((unsigned)lane * 4u, (const char*)(d ? g : b) + rot * 128, lds0 + (d ? LDS_G : LDS_B));
    issue(1);
    wait_vm<8>();
    BARRIER_N();
#pragma unroll 1
    for (int j = 0; j < NST - 2; ++j) {
      issue(j + 2);
      wait_vm<8>();
      BARRIER_N();
    }
    wait_vm<0>();
    BARRIER_N();
  } else {
    const int wn = wave;
    {
      const u32x2 g4 = *(const GAS u32x2*)((const char*)g + ((tid * 8 + rot * 128) & 2047));
      const unsigned a = lds0 + LDS_GT + (unsigned)(((tid >> 1) & 3) * 4 * GROW + (tid >> 3) * 4 + (tid & 1) * 2);
      *(LAS u16*)(a) = (u16)g4[0]; *(LAS u16*)(a + GROW) = (u16)(g4[0] >> 16);
      *(LAS u16*)(a + 2 * GROW) = (u16)g4[1]; *(LAS u16*)(a + 3 * GROW) = (u16)(g4[1] >> 16);
    }
    const int r = lane & 15, q4 = lane >> 4, sw = (r >> 1) & 7;
    unsigned offx[2], offw[4];
#pragma unroll
    for (int ks = 0; ks < 2; ++ks) offx[ks] = lds0 + (unsigned)r * 128u + (unsigned)(((4 * ks + q4) ^ sw) * 16);
    {
      const int q = (lane & 15) >> 2, p = lane & 3;
#pragma unroll
      for (int i = 0; i < 4; ++i) {
        const int tt = i & 1, c = i >> 1, row = 8 * q4 + 4 * tt + q, ch = 4 * wn + 2 * c + (p >> 1);
        offw[i] = lds0 + 16384u + (unsigned)(256 * row + 16 * (ch ^ ((q << 2) | ((2 * q4 + tt) & 3))) + 8 * (p & 1));
      }
    }
    unsigned offg = lds0 + LDS_GT + (unsigned)((q4 * 4 + (lane & 3)) * GROW);
    const unsigned li = lane & 3;
    unsigned sel[4] = {li == 0 ? 0x0c0c0100u : (li == 1 ? 0x01000c0cu : 0x0c0c0c0cu), li == 2 ? 0x0c0c0100u : (li == 3 ? 0x01000c0cu : 0x0c0c0c0cu),
                       li == 0 ? 0x0c0c0302u : (li == 1 ? 0x03020c0cu : 0x0c0c0c0cu), li == 2 ? 0x0c0c0302u : (li == 3 ? 0x03020c0cu : 0x0c0c0c0cu)};
    f32x4 acc[8][2] = {};
    bf16x8 xf[8];
    s16x4 wr[4];
    u32x2 gq;
    u32x4 dg;
    u32x4 wa[2], wb[2];
    f32x4 dd[4];
    const f32x4 z4 = {0.f, 0.f, 0.f, 0.f};
#define RX(mt, ks) xf[mt] = *(const LAS bf16x8*)(offx[ks] + (mt) * 2048)
#define RW(i, ks) wr[i] = __builtin_amdgcn_ds_read_tr16_b64_v4i16((LAS s16x4*)(offw[i] + (ks) * 8192))
#define RG(o) gq = *(const LAS u32x2*)(offg + (o))
#define DG(i, G) dg[i] = __builtin_amdgcn_perm(G, G, sel[i])
#define SM(i) dd[i] = __builtin_amdgcn_mfma_f32_4x4x4bf16_1k(__builtin_bit_cast(s16x4, (u32x2){dg[((i) & 1) * 2], dg[((i) & 1) * 2 + 1]}), wr[i], z4, 0, 0, 0)
#define CV(W, e) W[(e) >> 2][(e) & 3] = cvt_pk(dd[(e) >> 1][((e) & 1) * 2], dd[(e) >> 1][((e) & 1) * 2 + 1])
#define MM(mt, c, W) acc[mt][c] = __builtin_amdgcn_mfma_f32_16x16x32_bf16(__builtin_bit_cast(bf16x8, W[c]), xf[mt], acc[mt][c], 0, 0, 0)
#define G2(mt, W, A, B) do { MM(mt, 0, W); SCHED(); A; SCHED(); MM(mt, 1, W); SCHED(); B; SCHED(); } while (0)
#define STEP(WC, WN, kx, kw, G, MID, E0, E1, E2) do { \
      G2(0, WC, (void)0, (RX(0, kx))); \
      SM(0); SM(1); SM(2); SM(3); SCHED(); \
      G2(1, WC, (DG(0, G), DG(1, G)), (RX(1, kx), DG(2, G))); \
      G2(2, WC, (CV(WN, 0), DG(3, G)), (RX(2, kx), CV(WN, 1))); \
      G2(3, WC, (CV(WN, 2), E0), (RX(3, kx), CV(WN, 3))); \
      G2(4, WC, (CV(WN, 4), E1), (RX(4, kx), CV(WN, 5))); \
      G2(5, WC, (CV(WN, 6), E2), (RX(5, kx), CV(WN, 7))); \
      MID; \
      G2(6, WC, (RW(0, kw), RW(1, kw)), (RX(6, kx))); \
      G2(7, WC, (RW(2, kw), RW(3, kw)), (RX(7, kx))); } while (0)
#pragma unroll
    for (int i = 0; i < 8; ++i) asm volatile("" : "+v"(acc[i][0]), "+v"(acc[i][1]));
    asm volatile("" : "+v"(offx[0]), "+v"(offx[1]), "+v"(offw[0]), "+v"(offw[1]), "+v"(offw[2]), "+v"(offw[3]), "+v"(offg),
                      "+v"(sel[0]), "+v"(sel[1]), "+v"(sel[2]), "+v"(sel[3]));
    __builtin_amdgcn_s_waitcnt(0xC07F);
    BARRIER_N();
    RG(0); RW(0, 0); RW(1, 0); RW(2, 0); RW(3, 0);
    RX(0, 0); RX(1, 0); RX(2, 0); RX(3, 0); RX(4, 0); RX(5, 0); RX(6, 0); RX(7, 0);
    DG(0, gq[0]); DG(1, gq[0]); DG(2, gq[0]); DG(3, gq[0]); SCHED();
    SM(0); SM(1); SM(2); SM(3); SCHED();
    RW(0, 1); RW(1, 1); RW(2, 1); RW(3, 1);
    DG(0, gq[1]); DG(1, gq[1]); DG(2, gq[1]); DG(3, gq[1]); SCHED();
    RG(8);
    CV(wa, 0); CV(wa, 1); CV(wa, 2); CV(wa, 3); CV(wa, 4); CV(wa, 5); CV(wa, 6); CV(wa, 7); SCHED();
#pragma unroll 1
    for (int s = 0; s < NST - 1; ++s) {
      const int dlt = ((s & (RING - 1)) == RING - 1) ? -(RING - 1) * SLOT : SLOT;
      STEP(wa, wb, 1, 0, gq[0], BARRIER_N()  , (offw[0] += dlt, offw[1] += dlt), (offw[2] += dlt, offw[3] += dlt), (offx[0] += dlt));
      STEP(wb, wa, 0, 1, gq[1], (void)0, (RG(16), offx[1] += dlt), (offg += 8u), (void)0);
    }
    f32x4 c1v[2], c2v[2];
    f32x2 rs[8];
    const unsigned tc = lds0 + LDS_TAB + 1024 + (unsigned)(wn * 32 + q4 * 4) * 4u, tr = lds0 + LDS_TAB + (unsigned)r * 8u;
#define RS(i) rs[i] = *(const LAS f32x2*)(tr + (i) * 128)
    G2(0, wa, (void)0, (RX(0, 1)));
    SM(0); SM(1); SM(2); SM(3); SCHED();
    G2(1, wa, (void)0, (RX(1, 1)));
    G2(2, wa, (CV(wb, 0)), (RX(2, 1), CV(wb, 1)));
    G2(3, wa, (CV(wb, 2)), (RX(3, 1), CV(wb, 3)));
    G2(4, wa, (CV(wb, 4)), (RX(4, 1), CV(wb, 5)));
    G2(5, wa, (CV(wb, 6)), (RX(5, 1), CV(wb, 7)));
    G2(6, wa, (void)0, (RX(6, 1)));
    G2(7, wa, (void)0, (RX(7, 1)));
    G2(0, wb, (c1v[0] = *(const LAS f32x4*)(tc)), (c1v[1] = *(const LAS f32x4*)(tc + 64)));
    G2(1, wb, (c2v[0] = *(const LAS f32x4*)(tc + 512)), (c2v[1] = *(const LAS f32x4*)(tc + 512 + 64)));
    G2(2, wb, (RS(0)), (RS(1))); G2(3, wb, (RS(2)), (RS(3))); G2(4, wb, (RS(4)), (RS(5))); G2(5, wb, (RS(6)), (RS(7)));
    G2(6, wb, (void)0, (void)0); G2(7, wb, (void)0, (void)0);
    const unsigned so = lds0 + (unsigned)r * 256u + (unsigned)(q4 & 1) * 8u;
    const unsigned sc0 = (unsigned)(((wn * 4 + (q4 >> 1)) ^ (r & 7)) * 16), sc1 = sc0 ^ 32u;
#pragma unroll
    for (int m2 = 0; m2 < 4; ++m2) {
      f32x4 tq[4];
#pragma unroll
      for (int i = 0; i < 4; ++i) tq[i] = acc[2 * m2 + (i >> 1)][i & 1] - c1v[i & 1] * rs[2 * m2 + (i >> 1)][0];
#pragma unroll
      for (int i = 0; i < 4; ++i) tq[i] = tq[i] * rs[2 * m2 + (i >> 1)][1] + c2v[i & 1];
#pragma unroll
      for (int i = 0; i < 4; ++i)
        *(LAS u32x2*)(so + (2 * m2 + (i >> 1)) * 4096 + ((i & 1) ? sc1 : sc0)) = (u32x2){cvt_pk(tq[i][0], tq[i][1]), cvt_pk(tq[i][2], tq[i][3])};
      SCHED();
      if (m2 == 1) BARRIER_L();
    }
  }
  if (wave >= 4) {
    BARRIER_N();
    store_rows<2>(lds0, (wave - 4) * 8, lane, (char*)out + (size_t)mi * 262144 + ni * 256);
  }
  BARRIER_L();
  if (wave < 8) store_rows<2>(lds0, 64 + wave * 8, lane, (char*)out + (size_t)mi * 262144 + ni * 256);
}

extern "C" void kernel_launch(void* const* d_in, const int* in_sizes, int n_in, void* d_out, int out_size, void* d_ws, size_t ws_size,
                              hipStream_t stream) {
  const u16* x = (const u16*)d_in[0]; const u16* g = (const u16*)d_in[1]; const u16* b = (const u16*)d_in[2]; const u16* w = (const u16*)d_in[3];
  (void)hipFuncSetAttribute((const void*)lnlin_kernel, hipFuncAttributeMaxDynamicSharedMemorySize, LDS_TOTAL);
  lnlin_kernel<<<256, 768, LDS_TOTAL, stream>>>(x, g, b, w, (u16*)d_out, (char*)d_ws);
}
```

```cpp
#include <hip/hip_runtime.h>
#include <hip/hip_bf16.h>
#include <utility>
#pragma clang diagnostic ignored "-Wint-to-pointer-cast"

typedef short bf16x8 __attribute__((ext_vector_type(8)));
typedef short s16x4 __attribute__((ext_vector_type(4)));
typedef float f32x4 __attribute__((ext_vector_type(4)));
typedef float f32x2 __attribute__((ext_vector_type(2)));
typedef unsigned u32x4 __attribute__((ext_vector_type(4)));
typedef unsigned u32x2 __attribute__((ext_vector_type(2)));
typedef __bf16 bf16x2_t __attribute__((ext_vector_type(2)));
typedef unsigned short u16;
typedef unsigned long long u64;
#define GAS __attribute__((address_space(1)))
#define LAS __attribute__((address_space(3)))

#define ST_FLAVOR "nt"

constexpr int KD = 1024, ND = 1024;
constexpr int NST = 16;
constexpr int RING = 4;
constexpr int SLOT = 32768;
constexpr int GROW = 136;
constexpr int LDS_GT = RING * SLOT;
constexpr int LDS_G = LDS_GT + 16 * GROW;
constexpr int LDS_B = LDS_G + 256;
constexpr int LDS_TAB = LDS_B + 256;
constexpr int LDS_TOTAL = LDS_TAB + 2048;

constexpr size_t WS_EPR = 0;
constexpr size_t WS_EPC = 32 * 128;
constexpr size_t WS_C = 8192;
constexpr size_t WS_RS = WS_C + 8 * 16384;


template <int B, int E, class F> __device__ __forceinline__ void static_for(F&& f) {
  if constexpr (B < E) { f(std::integral_constant<int, B>{}); static_for<B + 1, E>(f); }
}
__device__ __forceinline__ float bf_lo(unsigned u) { return __uint_as_float(u << 16); }
__device__ __forceinline__ float bf_hi(unsigned u) { return __uint_as_float(u & 0xffff0000u); }
__device__ __forceinline__ unsigned cvt_pk(float lo, float hi) {
  f32x2 v = {lo, hi}; return __builtin_bit_cast(unsigned, __builtin_convertvector(v, bf16x2_t));
}
template <int CTRL> __device__ __forceinline__ float dpp_add(float x) {
  return x + __builtin_bit_cast(float, __builtin_amdgcn_update_dpp(0, __builtin_bit_cast(int, x), CTRL, 0xf, 0xf, true));
}
__device__ __forceinline__ float sum16(float x) {
  x = dpp_add<0x128>(x); x = dpp_add<0x124>(x); x = dpp_add<0x122>(x); x = dpp_add<0x121>(x);
  return x;
}
__device__ __forceinline__ float add1(float a, float b) { float r; asm("v_add_f32 %0, %1, %2" : "=v"(r) : "v"(a), "v"(b)); return r; }
__device__ __forceinline__ float mul1(float a, float b) { float r; asm("v_mul_f32 %0, %1, %2" : "=v"(r) : "v"(a), "v"(b)); return r; }
__device__ __forceinline__ float fma1(float a, float b, float c) { float r; asm("v_fma_f32 %0, %1, %2, %3" : "=v"(r) : "v"(a), "v"(b), "v"(c)); return r; }
#define PIN8(a) asm volatile("" : "+v"(a[0]), "+v"(a[1]), "+v"(a[2]), "+v"(a[3]), "+v"(a[4]), "+v"(a[5]), "+v"(a[6]), "+v"(a[7]))
#define SCHED() __builtin_amdgcn_sched_barrier(0)
#define BARRIER_L() do { SCHED(); asm volatile("s_waitcnt lgkmcnt(0)\n\ts_barrier" ::: "memory"); SCHED(); } while (0)
#define BARRIER_N() do { SCHED(); asm volatile("s_barrier" ::: "memory"); SCHED(); } while (0)
template <int N> __device__ __forceinline__ void wait_vm() { asm volatile("s_waitcnt vmcnt(%0)" ::"n"(N) : "memory"); }

__device__ __forceinline__ void glds16x4(unsigned v0, unsigned v1, unsigned v2, unsigned v3, const void* sbase, unsigned lds_dst) {
  unsigned keep;
  asm volatile("s_mov_b32 %0, m0\n\ts_mov_b32 m0, %6\n\ts_nop 0\n\t"
               "global_load_lds_dwordx4 %1, %5\n\tglobal_load_lds_dwordx4 %2, %5 offset:1024\n\t"
               "global_load_lds_dwordx4 %3, %5 offset:2048\n\tglobal_load_lds_dwordx4 %4, %5 offset:3072\n\ts_mov_b32 m0, %0"
               : "=&s"(keep) : "v"(v0), "v"(v1), "v"(v2), "v"(v3), "s"(sbase), "s"(lds_dst) : "memory");
}
__device__ __forceinline__ void glds4(unsigned v0, const void* sbase, unsigned lds_dst) {
  unsigned keep;
  asm volatile("s_mov_b32 %0, m0\n\ts_mov_b32 m0, %3\n\ts_nop 0\n\tglobal_load_lds_dword %1, %2\n\ts_mov_b32 m0, %0"
               : "=&s"(keep) : "v"(v0), "s"(sbase), "s"(lds_dst) : "memory");
}
__device__ __forceinline__ void store_granule(char* ws, size_t off, unsigned tag, float v) {
  __hip_atomic_store((GAS u64*)(ws + off), ((u64)tag << 32) | __float_as_uint(v), __ATOMIC_RELAXED, __HIP_MEMORY_SCOPE_AGENT);
}
__device__ __forceinline__ unsigned load_word(const char* ws, size_t off) {
  return __hip_atomic_load((GAS unsigned*)(ws + off), __ATOMIC_RELAXED, __HIP_MEMORY_SCOPE_AGENT);
}

template <int N> __device__ __forceinline__ void store_rows(unsigned lds0, int rowb, int lane, char* ob) {
  const int row0 = rowb + (lane >> 4), ch = lane & 15;
  u32x4 v[N];
#pragma unroll
  for (int i = 0; i < N; ++i) { const int row = row0 + i * 4; v[i] = *(const LAS u32x4*)(lds0 + row * 256 + ((ch ^ (row & 7)) * 16)); }
  const unsigned oo = (unsigned)row0 * 2048u + (unsigned)ch * 16u;
#pragma unroll
  for (int i = 0; i < N; ++i)
    asm volatile("global_store_dwordx4 %0, %1, %2 " ST_FLAVOR "\n\ts_nop 1" :: "v"(oo + i * 8192u), "v"(v[i]), "s"(ob) : "memory");
}

__global__ __launch_bounds__(768, 3) void lnlin_kernel(const u16* __restrict__ x, const u16* __restrict__ g, const u16* __restrict__ b,
                                                       const u16* __restrict__ w, u16* __restrict__ out, char* ws) {
  extern __shared__ __attribute__((aligned(16))) char smem_g[];
  const int bid = blockIdx.x, xcd = bid & 7, idx = bid >> 3;
  const int mi = xcd * 4 + (idx >> 3), ni = idx & 7;
  const int slab = ((mi >> 2) + 2 * (ni & 3)) & 7, rot = slab * 2, sp = ni >> 2, rslab = (slab + sp) & 7;
  const int lane = threadIdx.x & 63;
  const int pwave = __builtin_amdgcn_readfirstlane(threadIdx.x >> 6);
  const int wave = pwave < 8 ? pwave ^ 4 : pwave;
  const int tid = wave * 64 + lane;
  const unsigned lds0 = (unsigned)(size_t)(LAS char*)smem_g;

  if (__builtin_expect(wave >= 4 && wave < 8, 1)) {
    const int d = wave - 4;
    unsigned xo[4], wo[4];
#pragma unroll
    for (int i = 0; i < 4; ++i) {
      const int row = i * 8 + (lane >> 3);
      xo[i] = (unsigned)row * 2048u + (unsigned)(((lane & 7) ^ ((row >> 1) & 7)) * 16) - (unsigned)i * 1024u;
      const int kr = i * 4 + (lane >> 4);
      wo[i] = (unsigned)kr * 2048u + (unsigned)(((lane & 15) ^ (((lane >> 4) << 2) | i)) * 16) - (unsigned)i * 1024u;
    }
    const unsigned dst = lds0 + (unsigned)d * 4096u;
    const char* xb = (const char*)x + (size_t)mi * 262144 + d * 65536;
    const char* wb = (const char*)w + d * 32768 + ni * 256;
    auto issue = [&](int st) {
      const unsigned ds_ = dst + (unsigned)(st & (RING - 1)) * SLOT;
      const int ks_ = (st + rot) & (NST - 1);
      glds16x4(wo[0], wo[1], wo[2], wo[3], wb + (size_t)ks_ * 131072, ds_ + 16384u);
      glds16x4(xo[0], xo[1], xo[2], xo[3], xb + ks_ * 128, ds_);
    };
    issue(0);
    if (d < 2) glds4((unsigned)lane * 4u, (const char*)(d ? g : b) + rot * 128, lds0 + (d ? LDS_G : LDS_B));
    issue(1);
    BARRIER_N(); BARRIER_N();
    wait_vm<8>();
    BARRIER_N();
#pragma unroll 1
    for (int j = 0; j < NST - 2; ++j) {
      issue(j + 2);
      wait_vm<8>();
      BARRIER_N();
    }
    wait_vm<0>();
    BARRIER_N();
  } else if (wave >= 8) {
    const int d = wave - 8, t = tid - 512;
    const int scol = (mi & 3) * 32 + d * 8;
    const unsigned cw = lds0 + 16384u + (unsigned)(256 * lane + 16 * ((scol >> 3) ^ (((lane & 3) << 2) | ((lane >> 2) & 3))));
    const int srow = t >> 1, l4 = lane & 15;
    const unsigned sx = lds0 + (unsigned)srow * 128u, ssw = (unsigned)(((t & 1) * 4) ^ ((srow >> 1) & 7));
    u32x4 xr[8];
    unsigned ep;
    float p1[8], p2[8], cs[4], part = 0.f, mean = 0.f, pub = 0.f, v0 = 0.f, v1 = 0.f;
    unsigned bad = 0u;
    u32x4 q[8];
    const unsigned gsrc = d < 2 ? (unsigned)WS_C + (unsigned)ni * 16384u + (unsigned)t * 16u : (unsigned)WS_RS + (unsigned)mi * 16384u + (unsigned)(t - 128) * 16u;
    auto gather = [&](int j0, int j1) __attribute__((always_inline)) {
#pragma unroll
      for (int j = j0; j < j1; ++j)
        q[j] = __builtin_amdgcn_raw_buffer_load_b128(__builtin_amdgcn_make_buffer_rsrc((void*)ws, 0, 0x100000, 0x00020000), gsrc + j * 2048u, 0, 16  );
    };
    auto combine = [&](unsigned tag) __attribute__((always_inline)) {
      bad = 0u; v0 = 0.f; v1 = 0.f;
#pragma unroll
      for (int j = 0; j < 8; ++j) { bad |= (q[j][1] ^ tag) | (q[j][3] ^ tag); v0 = add1(v0, __uint_as_float(q[j][0])); v1 = add1(v1, __uint_as_float(q[j][2])); }
      if (d >= 2) {
        v0 *= 0.125f;
        float dv = 0.f;
#pragma unroll
        for (int j = 0; j < 8; ++j) { const float e = __uint_as_float(q[j][0]) - v0; dv = fmaf(e, e, dv); }
        v1 = __builtin_amdgcn_rsqf(fmaf(dv, 128.f, v1) * (1.f / 1024.f) + 1e-5f);
      }
    };
    ep = load_word(ws, (lane & 1) ? WS_EPC + ni * 128 : WS_EPR + mi * 128);
    BARRIER_N(); BARRIER_N();
    BARRIER_N();
    unsigned epr = 0, epc = 0;
    static_for<0, NST - 1>([&](auto S) {
      constexpr int st = S;
      if constexpr (st <= 3) {
        if ((st >> 1) == sp) {
#pragma unroll
          for (int i = 0; i < 4; ++i) xr[(st & 1) * 4 + i] = *(const LAS u32x4*)(sx + st * SLOT + ((ssw ^ i) * 16u));
        }
      }
      if constexpr (st <= 1) {
        const u32x4 wv = *(const LAS u32x4*)(cw + st * SLOT);
        const float ga = bf_lo(*(const LAS u16*)(lds0 + LDS_G + st * 128 + lane * 2)), ba = bf_lo(*(const LAS u16*)(lds0 + LDS_B + st * 128 + lane * 2));
#pragma unroll
        for (int i = 0; i < 4; ++i) {
          const float l0 = bf_lo(wv[i]), h0 = bf_hi(wv[i]);
          const unsigned pa = cvt_pk(mul1(ga, l0), mul1(ga, h0));
          if constexpr (st == 0) { p1[2 * i] = bf_lo(pa); p1[2 * i + 1] = bf_hi(pa); p2[2 * i] = mul1(ba, l0); p2[2 * i + 1] = mul1(ba, h0); }
          else { p1[2 * i] = add1(p1[2 * i], bf_lo(pa)); p1[2 * i + 1] = add1(p1[2 * i + 1], bf_hi(pa)); p2[2 * i] = fma1(ba, l0, p2[2 * i]); p2[2 * i + 1] = fma1(ba, h0, p2[2 * i + 1]); }
        }
      }
      if constexpr (st == 2) {
        float a[8];
#pragma unroll
        for (int i = 0; i < 8; ++i) {
          auto rr = __builtin_amdgcn_permlane32_swap(__float_as_uint(p1[i]), __float_as_uint(p2[i]), false, false);
          a[i] = add1(__uint_as_float(rr[0]), __uint_as_float(rr[1]));
        }
        asm volatile("s_nop 1" : "+v"(a[0]), "+v"(a[1]), "+v"(a[2]), "+v"(a[3]), "+v"(a[4]), "+v"(a[5]), "+v"(a[6]), "+v"(a[7]));
#pragma unroll
        for (int i = 0; i < 4; ++i) {
          auto rr = __builtin_amdgcn_permlane16_swap(__float_as_uint(a[i]), __float_as_uint(a[4 + i]), false, false);
          cs[i] = add1(__uint_as_float(rr[0]), __uint_as_float(rr[1]));
        }
        asm volatile("" : "+v"(cs[0]), "+v"(cs[1]), "+v"(cs[2]), "+v"(cs[3]));
      }
      if constexpr (st == 3) {
#pragma unroll
        for (int i = 0; i < 4; ++i) cs[i] = sum16(cs[i]);
        pub = l4 == 0 ? cs[0] : (l4 == 1 ? cs[1] : (l4 == 2 ? cs[2] : cs[3]));
        epr = (unsigned)__builtin_amdgcn_readlane((int)ep, 0); epc = (unsigned)__builtin_amdgcn_readlane((int)ep, 1);
      }
      if constexpr (st <= 1) { PIN8(p1); PIN8(p2); }
      if constexpr (st == 3) asm volatile("" : "+v"(pub));
      if constexpr (st >= 4 && st <= 6) __builtin_amdgcn_s_setprio(3);
      if constexpr (st == 4) {
        f32x4 sa = {0.f, 0.f, 0.f, 0.f}, sb = sa;
        const s16x4 ones = {0x3f80, 0x3f80, 0x3f80, 0x3f80};
#pragma unroll
        for (int j = 0; j < 8; ++j) {
          sa = __builtin_amdgcn_mfma_f32_4x4x4bf16_1k(ones, __builtin_bit_cast(s16x4, (u32x2){xr[j][0], xr[j][1]}), sa, 0, 0, 0);
          sb = __builtin_amdgcn_mfma_f32_4x4x4bf16_1k(ones, __builtin_bit_cast(s16x4, (u32x2){xr[j][2], xr[j][3]}), sb, 0, 0, 0);
        }
        SCHED(); __builtin_amdgcn_s_setprio(0);
        mean = dpp_add<0xB1>(sa[0] + sb[0]) * (1.f / 128.f);
        asm volatile("" : "+v"(mean));
      }
      if constexpr (st == 5 || st == 6) {
        constexpr int j0 = (st - 5) * 4;
        const unsigned one = 0x3f80u << ((lane & 1) * 16);
        const s16x4 ident = __builtin_bit_cast(s16x4, (u32x2){(lane & 2) ? 0u : one, (lane & 2) ? one : 0u});
        const f32x4 nm = {-mean, -mean, -mean, -mean};
        f32x4 dv[8];
#pragma unroll
        for (int j = 0; j < 4; ++j) {
          dv[2 * j] = __builtin_amdgcn_mfma_f32_4x4x4bf16_1k(ident, __builtin_bit_cast(s16x4, (u32x2){xr[j0 + j][0], xr[j0 + j][1]}), nm, 0, 0, 0);
          dv[2 * j + 1] = __builtin_amdgcn_mfma_f32_4x4x4bf16_1k(ident, __builtin_bit_cast(s16x4, (u32x2){xr[j0 + j][2], xr[j0 + j][3]}), nm, 0, 0, 0);
        }
        SCHED(); __builtin_amdgcn_s_setprio(0);
        asm volatile("s_nop 7" : "+v"(dv[0]), "+v"(dv[1]), "+v"(dv[2]), "+v"(dv[3]), "+v"(dv[4]), "+v"(dv[5]), "+v"(dv[6]), "+v"(dv[7]));
        f32x4 s4 = {part, 0.f, 0.f, 0.f};
#pragma unroll
        for (int j = 0; j < 8; ++j)
#pragma unroll
          for (int e = 0; e < 4; ++e) s4[e] = fma1(dv[j][e], dv[j][e], s4[e]);
        part = add1(add1(s4[0], s4[1]), add1(s4[2], s4[3]));
        asm volatile("" : "+v"(part));
      }
      SCHED();
      if constexpr (st == 4) {
        if (l4 < 4) store_granule(ws, WS_C + ((size_t)((ni * 8 + slab) * 128 + scol + ((lane >> 4) & 1) * 4 + l4) * 2 + (lane >> 5)) * 8, epc + 1u, pub);
      }
      if constexpr (st == 7) {
        const float m2 = dpp_add<0xB1>(part);
        store_granule(ws, WS_RS + ((size_t)(mi * 8 + rslab) * 256 + t) * 8, epr + 1u, (t & 1) ? m2 : mean);
      }
      if constexpr (st == 8) { if (d < 2) gather(0, 4); }
      if constexpr (st == 9) { if (d < 2) gather(4, 8); }
      if constexpr (st == 10) { if (d >= 2) gather(0, 4); }
      if constexpr (st == 11) { if (d >= 2) gather(4, 8); }
      if constexpr (st == 13) { if (d >= 2) { combine(epr + 1u); asm volatile("" : "+v"(v0), "+v"(v1), "+v"(bad)); } }
      if constexpr (st == 11) { if (d < 2) { combine(epc + 1u); asm volatile("" : "+v"(v0), "+v"(v1), "+v"(bad)); } }
      if constexpr (st == 14) {
        const unsigned tag = (d < 2 ? epc : epr) + 1u;
        bool ok = bad == 0u;
        if (__builtin_expect(!__all(ok), 0)) {
#pragma unroll 1
          for (int it = 0; it < 20000; ++it) {
            asm volatile("" ::: "memory");
            gather(0, 8);
            combine(tag);
            ok = bad == 0u;
            if (__all(ok)) break;
            __builtin_amdgcn_s_sleep(2);
          }
        }
        if (__builtin_expect(!__all(ok), 0)) {
          v0 = 0.f; v1 = 0.f;
          if (d < 2) {
            const u16* wc = w + ni * 128 + t;
#pragma unroll 1
            for (int k = 0; k < KD; ++k) {
              const float wv = bf_lo(wc[(size_t)k * ND]);
              v0 += bf_lo(cvt_pk(bf_lo(g[k]) * wv, 0.f)); v1 = fmaf(bf_lo(b[k]), wv, v1);
            }
          } else {
            const u16* xrow = x + (size_t)(mi * 128 + (t - 128)) * KD;
#pragma unroll 1
            for (int k = 0; k < KD; ++k) v0 += bf_lo(xrow[k]);
            v0 *= (1.f / 1024.f);
#pragma unroll 1
            for (int k = 0; k < KD; ++k) { const float dd = bf_lo(xrow[k]) - v0; v1 = fmaf(dd, dd, v1); }
            v1 = __builtin_amdgcn_rsqf(v1 * (1.f / 1024.f) + 1e-5f);
          }
        }
        if (d < 2) { *(LAS float*)(lds0 + LDS_TAB + 1024 + t * 4) = v0; *(LAS float*)(lds0 + LDS_TAB + 1536 + t * 4) = v1; }
        else *(LAS f32x2*)(lds0 + LDS_TAB + (t - 128) * 8) = (f32x2){v0, v1};
      }
      if constexpr (st <= 3 || st == 14) BARRIER_L(); else BARRIER_N();
    });
    if (t == 0) {
      __hip_atomic_fetch_add((GAS unsigned*)(ws + WS_EPR + mi * 128), 1u, __ATOMIC_RELAXED, __HIP_MEMORY_SCOPE_AGENT);
      __hip_atomic_fetch_add((GAS unsigned*)(ws + WS_EPC + ni * 128), 1u, __ATOMIC_RELAXED, __HIP_MEMORY_SCOPE_AGENT);
    }
  } else {
    const int wn = wave;
    const u32x2 g4 = *(const GAS u32x2*)((const char*)g + ((tid * 8 + rot * 128) & 2047));
    const unsigned gta = lds0 + LDS_GT + (unsigned)(((tid >> 1) & 3) * 4 * GROW + (tid >> 3) * 4 + (tid & 1) * 2);
    const int r = lane & 15, q4 = lane >> 4, sw = (r >> 1) & 7;
    unsigned offx[2], offw[4];
#pragma unroll
    for (int ks = 0; ks < 2; ++ks) offx[ks] = lds0 + (unsigned)r * 128u + (unsigned)(((4 * ks + q4) ^ sw) * 16);
    {
      const int q = (lane & 15) >> 2, p = lane & 3;
#pragma unroll
      for (int i = 0; i < 4; ++i) {
        const int tt = i & 1, c = i >> 1, row = 8 * q4 + 4 * tt + q, ch = 4 * wn + 2 * c + (p >> 1);
        offw[i] = lds0 + 16384u + (unsigned)(256 * row + 16 * (ch ^ ((q << 2) | ((2 * q4 + tt) & 3))) + 8 * (p & 1));
      }
    }
    unsigned offg = lds0 + LDS_GT + (unsigned)((q4 * 4 + (lane & 3)) * GROW);
    const unsigned li = lane & 3;
    unsigned sel[4] = {li == 0 ? 0x0c0c0100u : (li == 1 ? 0x01000c0cu : 0x0c0c0c0cu), li == 2 ? 0x0c0c0100u : (li == 3 ? 0x01000c0cu : 0x0c0c0c0cu),
                       li == 0 ? 0x0c0c0302u : (li == 1 ? 0x03020c0cu : 0x0c0c0c0cu), li == 2 ? 0x0c0c0302u : (li == 3 ? 0x03020c0cu : 0x0c0c0c0cu)};
    f32x4 acc[8][2] = {};
    bf16x8 xf[8];
    s16x4 wr[4];
    u32x2 gq;
    u32x4 dg;
    u32x4 wa[2], wb[2];
    f32x4 dd[4];
    const f32x4 z4 = {0.f, 0.f, 0.f, 0.f};
#define RX(mt, ks) xf[mt] = *(const LAS bf16x8*)(offx[ks] + (mt) * 2048)
#define RW(i, ks) wr[i] = __builtin_amdgcn_ds_read_tr16_b64_v4i16((LAS s16x4*)(offw[i] + (ks) * 8192))
#define RG(o) gq = *(const LAS u32x2*)(offg + (o))
#define DG(i, G) dg[i] = __builtin_amdgcn_perm(G, G, sel[i])
#define SM(i) dd[i] = __builtin_amdgcn_mfma_f32_4x4x4bf16_1k(__builtin_bit_cast(s16x4, (u32x2){dg[((i) & 1) * 2], dg[((i) & 1) * 2 + 1]}), wr[i], z4, 0, 0, 0)
#define CV(W, e) W[(e) >> 2][(e) & 3] = cvt_pk(dd[(e) >> 1][((e) & 1) * 2], dd[(e) >> 1][((e) & 1) * 2 + 1])
#define MM(mt, c, W) acc[mt][c] = __builtin_amdgcn_mfma_f32_16x16x32_bf16(__builtin_bit_cast(bf16x8, W[c]), xf[mt], acc[mt][c], 0, 0, 0)
#define G2(mt, W, A, B) do { MM(mt, 0, W); SCHED(); A; SCHED(); MM(mt, 1, W); SCHED(); B; SCHED(); } while (0)
#define STEP(WC, WN, kx, kw, G, MID, E0, E1, E2) do { \
      G2(0, WC, (void)0, (RX(0, kx))); \
      SM(0); SM(1); SM(2); SM(3); SCHED(); \
      G2(1, WC, (DG(0, G), DG(1, G)), (RX(1, kx), DG(2, G))); \
      G2(2, WC, (CV(WN, 0), DG(3, G)), (RX(2, kx), CV(WN, 1))); \
      G2(3, WC, (CV(WN, 2), E0), (RX(3, kx), CV(WN, 3))); \
      G2(4, WC, (CV(WN, 4), E1), (RX(4, kx), CV(WN, 5))); \
      G2(5, WC, (CV(WN, 6), E2), (RX(5, kx), CV(WN, 7))); \
      MID; \
      G2(6, WC, (RW(0, kw), RW(1, kw)), (RX(6, kx))); \
      G2(7, WC, (RW(2, kw), RW(3, kw)), (RX(7, kx))); } while (0)
    const unsigned ox0 = offx[0], ox1 = offx[1], ow0 = offw[0], ow1 = offw[1], ow2 = offw[2], ow3 = offw[3], og = offg;
    __builtin_amdgcn_s_waitcnt(0xC07F);
#pragma unroll 1
    for (int pass = 0; pass < 2; ++pass) {
      offx[0] = ox0; offx[1] = ox1; offw[0] = ow0; offw[1] = ow1; offw[2] = ow2; offw[3] = ow3; offg = og;
      if (pass) {
        *(LAS u16*)(gta) = (u16)g4[0]; *(LAS u16*)(gta + GROW) = (u16)(g4[0] >> 16);
        *(LAS u16*)(gta + 2 * GROW) = (u16)g4[1]; *(LAS u16*)(gta + 3 * GROW) = (u16)(g4[1] >> 16);
      }
#pragma unroll
      for (int i = 0; i < 8; ++i) { acc[i][0] = z4; acc[i][1] = z4; asm volatile("" : "+v"(acc[i][0]), "+v"(acc[i][1])); }
      asm volatile("" : "+v"(offx[0]), "+v"(offx[1]), "+v"(offw[0]), "+v"(offw[1]), "+v"(offw[2]), "+v"(offw[3]), "+v"(offg),
                        "+v"(sel[0]), "+v"(sel[1]), "+v"(sel[2]), "+v"(sel[3]));
      __builtin_amdgcn_s_waitcnt(0xC07F);
      BARRIER_N();
      RG(0); RW(0, 0); RW(1, 0); RW(2, 0); RW(3, 0);
      RX(0, 0); RX(1, 0); RX(2, 0); RX(3, 0); RX(4, 0); RX(5, 0); RX(6, 0); RX(7, 0);
      DG(0, gq[0]); DG(1, gq[0]); DG(2, gq[0]); DG(3, gq[0]); SCHED();
      SM(0); SM(1); SM(2); SM(3); SCHED();
      RW(0, 1); RW(1, 1); RW(2, 1); RW(3, 1);
      DG(0, gq[1]); DG(1, gq[1]); DG(2, gq[1]); DG(3, gq[1]); SCHED();
      RG(8);
      CV(wa, 0); CV(wa, 1); CV(wa, 2); CV(wa, 3); CV(wa, 4); CV(wa, 5); CV(wa, 6); CV(wa, 7); SCHED();
      const int nit = pass ? NST - 1 : 1;
#pragma unroll 1
      for (int s = 0; s < nit; ++s) {
        const int dlt = ((s & (RING - 1)) == RING - 1) ? -(RING - 1) * SLOT : SLOT;
        STEP(wa, wb, 1, 0, gq[0], BARRIER_N()  , (offw[0] += dlt, offw[1] += dlt), (offw[2] += dlt, offw[3] += dlt), (offx[0] += dlt));
        STEP(wb, wa, 0, 1, gq[1], (void)0, (RG(16), offx[1] += dlt), (offg += 8u), (void)0);
      }
    }
    f32x4 c1v[2], c2v[2];
    f32x2 rs[8];
    const unsigned tc = lds0 + LDS_TAB + 1024 + (unsigned)(wn * 32 + q4 * 4) * 4u, tr = lds0 + LDS_TAB + (unsigned)r * 8u;
#define RS(i) rs[i] = *(const LAS f32x2*)(tr + (i) * 128)
    G2(0, wa, (void)0, (RX(0, 1)));
    SM(0); SM(1); SM(2); SM(3); SCHED();
    G2(1, wa, (void)0, (RX(1, 1)));
    G2(2, wa, (CV(wb, 0)), (RX(2, 1), CV(wb, 1)));
    G2(3, wa, (CV(wb, 2)), (RX(3, 1), CV(wb, 3)));
    G2(4, wa, (CV(wb, 4)), (RX(4, 1), CV(wb, 5)));
    G2(5, wa, (CV(wb, 6)), (RX(5, 1), CV(wb, 7)));
    G2(6, wa, (void)0, (RX(6, 1)));
    G2(7, wa, (void)0, (RX(7, 1)));
    G2(0, wb, (c1v[0] = *(const LAS f32x4*)(tc)), (c1v[1] = *(const LAS f32x4*)(tc + 64)));
    G2(1, wb, (c2v[0] = *(const LAS f32x4*)(tc + 512)), (c2v[1] = *(const LAS f32x4*)(tc + 512 + 64)));
    G2(2, wb, (RS(0)), (RS(1))); G2(3, wb, (RS(2)), (RS(3))); G2(4, wb, (RS(4)), (RS(5))); G2(5, wb, (RS(6)), (RS(7)));
    G2(6, wb, (void)0, (void)0); G2(7, wb, (void)0, (void)0);
    const unsigned so = lds0 + (unsigned)r * 256u + (unsigned)(q4 & 1) * 8u;
    const unsigned sc0 = (unsigned)(((wn * 4 + (q4 >> 1)) ^ (r & 7)) * 16), sc1 = sc0 ^ 32u;
#pragma unroll
    for (int m2 = 0; m2 < 4; ++m2) {
      f32x4 tq[4];
#pragma unroll
      for (int i = 0; i < 4; ++i) tq[i] = acc[2 * m2 + (i >> 1)][i & 1] - c1v[i & 1] * rs[2 * m2 + (i >> 1)][0];
#pragma unroll
      for (int i = 0; i < 4; ++i) tq[i] = tq[i] * rs[2 * m2 + (i >> 1)][1] + c2v[i & 1];
#pragma unroll
      for (int i = 0; i < 4; ++i)
        *(LAS u32x2*)(so + (2 * m2 + (i >> 1)) * 4096 + ((i & 1) ? sc1 : sc0)) = (u32x2){cvt_pk(tq[i][0], tq[i][1]), cvt_pk(tq[i][2], tq[i][3])};
      SCHED();
      if (m2 == 1) BARRIER_L();
    }
  }
  if (wave >= 4) {
    BARRIER_N();
    store_rows<2>(lds0, (wave - 4) * 8, lane, (char*)out + (size_t)mi * 262144 + ni * 256);
  }
  BARRIER_L();
  if (wave < 8) store_rows<2>(lds0, 64 + wave * 8, lane, (char*)out + (size_t)mi * 262144 + ni * 256);
}

extern "C" void kernel_launch(void* const* d_in, const int* in_sizes, int n_in, void* d_out, int out_size, void* d_ws, size_t ws_size,
                              hipStream_t stream) {
  const u16* x = (const u16*)d_in[0]; const u16* g = (const u16*)d_in[1]; const u16* b = (const u16*)d_in[2]; const u16* w = (const u16*)d_in[3];
  (void)hipFuncSetAttribute((const void*)lnlin_kernel, hipFuncAttributeMaxDynamicSharedMemorySize, LDS_TOTAL);
  lnlin_kernel<<<256, 768, LDS_TOTAL, stream>>>(x, g, b, w, (u16*)d_out, (char*)d_ws);
}
```

```cpp
#include <hip/hip_runtime.h>
#include <hip/hip_bf16.h>
#include <utility>
#pragma clang diagnostic ignored "-Wint-to-pointer-cast"

typedef short bf16x8 __attribute__((ext_vector_type(8)));
typedef short s16x4 __attribute__((ext_vector_type(4)));
typedef float f32x4 __attribute__((ext_vector_type(4)));
typedef float f32x2 __attribute__((ext_vector_type(2)));
typedef unsigned u32x4 __attribute__((ext_vector_type(4)));
typedef unsigned u32x2 __attribute__((ext_vector_type(2)));
typedef __bf16 bf16x2_t __attribute__((ext_vector_type(2)));
typedef unsigned short u16;
typedef unsigned long long u64;
#define GAS __attribute__((address_space(1)))
#define LAS __attribute__((address_space(3)))

#define ST_FLAVOR "nt"

constexpr int KD = 1024, ND = 1024;
constexpr int NST = 16;
constexpr int RING = 4;
constexpr int SLOT = 32768;
constexpr int GROW = 136;
constexpr int LDS_GT = RING * SLOT;
constexpr int LDS_G = LDS_GT + 16 * GROW;
constexpr int LDS_B = LDS_G + 256;
constexpr int LDS_TAB = LDS_B + 256;
constexpr int LDS_TOTAL = LDS_TAB + 2048;

constexpr size_t WS_EPR = 0;
constexpr size_t WS_EPC = 32 * 128;
constexpr size_t WS_C = 8192;
constexpr size_t WS_RS = WS_C + 8 * 16384;


template <int B, int E, class F> __device__ __forceinline__ void static_for(F&& f) {
  if constexpr (B < E) { f(std::integral_constant<int, B>{}); static_for<B + 1, E>(f); }
}
__device__ __forceinline__ float bf_lo(unsigned u) { return __uint_as_float(u << 16); }
__device__ __forceinline__ float bf_hi(unsigned u) { return __uint_as_float(u & 0xffff0000u); }
__device__ __forceinline__ unsigned cvt_pk(float lo, float hi) {
  f32x2 v = {lo, hi}; return __builtin_bit_cast(unsigned, __builtin_convertvector(v, bf16x2_t));
}
template <int CTRL> __device__ __forceinline__ float dpp_add(float x) {
  return x + __builtin_bit_cast(float, __builtin_amdgcn_update_dpp(0, __builtin_bit_cast(int, x), CTRL, 0xf, 0xf, true));
}
__device__ __forceinline__ float sum16(float x) {
  x = dpp_add<0x128>(x); x = dpp_add<0x124>(x); x = dpp_add<0x122>(x); x = dpp_add<0x121>(x);
  return x;
}
__device__ __forceinline__ float add1(float a, float b) { float r; asm("v_add_f32 %0, %1, %2" : "=v"(r) : "v"(a), "v"(b)); return r; }
__device__ __forceinline__ float mul1(float a, float b) { float r; asm("v_mul_f32 %0, %1, %2" : "=v"(r) : "v"(a), "v"(b)); return r; }
__device__ __forceinline__ float fma1(float a, float b, float c) { float r; asm("v_fma_f32 %0, %1, %2, %3" : "=v"(r) : "v"(a), "v"(b), "v"(c)); return r; }
#define PIN8(a) asm volatile("" : "+v"(a[0]), "+v"(a[1]), "+v"(a[2]), "+v"(a[3]), "+v"(a[4]), "+v"(a[5]), "+v"(a[6]), "+v"(a[7]))
#define SCHED() __builtin_amdgcn_sched_barrier(0)
#define BARRIER_L() do { SCHED(); asm volatile("s_waitcnt lgkmcnt(0)\n\ts_barrier" ::: "memory"); SCHED(); } while (0)
#define BARRIER_N() do { SCHED(); asm volatile("s_barrier" ::: "memory"); SCHED(); } while (0)
template <int N> __device__ __forceinline__ void wait_vm() { asm volatile("s_waitcnt vmcnt(%0)" ::"n"(N) : "memory"); }

__device__ __forceinline__ void glds16x4(unsigned v0, unsigned v1, unsigned v2, unsigned v3, const void* sbase, unsigned lds_dst) {
  unsigned keep;
  asm volatile("s_mov_b32 %0, m0\n\ts_mov_b32 m0, %6\n\ts_nop 0\n\t"
               "global_load_lds_dwordx4 %1, %5\n\tglobal_load_lds_dwordx4 %2, %5 offset:1024\n\t"
               "global_load_lds_dwordx4 %3, %5 offset:2048\n\tglobal_load_lds_dwordx4 %4, %5 offset:3072\n\ts_mov_b32 m0, %0"
               : "=&s"(keep) : "v"(v0), "v"(v1), "v"(v2), "v"(v3), "s"(sbase), "s"(lds_dst) : "memory");
}
__device__ __forceinline__ void glds4(unsigned v0, const void* sbase, unsigned lds_dst) {
  unsigned keep;
  asm volatile("s_mov_b32 %0, m0\n\ts_mov_b32 m0, %3\n\ts_nop 0\n\tglobal_load_lds_dword %1, %2\n\ts_mov_b32 m0, %0"
               : "=&s"(keep) : "v"(v0), "s"(sbase), "s"(lds_dst) : "memory");
}
__device__ __forceinline__ void store_granule(char* ws, size_t off, unsigned tag, float v) {
  __hip_atomic_store((GAS u64*)(ws + off), ((u64)tag << 32) | __float_as_uint(v), __ATOMIC_RELAXED, __HIP_MEMORY_SCOPE_AGENT);
}
__device__ __forceinline__ unsigned load_word(const char* ws, size_t off) {
  return __hip_atomic_load((GAS unsigned*)(ws + off), __ATOMIC_RELAXED, __HIP_MEMORY_SCOPE_AGENT);
}

template <int N> __device__ __forceinline__ void store_rows(unsigned lds0, int rowb, int lane, char* ob) {
  const int row0 = rowb + (lane >> 4), ch = lane & 15;
  u32x4 v[N];
#pragma unroll
  for (int i = 0; i < N; ++i) { const int row = row0 + i * 4; v[i] = *(const LAS u32x4*)(lds0 + row * 256 + ((ch ^ (row & 7)) * 16)); }
  const unsigned oo = (unsigned)row0 * 2048u + (unsigned)ch * 16u;
#pragma unroll
  for (int i = 0; i < N; ++i)
    asm volatile("global_store_dwordx4 %0, %1, %2 " ST_FLAVOR "\n\ts_nop 1" :: "v"(oo + i * 8192u), "v"(v[i]), "s"(ob) : "memory");
}

__global__ __launch_bounds__(768, 3) void lnlin_kernel(const u16* __restrict__ x, const u16* __restrict__ g, const u16* __restrict__ b,
                                                       const u16* __restrict__ w, u16* __restrict__ out, char* ws) {
  extern __shared__ __attribute__((aligned(16))) char smem_g[];
  const int bid = blockIdx.x, xcd = bid & 7, idx = bid >> 3;
  const int mi = xcd * 4 + (idx >> 3), ni = idx & 7;
  const int slab = ((mi >> 2) + 2 * (ni & 3)) & 7, rot = slab * 2, sp = ni >> 2, rslab = (slab + sp) & 7;
  const int lane = threadIdx.x & 63;
  const int pwave = __builtin_amdgcn_readfirstlane(threadIdx.x >> 6);
  const int wave = pwave < 8 ? pwave ^ 4 : pwave;
  const int tid = wave * 64 + lane;
  const unsigned lds0 = (unsigned)(size_t)(LAS char*)smem_g;

  if (__builtin_expect(wave >= 4 && wave < 8, 1)) {
    const int d = wave - 4;
    unsigned xo[4], wo[4];
#pragma unroll
    for (int i = 0; i < 4; ++i) {
      const int row = i * 8 + (lane >> 3);
      xo[i] = (unsigned)row * 2048u + (unsigned)(((lane & 7) ^ ((row >> 1) & 7)) * 16) - (unsigned)i * 1024u;
      const int kr = i * 4 + (lane >> 4);
      wo[i] = (unsigned)kr * 2048u + (unsigned)(((lane & 15) ^ (((lane >> 4) << 2) | i)) * 16) - (unsigned)i * 1024u;
    }
    const unsigned dst = lds0 + (unsigned)d * 4096u;
    const char* xb = (const char*)x + (size_t)mi * 262144 + d * 65536;
    const char* wb = (const char*)w + d * 32768 + ni * 256;
    auto issue = [&](int st) {
      const unsigned ds_ = dst + (unsigned)(st & (RING - 1)) * SLOT;
      const int ks_ = (st + rot) & (NST - 1);
      glds16x4(wo[0], wo[1], wo[2], wo[3], wb + (size_t)ks_ * 131072, ds_ + 16384u);
      glds16x4(xo[0], xo[1], xo[2], xo[3], xb + ks_ * 128, ds_);
    };
    issue(0);
    if (d < 2) glds4((unsigned)lane * 4u, (const char*)(d ? g : b) + rot * 128, lds0 + (d ? LDS_G : LDS_B));
    issue(1);
    wait_vm<8>();
    BARRIER_N();
#pragma unroll 1
    for (int j = 0; j < NST - 2; ++j) {
      issue(j + 2);
      wait_vm<8>();
      BARRIER_N();
    }
    wait_vm<0>();
    BARRIER_N();
  } else if (wave >= 8) {
    const int d = wave - 8, t = tid - 512;
    const int scol = (mi & 3) * 32 + d * 8;
    const unsigned cw = lds0 + 16384u + (unsigned)(256 * lane + 16 * ((scol >> 3) ^ (((lane & 3) << 2) | ((lane >> 2) & 3))));
    const int srow = t >> 1, l4 = lane & 15;
    const unsigned sx = lds0 + (unsigned)srow * 128u, ssw = (unsigned)(((t & 1) * 4) ^ ((srow >> 1) & 7));
    u32x4 xr[8];
    unsigned ep;
    float p1[8], p2[8], cs[4], part = 0.f, mean = 0.f, pub = 0.f, v0 = 0.f, v1 = 0.f;
    unsigned bad = 0u;
    u32x4 q[8];
    const unsigned gsrc = d < 2 ? (unsigned)WS_C + (unsigned)ni * 16384u + (unsigned)t * 16u : (unsigned)WS_RS + (unsigned)mi * 16384u + (unsigned)(t - 128) * 16u;
    auto gather = [&](int j0, int j1) __attribute__((always_inline)) {
#pragma unroll
      for (int j = j0; j < j1; ++j)
        q[j] = __builtin_amdgcn_raw_buffer_load_b128(__builtin_amdgcn_make_buffer_rsrc((void*)ws, 0, 0x100000, 0x00020000), gsrc + j * 2048u, 0, 16  );
    };
    auto combine = [&](unsigned tag) __attribute__((always_inline)) {
      bad = 0u; v0 = 0.f; v1 = 0.f;
#pragma unroll
      for (int j = 0; j < 8; ++j) { bad |= (q[j][1] ^ tag) | (q[j][3] ^ tag); v0 = add1(v0, __uint_as_float(q[j][0])); v1 = add1(v1, __uint_as_float(q[j][2])); }
      if (d >= 2) {
        v0 *= 0.125f;
        float dv = 0.f;
#pragma unroll
        for (int j = 0; j < 8; ++j) { const float e = __uint_as_float(q[j][0]) - v0; dv = fmaf(e, e, dv); }
        v1 = __builtin_amdgcn_rsqf(fmaf(dv, 128.f, v1) * (1.f / 1024.f) + 1e-5f);
      }
    };
    ep = load_word(ws, (lane & 1) ? WS_EPC + ni * 128 : WS_EPR + mi * 128);
    BARRIER_N();
    unsigned epr = 0, epc = 0;
    static_for<0, NST - 1>([&](auto S) {
      constexpr int st = S;
      if constexpr (st <= 3) {
        if ((st >> 1) == sp) {
#pragma unroll
          for (int i = 0; i < 4; ++i) xr[(st & 1) * 4 + i] = *(const LAS u32x4*)(sx + st * SLOT + ((ssw ^ i) * 16u));
        }
      }
      if constexpr (st <= 1) {
        const u32x4 wv = *(const LAS u32x4*)(cw + st * SLOT);
        const float ga = bf_lo(*(const LAS u16*)(lds0 + LDS_G + st * 128 + lane * 2)), ba = bf_lo(*(const LAS u16*)(lds0 + LDS_B + st * 128 + lane * 2));
#pragma unroll
        for (int i = 0; i < 4; ++i) {
          const float l0 = bf_lo(wv[i]), h0 = bf_hi(wv[i]);
          const unsigned pa = cvt_pk(mul1(ga, l0), mul1(ga, h0));
          if constexpr (st == 0) { p1[2 * i] = bf_lo(pa); p1[2 * i + 1] = bf_hi(pa); p2[2 * i] = mul1(ba, l0); p2[2 * i + 1] = mul1(ba, h0); }
          else { p1[2 * i] = add1(p1[2 * i], bf_lo(pa)); p1[2 * i + 1] = add1(p1[2 * i + 1], bf_hi(pa)); p2[2 * i] = fma1(ba, l0, p2[2 * i]); p2[2 * i + 1] = fma1(ba, h0, p2[2 * i + 1]); }
        }
      }
      if constexpr (st == 2) {
        float a[8];
#pragma unroll
        for (int i = 0; i < 8; ++i) {
          auto rr = __builtin_amdgcn_permlane32_swap(__float_as_uint(p1[i]), __float_as_uint(p2[i]), false, false);
          a[i] = add1(__uint_as_float(rr[0]), __uint_as_float(rr[1]));
        }
        asm volatile("s_nop 1" : "+v"(a[0]), "+v"(a[1]), "+v"(a[2]), "+v"(a[3]), "+v"(a[4]), "+v"(a[5]), "+v"(a[6]), "+v"(a[7]));
#pragma unroll
        for (int i = 0; i < 4; ++i) {
          auto rr = __builtin_amdgcn_permlane16_swap(__float_as_uint(a[i]), __float_as_uint(a[4 + i]), false, false);
          cs[i] = add1(__uint_as_float(rr[0]), __uint_as_float(rr[1]));
        }
        asm volatile("" : "+v"(cs[0]), "+v"(cs[1]), "+v"(cs[2]), "+v"(cs[3]));
      }
      if constexpr (st == 3) {
#pragma unroll
        for (int i = 0; i < 4; ++i) cs[i] = sum16(cs[i]);
        pub = l4 == 0 ? cs[0] : (l4 == 1 ? cs[1] : (l4 == 2 ? cs[2] : cs[3]));
        epr = (unsigned)__builtin_amdgcn_readlane((int)ep, 0); epc = (unsigned)__builtin_amdgcn_readlane((int)ep, 1);
      }
      if constexpr (st <= 1) { PIN8(p1); PIN8(p2); }
      if constexpr (st == 3) asm volatile("" : "+v"(pub));
      if constexpr (st >= 4 && st <= 6) __builtin_amdgcn_s_setprio(3);
      if constexpr (st == 4) {
        f32x4 sa = {0.f, 0.f, 0.f, 0.f}, sb = sa;
        const s16x4 ones = {0x3f80, 0x3f80, 0x3f80, 0x3f80};
#pragma unroll
        for (int j = 0; j < 8; ++j) {
          sa = __builtin_amdgcn_mfma_f32_4x4x4bf16_1k(ones, __builtin_bit_cast(s16x4, (u32x2){xr[j][0], xr[j][1]}), sa, 0, 0, 0);
          sb = __builtin_amdgcn_mfma_f32_4x4x4bf16_1k(ones, __builtin_bit_cast(s16x4, (u32x2){xr[j][2], xr[j][3]}), sb, 0, 0, 0);
        }
        SCHED(); __builtin_amdgcn_s_setprio(0);
        mean = dpp_add<0xB1>(sa[0] + sb[0]) * (1.f / 128.f);
        asm volatile("" : "+v"(mean));
      }
      if constexpr (st == 5 || st == 6) {
        constexpr int j0 = (st - 5) * 4;
        const unsigned one = 0x3f80u << ((lane & 1) * 16);
        const s16x4 ident = __builtin_bit_cast(s16x4, (u32x2){(lane & 2) ? 0u : one, (lane & 2) ? one : 0u});
        const f32x4 nm = {-mean, -mean, -mean, -mean};
        f32x4 dv[8];
#pragma unroll
        for (int j = 0; j < 4; ++j) {
          dv[2 * j] = __builtin_amdgcn_mfma_f32_4x4x4bf16_1k(ident, __builtin_bit_cast(s16x4, (u32x2){xr[j0 + j][0], xr[j0 + j][1]}), nm, 0, 0, 0);
          dv[2 * j + 1] = __builtin_amdgcn_mfma_f32_4x4x4bf16_1k(ident, __builtin_bit_cast(s16x4, (u32x2){xr[j0 + j][2], xr[j0 + j][3]}), nm, 0, 0, 0);
        }
        SCHED(); __builtin_amdgcn_s_setprio(0);
        asm volatile("s_nop 7" : "+v"(dv[0]), "+v"(dv[1]), "+v"(dv[2]), "+v"(dv[3]), "+v"(dv[4]), "+v"(dv[5]), "+v"(dv[6]), "+v"(dv[7]));
        f32x4 s4 = {part, 0.f, 0.f, 0.f};
#pragma unroll
        for (int j = 0; j < 8; ++j)
#pragma unroll
          for (int e = 0; e < 4; ++e) s4[e] = fma1(dv[j][e], dv[j][e], s4[e]);
        part = add1(add1(s4[0], s4[1]), add1(s4[2], s4[3]));
        asm volatile("" : "+v"(part));
      }
      SCHED();
      if constexpr (st == 4) {
        if (l4 < 4) store_granule(ws, WS_C + ((size_t)((ni * 8 + slab) * 128 + scol + ((lane >> 4) & 1) * 4 + l4) * 2 + (lane >> 5)) * 8, epc + 1u, pub);
      }
      if constexpr (st == 7) {
        const float m2 = dpp_add<0xB1>(part);
        store_granule(ws, WS_RS + ((size_t)(mi * 8 + rslab) * 256 + t) * 8, epr + 1u, (t & 1) ? m2 : mean);
      }
      if constexpr (st == 8) { if (d < 2) gather(0, 4); }
      if constexpr (st == 9) { if (d < 2) gather(4, 8); }
      if constexpr (st == 10) { if (d >= 2) gather(0, 4); }
      if constexpr (st == 11) { if (d >= 2) gather(4, 8); }
      if constexpr (st == 13) { if (d >= 2) { combine(epr + 1u); asm volatile("" : "+v"(v0), "+v"(v1), "+v"(bad)); } }
      if constexpr (st == 11) { if (d < 2) { combine(epc + 1u); asm volatile("" : "+v"(v0), "+v"(v1), "+v"(bad)); } }
      if constexpr (st == 14) {
        const unsigned tag = (d < 2 ? epc : epr) + 1u;
        bool ok = bad == 0u;
        if (__builtin_expect(!__all(ok), 0)) {
#pragma unroll 1
          for (int it = 0; it < 20000; ++it) {
            asm volatile("" ::: "memory");
            gather(0, 8);
            combine(tag);
            ok = bad == 0u;
            if (__all(ok)) break;
            __builtin_amdgcn_s_sleep(2);
          }
        }
        if (__builtin_expect(!__all(ok), 0)) {
          v0 = 0.f; v1 = 0.f;
          if (d < 2) {
            const u16* wc = w + ni * 128 + t;
#pragma unroll 1
            for (int k = 0; k < KD; ++k) {
              const float wv = bf_lo(wc[(size_t)k * ND]);
              v0 += bf_lo(cvt_pk(bf_lo(g[k]) * wv, 0.f)); v1 = fmaf(bf_lo(b[k]), wv, v1);
            }
          } else {
            const u16* xrow = x + (size_t)(mi * 128 + (t - 128)) * KD;
#pragma unroll 1
            for (int k = 0; k < KD; ++k) v0 += bf_lo(xrow[k]);
            v0 *= (1.f / 1024.f);
#pragma unroll 1
            for (int k = 0; k < KD; ++k) { const float dd = bf_lo(xrow[k]) - v0; v1 = fmaf(dd, dd, v1); }
            v1 = __builtin_amdgcn_rsqf(v1 * (1.f / 1024.f) + 1e-5f);
          }
        }
        if (d < 2) { *(LAS float*)(lds0 + LDS_TAB + 1024 + t * 4) = v0; *(LAS float*)(lds0 + LDS_TAB + 1536 + t * 4) = v1; }
        else *(LAS f32x2*)(lds0 + LDS_TAB + (t - 128) * 8) = (f32x2){v0, v1};
      }
      if constexpr (st <= 3 || st == 14) BARRIER_L(); else BARRIER_N();
    });
    if (t == 0) {
      __hip_atomic_fetch_add((GAS unsigned*)(ws + WS_EPR + mi * 128), 1u, __ATOMIC_RELAXED, __HIP_MEMORY_SCOPE_AGENT);
      __hip_atomic_fetch_add((GAS unsigned*)(ws + WS_EPC + ni * 128), 1u, __ATOMIC_RELAXED, __HIP_MEMORY_SCOPE_AGENT);
    }
  } else {
    const int wn = wave;
    {
      const u32x2 g4 = *(const GAS u32x2*)((const char*)g + ((tid * 8 + rot * 128) & 2047));
      const unsigned a = lds0 + LDS_GT + (unsigned)(((tid >> 1) & 3) * 4 * GROW + (tid >> 3) * 4 + (tid & 1) * 2);
      *(LAS u16*)(a) = (u16)g4[0]; *(LAS u16*)(a + GROW) = (u16)(g4[0] >> 16);
      *(LAS u16*)(a + 2 * GROW) = (u16)g4[1]; *(LAS u16*)(a + 3 * GROW) = (u16)(g4[1] >> 16);
    }
    const int r = lane & 15, q4 = lane >> 4, sw = (r >> 1) & 7;
    unsigned offx[2], offw[4];
#pragma unroll
    for (int ks = 0; ks < 2; ++ks) offx[ks] = lds0 + (unsigned)r * 128u + (unsigned)(((4 * ks + q4) ^ sw) * 16);
    {
      const int q = (lane & 15) >> 2, p = lane & 3;
#pragma unroll
      for (int i = 0; i < 4; ++i) {
        const int tt = i & 1, c = i >> 1, row = 8 * q4 + 4 * tt + q, ch = 4 * wn + 2 * c + (p >> 1);
        offw[i] = lds0 + 16384u + (unsigned)(256 * row + 16 * (ch ^ ((q << 2) | ((2 * q4 + tt) & 3))) + 8 * (p & 1));
      }
    }
    unsigned offg = lds0 + LDS_GT + (unsigned)((q4 * 4 + (lane & 3)) * GROW);
    const unsigned li = lane & 3;
    unsigned sel[4] = {li == 0 ? 0x0c0c0100u : (li == 1 ? 0x01000c0cu : 0x0c0c0c0cu), li == 2 ? 0x0c0c0100u : (li == 3 ? 0x01000c0cu : 0x0c0c0c0cu),
                       li == 0 ? 0x0c0c0302u : (li == 1 ? 0x03020c0cu : 0x0c0c0c0cu), li == 2 ? 0x0c0c0302u : (li == 3 ? 0x03020c0cu : 0x0c0c0c0cu)};
    f32x4 acc[8][2] = {};
    bf16x8 xf[8];
    s16x4 wr[4];
    u32x2 gq;
    u32x4 dg;
    u32x4 wa[2], wb[2];
    f32x4 dd[4];
    const f32x4 z4 = {0.f, 0.f, 0.f, 0.f};
#define RX(mt, ks) xf[mt] = *(const LAS bf16x8*)(offx[ks] + (mt) * 2048)
#define RW(i, ks) wr[i] = __builtin_amdgcn_ds_read_tr16_b64_v4i16((LAS s16x4*)(offw[i] + (ks) * 8192))
#define RG(o) gq = *(const LAS u32x2*)(offg + (o))
#define DG(i, G) dg[i] = __builtin_amdgcn_perm(G, G, sel[i])
#define SM(i) dd[i] = __builtin_amdgcn_mfma_f32_4x4x4bf16_1k(__builtin_bit_cast(s16x4, (u32x2){dg[((i) & 1) * 2], dg[((i) & 1) * 2 + 1]}), wr[i], z4, 0, 0, 0)
#define CV(W, e) W[(e) >> 2][(e) & 3] = cvt_pk(dd[(e) >> 1][((e) & 1) * 2], dd[(e) >> 1][((e) & 1) * 2 + 1])
#define MM(mt, c, W) acc[mt][c] = __builtin_amdgcn_mfma_f32_16x16x32_bf16(__builtin_bit_cast(bf16x8, W[c]), xf[mt], acc[mt][c], 0, 0, 0)
#define G2(mt, W, A, B) do { MM(mt, 0, W); SCHED(); A; SCHED(); MM(mt, 1, W); SCHED(); B; SCHED(); } while (0)
#define STEP(WC, WN, kx, kw, G, MID, E0, E1, E2) do { \
      G2(0, WC, (void)0, (RX(0, kx))); \
      SM(0); SM(1); SM(2); SM(3); SCHED(); \
      G2(1, WC, (DG(0, G), DG(1, G)), (RX(1, kx), DG(2, G))); \
      G2(2, WC, (CV(WN, 0), DG(3, G)), (RX(2, kx), CV(WN, 1))); \
      G2(3, WC, (CV(WN, 2), E0), (RX(3, kx), CV(WN, 3))); \
      G2(4, WC, (CV(WN, 4), E1), (RX(4, kx), CV(WN, 5))); \
      G2(5, WC, (CV(WN, 6), E2), (RX(5, kx), CV(WN, 7))); \
      MID; \
      G2(6, WC, (RW(0, kw), RW(1, kw)), (RX(6, kx))); \
      G2(7, WC, (RW(2, kw), RW(3, kw)), (RX(7, kx))); } while (0)
#pragma unroll
    for (int i = 0; i < 8; ++i) asm volatile("" : "+v"(acc[i][0]), "+v"(acc[i][1]));
    asm volatile("" : "+v"(offx[0]), "+v"(offx[1]), "+v"(offw[0]), "+v"(offw[1]), "+v"(offw[2]), "+v"(offw[3]), "+v"(offg),
                      "+v"(sel[0]), "+v"(sel[1]), "+v"(sel[2]), "+v"(sel[3]));
    __builtin_amdgcn_s_waitcnt(0xC07F);
    BARRIER_N();
    RG(0); RW(0, 0); RW(1, 0); RW(2, 0); RW(3, 0);
    RX(0, 0); RX(1, 0); RX(2, 0); RX(3, 0); RX(4, 0); RX(5, 0); RX(6, 0); RX(7, 0);
    DG(0, gq[0]); DG(1, gq[0]); DG(2, gq[0]); DG(3, gq[0]); SCHED();
    SM(0); SM(1); SM(2); SM(3); SCHED();
    RW(0, 1); RW(1, 1); RW(2, 1); RW(3, 1);
    DG(0, gq[1]); DG(1, gq[1]); DG(2, gq[1]); DG(3, gq[1]); SCHED();
    RG(8);
    CV(wa, 0); CV(wa, 1); CV(wa, 2); CV(wa, 3); CV(wa, 4); CV(wa, 5); CV(wa, 6); CV(wa, 7); SCHED();
#pragma unroll 1
    for (int s = 0; s < NST - 1; ++s) {
      const int dlt = ((s & (RING - 1)) == RING - 1) ? -(RING - 1) * SLOT : SLOT;
      STEP(wa, wb, 1, 0, gq[0], BARRIER_N()  , (offw[0] += dlt, offw[1] += dlt), (offw[2] += dlt, offw[3] += dlt), (offx[0] += dlt));
      STEP(wb, wa, 0, 1, gq[1], (void)0, (RG(16), offx[1] += dlt), (offg += 8u), (void)0);
    }
    f32x4 c1v[2], c2v[2];
    f32x2 rs[8];
    const unsigned tc = lds0 + LDS_TAB + 1024 + (unsigned)(wn * 32 + q4 * 4) * 4u, tr = lds0 + LDS_TAB + (unsigned)r * 8u;
#define RS(i) rs[i] = *(const LAS f32x2*)(tr + (i) * 128)
    G2(0, wa, (void)0, (RX(0, 1)));
    SM(0); SM(1); SM(2); SM(3); SCHED();
    G2(1, wa, (void)0, (RX(1, 1)));
    G2(2, wa, (CV(wb, 0)), (RX(2, 1), CV(wb, 1)));
    G2(3, wa, (CV(wb, 2)), (RX(3, 1), CV(wb, 3)));
    G2(4, wa, (CV(wb, 4)), (RX(4, 1), CV(wb, 5)));
    G2(5, wa, (CV(wb, 6)), (RX(5, 1), CV(wb, 7)));
    G2(6, wa, (void)0, (RX(6, 1)));
    G2(7, wa, (void)0, (RX(7, 1)));
    G2(0, wb, (c1v[0] = *(const LAS f32x4*)(tc)), (c1v[1] = *(const LAS f32x4*)(tc + 64)));
    G2(1, wb, (c2v[0] = *(const LAS f32x4*)(tc + 512)), (c2v[1] = *(const LAS f32x4*)(tc + 512 + 64)));
    G2(2, wb, (RS(0)), (RS(1))); G2(3, wb, (RS(2)), (RS(3))); G2(4, wb, (RS(4)), (RS(5))); G2(5, wb, (RS(6)), (RS(7)));
    G2(6, wb, (void)0, (void)0); G2(7, wb, (void)0, (void)0);
    const unsigned so = lds0 + (unsigned)r * 256u + (unsigned)(q4 & 1) * 8u;
    const unsigned sc0 = (unsigned)(((wn * 4 + (q4 >> 1)) ^ (r & 7)) * 16), sc1 = sc0 ^ 32u;
#pragma unroll
    for (int m2 = 0; m2 < 4; ++m2) {
      f32x4 tq[4];
#pragma unroll
      for (int i = 0; i < 4; ++i) tq[i] = acc[2 * m2 + (i >> 1)][i & 1] - c1v[i & 1] * rs[2 * m2 + (i >> 1)][0];
#pragma unroll
      for (int i = 0; i < 4; ++i) tq[i] = tq[i] * rs[2 * m2 + (i >> 1)][1] + c2v[i & 1];
#pragma unroll
      for (int i = 0; i < 4; ++i)
        *(LAS u32x2*)(so + (2 * m2 + (i >> 1)) * 4096 + ((i & 1) ? sc1 : sc0)) = (u32x2){cvt_pk(tq[i][0], tq[i][1]), cvt_pk(tq[i][2], tq[i][3])};
      SCHED();
      if (m2 == 1) BARRIER_L();
    }
  }
  if (wave >= 4) {
    BARRIER_N();
    store_rows<2>(lds0, (wave - 4) * 8, lane, (char*)out + (size_t)mi * 262144 + ni * 256);
  }
  BARRIER_L();
  if (wave < 8) store_rows<2>(lds0, 64 + wave * 8, lane, (char*)out + (size_t)mi * 262144 + ni * 256);
}

extern "C" void kernel_launch(void* const* d_in, const int* in_sizes, int n_in, void* d_out, int out_size, void* d_ws, size_t ws_size,
                              hipStream_t stream) {
  const u16* x = (const u16*)d_in[0]; const u16* g = (const u16*)d_in[1]; const u16* b = (const u16*)d_in[2]; const u16* w = (const u16*)d_in[3];
  (void)hipFuncSetAttribute((const void*)lnlin_kernel, hipFuncAttributeMaxDynamicSharedMemorySize, LDS_TOTAL);
  lnlin_kernel<<<256, 768, LDS_TOTAL, stream>>>(x, g, b, w, (u16*)d_out, (char*)d_ws);
}
```
